```python
import torch

def _softmax(x, axis=-1):
    return torch.softmax(x, dim=axis)

def _gelu(x):
    return torch.nn.functional.gelu(x, approximate='tanh')

def _silu(x):
    return torch.nn.functional.silu(x)

def _sigmoid(x):
    return torch.sigmoid(x)

def _layernorm(x, axis=-1, eps=1e-05):
    m = x.mean(dim=axis, keepdim=True)
    v = x.var(dim=axis, keepdim=True, unbiased=False)
    return (x - m) / torch.sqrt(v + eps)

def _scatter_add(tgt, idx, src, axis=0):
    return tgt.clone().index_add_(axis, idx, src)

def _segment_sum(src, seg, n):
    out = torch.zeros((n,) + src.shape[1:], dtype=src.dtype)
    return out.index_add_(0, seg, src)

def _pad(x, pw):
    flat = tuple((v for pair in reversed(pw) for v in pair))
    return torch.nn.functional.pad(x, flat)
kernel_name = 'recurrent_scan__softmax_full_mha__mixer_gmlp__prompt'

def setup_inputs():

    _w = lambda a: (lambda t: t.to(torch.bfloat16) if t.is_floating_point() else t)(torch.as_tensor(a))

    def _A_setup():
        B, S, D = (1, 2048, 512)
        return {'x': _w((lambda *s: torch.randn(*s, dtype=torch.bfloat16) * 0.05)(B, S, D)), 'wqkv': _w((lambda *s: torch.randn(*s, dtype=torch.bfloat16) * 0.05)(D, 3 * D)), 'wo': _w((lambda *s: torch.randn(*s, dtype=torch.bfloat16) * 0.05)(D, D))}
    _A_in = _A_setup()

    def _B_setup():
        B, S, D, F = (1, 2048, 512, 1792)
        Fh = F // 2
        return {'x': _w((lambda *s: torch.randn(*s, dtype=torch.bfloat16) * 0.05)(B, S, D)), 'w_in': _w((lambda *s: torch.randn(*s, dtype=torch.bfloat16) * 0.8)(D, 2 * Fh)), 'w_sp': _w(0.01 * (lambda *s: torch.randn(*s, dtype=torch.bfloat16) * 0.8)(S, S)), 'b_sp': _w(1.0 + 0.01 * (lambda *s: torch.randn(*s, dtype=torch.bfloat16) * 0.05)(S)), 'w_out': _w((lambda *s: torch.randn(*s, dtype=torch.bfloat16) * 0.05)(Fh, D))}
    _B_in = _B_setup()
    return {'A_x': _A_in['x'], 'A_wqkv': _A_in['wqkv'], 'A_wo': _A_in['wo'], 'B_x': _B_in['x'], 'B_w_in': _B_in['w_in'], 'B_w_sp': _B_in['w_sp'], 'B_b_sp': _B_in['b_sp'], 'B_w_out': _B_in['w_out']}

def reference(A_x, A_wqkv, A_wo, B_x, B_w_in, B_w_sp, B_b_sp, B_w_out):

    def _A_ref(x, wqkv, wo):
        B, S, D = x.shape
        H = 32
        Dh = D // H
        qkv = (lambda x, *d: x.permute(*d))((x @ wqkv).reshape(B, S, 3, H, Dh), 2, 0, 3, 1, 4)
        q, k, v = (qkv[0], qkv[1], qkv[2])
        scale = Dh ** (-0.5)
        scores = torch.einsum('bhqd,bhkd->bhqk', q, k) * scale
        p = _softmax(scores, axis=-1)
        o = torch.einsum('bhqk,bhkd->bhqd', p, v)
        return (lambda x, *d: x.permute(*d))(o, 0, 2, 1, 3).reshape(B, S, D) @ wo

    def _B_ref(x, w_in, w_sp, b_sp, w_out):
        B, S, D = x.shape
        F = w_in.shape[-1]
        Fh = F // 2
        z = _gelu(torch.einsum('bsd,df->bsf', x, w_in))
        z1 = z[..., :Fh].clone()
        z2 = _layernorm(z[..., Fh:], axis=-1)
        g = torch.einsum('ts,bsf->btf', w_sp, z2) + b_sp[None, :, None]
        return torch.einsum('bsf,fd->bsd', z1 * g, w_out)
    x = A_x
    xn = _layernorm(x, axis=-1)
    s = _A_ref(xn, A_wqkv, A_wo)
    g = _silu(_B_ref(xn, B_w_in, B_w_sp, B_b_sp, B_w_out))
    return x + s * g
```

```cpp
#include <hip/hip_runtime.h>
#include <hip/hip_bf16.h>
#include <stdint.h>

typedef unsigned short u16;
using bf16x8 = __attribute__((ext_vector_type(8))) short;
using s16x4 = __attribute__((ext_vector_type(4))) short;
using f32x16 = __attribute__((ext_vector_type(16))) float;
using f32x4 = __attribute__((ext_vector_type(4))) float;
using u32x4 = __attribute__((ext_vector_type(4))) unsigned;
using u32x2 = __attribute__((ext_vector_type(2))) unsigned;
typedef __bf16 bf16x2_t __attribute__((ext_vector_type(2)));
typedef float f32x2_t __attribute__((ext_vector_type(2)));
typedef short v4i16_t __attribute__((ext_vector_type(4)));
#define LDSAS __attribute__((address_space(3)))
typedef LDSAS const char* lds_cptr;
#define MFMA32(a, b, c) __builtin_amdgcn_mfma_f32_32x32x16_bf16(a, b, c, 0, 0, 0)
#define MFMA16(a, b, c) __builtin_amdgcn_mfma_f32_16x16x32_bf16(a, b, c, 0, 0, 0)
#define SBAR() __builtin_amdgcn_sched_barrier(0)
#define PIN(x) asm volatile("" : "+v"(x))
#define WAIT_BAR(N) asm volatile("s_waitcnt vmcnt(" #N ") lgkmcnt(0)\n\ts_barrier" ::: "memory")
#define DI __device__ __forceinline__

constexpr int S = 2048, D = 512, H = 32, DH = 16, FH = 896;
constexpr float LOG2E = 1.4426950408889634f;

constexpr size_t WS_Q = 0, WS_K = 2u << 20, WS_V = 4u << 20, WS_Z1 = 6u << 20, WS_Z2R = 10u << 20, WS_Z2N = 14u << 20, WS_H = 18u << 20, WS_O = 22u << 20, WS_KMAX = 24u << 20;

DI unsigned cvtpk(float lo, float hi) { f32x2_t v = {lo, hi}; return __builtin_bit_cast(unsigned, __builtin_convertvector(v, bf16x2_t)); }
DI float bflo(unsigned w) { return __uint_as_float(w << 16); }
DI float bfhi(unsigned w) { return __uint_as_float(w & 0xFFFF0000u); }
DI float dot2(unsigned a, unsigned b, float c) { return __builtin_amdgcn_fdot2_f32_bf16(__builtin_bit_cast(bf16x2_t, a), __builtin_bit_cast(bf16x2_t, b), c, false); }
DI void glds16s(unsigned voff, const void* sbase, unsigned lds_base) {
    unsigned sv; asm volatile("s_mov_b32 %0, m0\n\ts_mov_b32 m0, %3\n\ts_nop 0\n\tglobal_load_lds_dwordx4 %1, %2\n\ts_mov_b32 m0, %0" : "=&s"(sv) : "v"(voff), "s"(sbase), "s"(lds_base) : "memory"); }
DI bf16x8 ldsr128(lds_cptr p) { return *(const LDSAS bf16x8*)p; }
DI s16x4 ldstr(lds_cptr p) { return __builtin_bit_cast(s16x4, __builtin_amdgcn_ds_read_tr16_b64_v4i16((LDSAS v4i16_t*)p)); }
DI bf16x8 cat(s16x4 a, s16x4 b) { return __builtin_shufflevector(a, b, 0, 1, 2, 3, 4, 5, 6, 7); }
DI float swap32(float x, bool sum) { auto rr = __builtin_amdgcn_permlane32_swap(__float_as_uint(x), __float_as_uint(x), false, false); return sum ? __uint_as_float(rr[0]) + __uint_as_float(rr[1]) : __uint_as_float(rr[0]); }
DI float gelu_t(float x) {
    const float c1 = -2.0f * 0.7978845608028654f * LOG2E, c3 = c1 * 0.044715f;
    const float u = x * fmaf(x * x, c3, c1);
    return x * __builtin_amdgcn_rcpf(1.0f + __builtin_amdgcn_exp2f(u));
}

constexpr int K1_KSB = 13312;
constexpr int K1_STAGE = 4 * K1_KSB;
constexpr int K1_CS = 3 * K1_STAGE;
constexpr int K1_LDS = K1_CS + 3328 + 512;

__global__ __launch_bounds__(256, 1) void k1_proj(const u16* __restrict__ x, const u16* __restrict__ wqkv, const u16* __restrict__ win, char* __restrict__ ws) {
    extern __shared__ __attribute__((aligned(16))) char lds[];
    const int tid = threadIdx.x, lane = tid & 63, r16 = lane & 15, g = lane >> 4;
    const int wid = __builtin_amdgcn_readfirstlane(tid >> 6);
    const int b = blockIdx.x, xcd = b & 7, idx = b >> 3;
    const int panel = 4 * (xcd >> 1) + (idx & 3), j = 8 * (xcd & 1) + (idx >> 2);
    const unsigned lds0 = (unsigned)(uintptr_t)lds;
    const int pi = j >> 1, odd = j & 1;
    const int n1 = odd ? 3 : 4;
    const int z1f0 = 7 * pi + (odd ? 4 : 0), z2f0 = 7 * pi + (odd ? 3 : 0);
    unsigned voq[2], voz[7];
#pragma unroll
    for (int e = 0; e < 2; ++e) { const int rho = 16 * e + (lane >> 2); const int k = 8 * ((rho >> 2) & 3) + 4 * (rho >> 4) + (rho & 3);
        voq[e] = (unsigned)(k * 3072 + (((lane & 3) ^ (((rho >> 2) & 1) << 1)) * 16)); }
#pragma unroll
    for (int e = 0; e < 7; ++e) { const int i = 64 * e + lane; const int rho = i / 14, c = i - rho * 14; const int k = 8 * ((rho >> 2) & 3) + 4 * (rho >> 4) + (rho & 3);
        const int colb = (c < 2 * n1) ? (z1f0 * 32 + c * 16) : (FH * 2 + z2f0 * 32 + (c - 2 * n1) * 16);
        voz[e] = (unsigned)(k * 3584 + colb); }
    const char* wq = (const char*)wqkv; const char* wi = (const char*)win;
#define K1_DMA(ks, slotbase) do { const char* bq_ = wq + (size_t)(ks) * 32 * 3072; const char* bz_ = wi + (size_t)(ks) * 32 * 3584; const unsigned d_ = lds0 + (slotbase); \
        glds16s(voq[0], bq_ + 2048 + j * 64, d_); glds16s(voq[1], bq_ + 2048 + j * 64, d_ + 1024); \
        glds16s(voq[0], bq_ + j * 64, d_ + 2048); glds16s(voq[1], bq_ + j * 64, d_ + 3072); \
        glds16s(voq[0], bq_ + 1024 + j * 64, d_ + 4096); glds16s(voq[1], bq_ + 1024 + j * 64, d_ + 5120); \
        _Pragma("unroll") for (int e_ = 0; e_ < 7; ++e_) glds16s(voz[e_], bz_, d_ + 6144 + e_ * 1024); } while (0)
    u32x4 xf[2][16];
    const unsigned xvo = (unsigned)((wid * 32 + r16) * 1024 + g * 16);
    const char* xb = (const char*)x + (size_t)panel * 128 * 1024;
#define XLD(m, ks) asm volatile("global_load_dwordx4 %0, %1, %2 offset:%3" : "=v"(xf[m][ks]) : "v"(xvo + (m) * 16384), "s"(xb), "i"((ks) * 64) : "memory")
#define XLD_STAGE(s) do { XLD(0, 4*(s)); XLD(1, 4*(s)); XLD(0, 4*(s)+1); XLD(1, 4*(s)+1); XLD(0, 4*(s)+2); XLD(1, 4*(s)+2); XLD(0, 4*(s)+3); XLD(1, 4*(s)+3); } while (0)
#define XWAIT(s, N) asm volatile("s_waitcnt vmcnt(" #N ") lgkmcnt(0)\n\ts_barrier" : "+v"(xf[0][4*(s)]), "+v"(xf[1][4*(s)]), "+v"(xf[0][4*(s)+1]), "+v"(xf[1][4*(s)+1]), "+v"(xf[0][4*(s)+2]), "+v"(xf[1][4*(s)+2]), "+v"(xf[0][4*(s)+3]), "+v"(xf[1][4*(s)+3]) :: "memory")
    K1_DMA(0 + wid, 0 * K1_STAGE + wid * K1_KSB);
    XLD_STAGE(0);
    K1_DMA(4 + wid, 1 * K1_STAGE + wid * K1_KSB);
    XLD_STAGE(1);
    K1_DMA(8 + wid, 2 * K1_STAGE + wid * K1_KSB);
    XLD_STAGE(2);
    XLD_STAGE(3);

    f32x4 acc[2][13];
#pragma unroll
    for (int m = 0; m < 2; ++m)
#pragma unroll
        for (int s = 0; s < 13; ++s) acc[m][s] = (f32x4){0.f, 0.f, 0.f, 0.f};
    float cs[13];
#pragma unroll
    for (int s = 0; s < 13; ++s) cs[s] = 0.f;
    float sx[2] = {0.f, 0.f}, sxx[2] = {0.f, 0.f};
    const int qq = r16 >> 2, pp = lane & 3;
    const int rr = 4 * g + qq;
    const lds_cptr wqb0 = (lds_cptr)lds + rr * 64 + ((0 ^ (g & 1)) * 32) + pp * 8;
    const lds_cptr wqb1 = (lds_cptr)lds + rr * 64 + ((1 ^ (g & 1)) * 32) + pp * 8;
    const lds_cptr wzb = (lds_cptr)lds + 6144 + rr * 224 + pp * 8;
    auto kstep = [&](const int ks, const int base) __attribute__((always_inline)) {
        bf16x8 wf[13];
#pragma unroll
        for (int s = 0; s < 6; ++s) { const lds_cptr p = ((s & 1) ? wqb1 : wqb0) + base + (s >> 1) * 2048; wf[s] = cat(ldstr(p), ldstr(p + 1024)); }
#pragma unroll
        for (int s = 0; s < 7; ++s) { const lds_cptr p = wzb + base + s * 32; wf[6 + s] = cat(ldstr(p), ldstr(p + 16 * 224)); }
        const bf16x8 x0 = __builtin_bit_cast(bf16x8, xf[0][ks]), x1 = __builtin_bit_cast(bf16x8, xf[1][ks]);
#pragma unroll
        for (int s = 0; s < 2; ++s) { acc[0][s] = MFMA16(x0, wf[s], acc[0][s]); acc[1][s] = MFMA16(x1, wf[s], acc[1][s]); }
#pragma unroll
        for (int s = 2; s < 13; ++s) { acc[0][s] = MFMA16(wf[s], x0, acc[0][s]); acc[1][s] = MFMA16(wf[s], x1, acc[1][s]); }
#pragma unroll
        for (int m = 0; m < 2; ++m)
#pragma unroll
            for (int i = 0; i < 4; ++i) { sx[m] = dot2(xf[m][ks][i], 0x3F803F80u, sx[m]); sxx[m] = dot2(xf[m][ks][i], xf[m][ks][i], sxx[m]); }
        if ((ks & 3) == wid) {
#pragma unroll
            for (int s = 0; s < 13; ++s) { const u32x4 w = __builtin_bit_cast(u32x4, wf[s]);
#pragma unroll
                for (int i = 0; i < 4; ++i) cs[s] = dot2(w[i], 0x3F803F80u, cs[s]); }
        }
    };
    XWAIT(0, 50);
#pragma unroll
    for (int i = 0; i < 4; ++i) kstep(i, 0 * K1_STAGE + i * K1_KSB);
    XWAIT(1, 29);
    K1_DMA(12 + wid, 0 * K1_STAGE + wid * K1_KSB);
#pragma unroll
    for (int i = 0; i < 4; ++i) kstep(4 + i, 1 * K1_STAGE + i * K1_KSB);
    XWAIT(2, 21);
#pragma unroll
    for (int i = 0; i < 4; ++i) kstep(8 + i, 2 * K1_STAGE + i * K1_KSB);
    XWAIT(3, 0);
#pragma unroll
    for (int i = 0; i < 4; ++i) kstep(12 + i, 0 * K1_STAGE + i * K1_KSB);

    float* csl = (float*)(lds + K1_CS);
#pragma unroll
    for (int s = 0; s < 13; ++s) { float v = cs[s]; v += __shfl_xor(v, 16); v += __shfl_xor(v, 32); if (g == 0) csl[(wid * 13 + s) * 16 + r16] = v; }
    float mu[2], rs[2];
#pragma unroll
    for (int m = 0; m < 2; ++m) { float a = sx[m], q = sxx[m]; a += __shfl_xor(a, 16); a += __shfl_xor(a, 32); q += __shfl_xor(q, 16); q += __shfl_xor(q, 32);
        mu[m] = a * (1.0f / 512.0f); const float var = fmaxf(q * (1.0f / 512.0f) - mu[m] * mu[m], 0.f); rs[m] = 1.0f / __builtin_sqrtf(var + 1e-5f); }
    __syncthreads();
    const int tok0 = panel * 128 + wid * 32;
    u16* Qs = (u16*)(ws + WS_Q); u16* Kb = (u16*)(ws + WS_K); u16* Vt = (u16*)(ws + WS_V); u16* Z1 = (u16*)(ws + WS_Z1); u16* Z2R = (u16*)(ws + WS_Z2R); float* kmax = (float*)(ws + WS_KMAX);
    auto cs4 = [&](int s) __attribute__((always_inline)) { f32x4 v = *(const f32x4*)(csl + s * 16 + 4 * g);
#pragma unroll
        for (int w = 1; w < 4; ++w) v += *(const f32x4*)(csl + (w * 13 + s) * 16 + 4 * g); return v; };
#pragma unroll
    for (int s = 0; s < 2; ++s) {
        float c = csl[s * 16 + r16];
#pragma unroll
        for (int w = 1; w < 4; ++w) c += csl[(w * 13 + s) * 16 + r16];
        const int head = 2 * j + s;
#pragma unroll
        for (int m = 0; m < 2; ++m) {
            float v[4];
#pragma unroll
            for (int r = 0; r < 4; ++r) { const float mm = __shfl(mu[m], 4 * g + r), rv = __shfl(rs[m], 4 * g + r); v[r] = rv * (acc[m][s][r] - mm * c); }
            const int kst = (tok0 + 16 * m) >> 4;
            u32x2 w = {cvtpk(v[0], v[1]), cvtpk(v[2], v[3])};
            *(u32x2*)(Vt + ((size_t)head * 128 + kst) * 256 + (g & 1) * 128 + r16 * 8 + (g >> 1) * 4) = w;
        }
    }
#pragma unroll
    for (int s = 2; s < 6; ++s) {
        const f32x4 c = cs4(s); const int head = 2 * j + (s & 1); const bool isq = s < 4;
        float kn = 0.f;
#pragma unroll
        for (int m = 0; m < 2; ++m) {
            const float sc = isq ? rs[m] * (0.25f * LOG2E) : rs[m];
            float v[4];
#pragma unroll
            for (int r = 0; r < 4; ++r) v[r] = sc * (acc[m][s][r] - mu[m] * c[r]);
            u32x2 w = {cvtpk(v[0], v[1]), cvtpk(v[2], v[3])};
            *(u32x2*)((isq ? Qs : Kb) + ((size_t)head * S + tok0 + 16 * m + r16) * 16 + 4 * g) = w;
            if (!isq) { float n = bflo(w[0]) * bflo(w[0]); n = fmaf(bfhi(w[0]), bfhi(w[0]), n); n = fmaf(bflo(w[1]), bflo(w[1]), n); n = fmaf(bfhi(w[1]), bfhi(w[1]), n);
                n += __shfl_xor(n, 16); n += __shfl_xor(n, 32); kn = fmaxf(kn, n); }
        }
        if (!isq) {
#pragma unroll
            for (int o = 1; o < 16; o <<= 1) kn = fmaxf(kn, __shfl_xor(kn, o));
            if (lane == 0) kmax[(panel * 4 + wid) * H + head] = kn;
        }
    }
#pragma unroll
    for (int s = 6; s < 13; ++s) {
        const f32x4 c = cs4(s); const int zi = s - 6; const bool isz1 = zi < n1;
#pragma unroll
        for (int m = 0; m < 2; ++m) {
            float v[4];
#pragma unroll
            for (int r = 0; r < 4; ++r) v[r] = gelu_t(rs[m] * (acc[m][s][r] - mu[m] * c[r]));
            u32x2 w = {cvtpk(v[0], v[1]), cvtpk(v[2], v[3])};
            const int tok = tok0 + 16 * m + r16;
            if (isz1) { const int f = z1f0 + zi;
                const int q = 2 * (f & 1) + (g >> 1), hi = g & 1;
                *(u32x2*)(Z1 + ((size_t)((tok >> 5) * 28 + (f >> 1)) * 1024) + ((q * 2 + hi) * 32 + (tok & 31)) * 4) = w;
            } else { const int f = z2f0 + (zi - n1);
                *(u32x2*)(Z2R + (size_t)tok * FH + f * 16 + 4 * g) = w; }
        }
    }
}

__global__ __launch_bounds__(256) void k1b_norm(char* __restrict__ ws) {
    const int lane = threadIdx.x & 63, row = blockIdx.x * 4 + (threadIdx.x >> 6);
    const u16* src = (const u16*)(ws + WS_Z2R) + (size_t)row * FH; u16* dst = (u16*)(ws + WS_Z2N);
    float v[14]; float s = 0.f;
#pragma unroll
    for (int i = 0; i < 7; ++i) { const unsigned w = *(const unsigned*)(src + i * 128 + lane * 2); v[2 * i] = bflo(w); v[2 * i + 1] = bfhi(w); s += v[2 * i] + v[2 * i + 1]; }
#pragma unroll
    for (int o = 1; o < 64; o <<= 1) s += __shfl_xor(s, o);
    const float mu = s * (1.0f / FH); float q = 0.f;
#pragma unroll
    for (int i = 0; i < 14; ++i) { const float d = v[i] - mu; q = fmaf(d, d, q); }
#pragma unroll
    for (int o = 1; o < 64; o <<= 1) q += __shfl_xor(q, o);
    const float r = 1.0f / __builtin_sqrtf(q * (1.0f / FH) + 1e-5f);
#pragma unroll
    for (int i = 0; i < 7; ++i) { const int f = lane * 2;
        *(unsigned*)(dst + ((size_t)((i * 32 + (row >> 6)) * 4 + (f >> 5)) * 64 + (row & 63)) * 32 + (f & 31)) = cvtpk((v[2 * i] - mu) * r, (v[2 * i + 1] - mu) * r); }
}

constexpr int A_KR = 0, A_VR = 32768, A_CONST = 65536, K2_LDS = 81920;
constexpr float SAFE_T = 40.f;

DI void attn_role(int b, char* __restrict__ ws, char* lds) {
    const u16* Qs = (const u16*)(ws + WS_Q); const u16* Kb = (const u16*)(ws + WS_K); const u16* Vt = (const u16*)(ws + WS_V); const float* kmax = (const float*)(ws + WS_KMAX); u16* Ot = (u16*)(ws + WS_O);
    const int tid = threadIdx.x, lane = tid & 63, r32 = lane & 31, hi = lane >> 5;
    const int wid = __builtin_amdgcn_readfirstlane(tid >> 6);
    const int xcd = b & 7, idx = b >> 3, head = xcd * 4 + (idx & 3), qt = idx >> 2;
    const int qp = wid & 3, kh = wid >> 2;
    const unsigned lds0 = (unsigned)(uintptr_t)lds;
    const char* kg = (const char*)(Kb + (size_t)head * S * DH);
    const char* vg = (const char*)(Vt + (size_t)head * S * DH);
    const unsigned kvoff = wid * 1024 + ((lane ^ ((lane >> 5) & 1)) * 16);
    const unsigned vvoff = wid * 1024 + lane * 16;
#define DMA_CHUNK(c) do { glds16s(kvoff, kg + (c) * 8192, lds0 + A_KR + ((c) & 3) * 8192 + wid * 1024); glds16s(vvoff, vg + (c) * 8192, lds0 + A_VR + ((c) & 3) * 8192 + wid * 1024); } while (0)
    DMA_CHUNK(0); DMA_CHUNK(1);
    const int q0 = qt * 256 + qp * 64;
    const bf16x8 qa = *(const bf16x8*)(Qs + ((size_t)head * S + q0 + r32) * DH + hi * 8);
    const bf16x8 qb = *(const bf16x8*)(Qs + ((size_t)head * S + q0 + 32 + r32) * DH + hi * 8);
    float km = kmax[lane * H + head];
    DMA_CHUNK(2);
    if (tid < 256) { const unsigned w = (tid & 31) ? 0u : 0x3F803F80u; u32x4 v = {w, w, w, w}; *(u32x4*)(lds + A_CONST + tid * 16) = v; }
#pragma unroll
    for (int o = 1; o < 64; o <<= 1) km = fmaxf(km, __shfl_xor(km, o));
    float qna = 0.f, qnb = 0.f;
#pragma unroll
    for (int j = 0; j < 8; ++j) { const float va = bflo((u16)qa[j]), vb = bflo((u16)qb[j]); qna = fmaf(va, va, qna); qnb = fmaf(vb, vb, qnb); }
    qna = swap32(qna, true); qnb = swap32(qnb, true);
    const float mh = __builtin_sqrtf(fmaxf(qna, qnb) * km) * 1.001f;
    f32x16 cinit;
#pragma unroll
    for (int r = 0; r < 16; ++r) cinit[r] = -mh;
    PIN(cinit);
    f32x16 oa = {}, ob = {};
    const lds_cptr kp0 = (lds_cptr)lds + A_KR + kh * 4096 + r32 * 32 + ((hi ^ (r32 >> 4)) * 16);
    const bool vreal = r32 < 16;
    const unsigned vlane = vreal ? (unsigned)(A_VR + kh * 4096 + hi * 256 + r32 * 16) : (unsigned)(A_CONST + (r32 == 16 ? 0 : ((r32 >= 20 && r32 < 28) ? 64 : 16)));
    const unsigned vmask = vreal ? 0xFFFFFFFFu : 0u;
    WAIT_BAR(2);
    bf16x8 kf, v0, v1; f32x16 sA, sB; u32x4 p0, p1;
#define EX(x) __builtin_amdgcn_exp2f(x)
#define BF(p) __builtin_bit_cast(bf16x8, p)
    kf = ldsr128(kp0);
    { lds_cptr vp = (lds_cptr)lds + vlane; v0 = ldsr128(vp); v1 = ldsr128(vp + 512); }
    sA = MFMA32(kf, qa, cinit);
    p1 = (u32x4){0u, 0u, 0u, 0u};
#define HSTEP(SC, SN, QN, OP, OC, PRE1, PRE2, PRE3) do { \
        SN = MFMA32(kf, QN, cinit); PIN(SN); SBAR(); \
        PRE1; \
        SC[0] = EX(SC[0]); SC[1] = EX(SC[1]); SC[2] = EX(SC[2]); SC[3] = EX(SC[3]); SC[4] = EX(SC[4]); SC[5] = EX(SC[5]); PIN(SC); SBAR(); \
        OP = MFMA32(v1, BF(p1), OP); PIN(OP); SBAR(); \
        PRE2; \
        SC[6] = EX(SC[6]); SC[7] = EX(SC[7]); \
        p0[0] = cvtpk(SC[0], SC[1]); p0[1] = cvtpk(SC[2], SC[3]); p0[2] = cvtpk(SC[4], SC[5]); p0[3] = cvtpk(SC[6], SC[7]); PIN(p0); SBAR(); \
        SC[8] = EX(SC[8]); SC[9] = EX(SC[9]); SC[10] = EX(SC[10]); PIN(SC); SBAR(); \
        OC = MFMA32(v0, BF(p0), OC); PIN(OC); SBAR(); \
        PRE3; \
        SC[11] = EX(SC[11]); SC[12] = EX(SC[12]); SC[13] = EX(SC[13]); SC[14] = EX(SC[14]); SC[15] = EX(SC[15]); \
        p1[0] = cvtpk(SC[8], SC[9]); p1[1] = cvtpk(SC[10], SC[11]); p1[2] = cvtpk(SC[12], SC[13]); p1[3] = cvtpk(SC[14], SC[15]); PIN(p1); SBAR(); \
    } while (0)
#define BLOCK(kn, vc, vco, vn, vno) do { \
        HSTEP(sA, sB, qb, ob, oa, kf = ldsr128(kn), v1 = ldsr128((vc) + (vco) + 512), (void)0); \
        HSTEP(sB, sA, qa, oa, ob, (void)0, (void)0, v0 = ldsr128((vn) + (vno))); \
    } while (0)
    for (int c = 0; c < 8; ++c) {
        if (c > 0) { if (c < 6) WAIT_BAR(2); else if (c == 6) WAIT_BAR(0); else asm volatile("s_waitcnt lgkmcnt(0)\n\ts_barrier" ::: "memory"); }
        if (c + 3 < 8 && c > 0) DMA_CHUNK(c + 3);
        if (c == 0) DMA_CHUNK(3);
        const unsigned ro = (unsigned)((c & 3) * 8192), rn = (unsigned)(((c + 1) & 3) * 8192);
        const lds_cptr vp = (lds_cptr)lds + (vlane + (ro & vmask)), vpn = (lds_cptr)lds + (vlane + (rn & vmask));
        const lds_cptr kp = kp0 + ro, kpn = kp0 + rn;
        BLOCK(kp + 1024, vp, 0, vp, 1024);
        BLOCK(kp + 2048, vp, 1024, vp, 2048);
        BLOCK(kp + 3072, vp, 2048, vp, 3072);
        BLOCK(kpn, vp, 3072, vpn, 0);
    }
    ob = MFMA32(v1, BF(p1), ob);
    __syncthreads();
    float* red = (float*)lds;
    { float* dst = red + (size_t)(wid ^ 4) * 9 * 64 + lane;
#pragma unroll
      for (int r = 0; r < 9; ++r) dst[r * 64] = kh ? oa[r] : ob[r]; }
    __syncthreads();
    float o[9];
    { const float* src = red + (size_t)wid * 9 * 64 + lane;
#pragma unroll
      for (int r = 0; r < 9; ++r) o[r] = (kh ? ob[r] : oa[r]) + src[r * 64]; }
    const float rl = 1.0f / swap32(o[8], false);
    u16* op = Ot + ((size_t)(((q0 >> 5) + kh) * H + head) * 512) + r32 * 8 + hi * 4;
    u32x2 w0 = {cvtpk(o[0] * rl, o[1] * rl), cvtpk(o[2] * rl, o[3] * rl)};
    u32x2 w1 = {cvtpk(o[4] * rl, o[5] * rl), cvtpk(o[6] * rl, o[7] * rl)};
    *(u32x2*)op = w0; *(u32x2*)(op + 256) = w1;
#undef DMA_CHUNK
#undef HSTEP
#undef BLOCK
}

constexpr int G_STAGE = 24576;
DI void gate_role(int b, const u16* __restrict__ wsp, const u16* __restrict__ bsp, char* __restrict__ ws, char* lds) {
    const int xcd = b & 7, idx = b >> 3;
    if (idx >= 28) return;
    const int tt = xcd * 4 + (idx & 3), ct = idx >> 2;
    const int tid = threadIdx.x, lane = tid & 63, r32 = lane & 31, hi = lane >> 5;
    const int wid = __builtin_amdgcn_readfirstlane(tid >> 6);
    const int th = wid & 1, fh = (wid >> 1) & 1, kh = wid >> 2;
    const unsigned lds0 = (unsigned)(uintptr_t)lds;
    const char* ag = ws + WS_Z2N + (size_t)ct * 32 * 16384;
    const char* bg = (const char*)wsp + (size_t)tt * 64 * 4096;
    const unsigned avoff = wid * 2048 + lane * 16;
    const int brow = wid * 8 + (lane >> 3);
    const unsigned bvoff = (unsigned)(brow * 4096 + (((lane & 7) ^ ((brow >> 1) & 7)) * 16));
#define G_DMA(kt) do { const unsigned d_ = lds0 + ((kt) % 3) * G_STAGE; glds16s(avoff, ag + (size_t)(kt) * 16384, d_ + wid * 2048); glds16s(avoff + 1024, ag + (size_t)(kt) * 16384, d_ + wid * 2048 + 1024); \
        glds16s(bvoff, bg + (kt) * 128, d_ + 16384 + wid * 1024); } while (0)
    G_DMA(0); G_DMA(1);
    f32x16 acc0 = {}, acc1 = {};
    const int g = lane >> 4, qq = (lane & 15) >> 2, pp = lane & 3;
    const unsigned aoff = (unsigned)(fh * 8192 + kh * 2048 + (8 * (g >> 1) + qq) * 64 + (g & 1) * 32 + pp * 8);
    const int trow = 32 * th + r32;
    const unsigned boff0 = (unsigned)(16384 + trow * 128 + (((4 * kh + hi) ^ ((trow >> 1) & 7)) * 16));
    const unsigned boff1 = (unsigned)(16384 + trow * 128 + (((4 * kh + 2 + hi) ^ ((trow >> 1) & 7)) * 16));
    int slot = 0;
    for (int kt = 0; kt < 32; ++kt) {
        if (kt < 31) WAIT_BAR(3); else WAIT_BAR(0);
        if (kt + 2 < 32) { const unsigned d_ = lds0 + (slot == 0 ? 2 : slot - 1) * G_STAGE; const char* a_ = ag + (size_t)(kt + 2) * 16384;
            glds16s(avoff, a_, d_ + wid * 2048); glds16s(avoff + 1024, a_, d_ + wid * 2048 + 1024); glds16s(bvoff, bg + (kt + 2) * 128, d_ + 16384 + wid * 1024); }
        const lds_cptr sb = (lds_cptr)lds + slot * G_STAGE;
        const bf16x8 b0 = ldsr128(sb + boff0), b1 = ldsr128(sb + boff1);
        const bf16x8 a00 = cat(ldstr(sb + aoff), ldstr(sb + aoff + 256)), a10 = cat(ldstr(sb + aoff + 4096), ldstr(sb + aoff + 4096 + 256));
        const bf16x8 a01 = cat(ldstr(sb + aoff + 1024), ldstr(sb + aoff + 1280)), a11 = cat(ldstr(sb + aoff + 5120), ldstr(sb + aoff + 5120 + 256));
        acc0 = MFMA32(a00, b0, acc0); acc1 = MFMA32(a10, b0, acc1);
        acc0 = MFMA32(a01, b1, acc0); acc1 = MFMA32(a11, b1, acc1);
        slot = slot == 2 ? 0 : slot + 1;
    }
    __syncthreads();
    float* red = (float*)lds;
    { float* dst = red + (size_t)(wid ^ 4) * 1024 + lane;
#pragma unroll
      for (int r = 0; r < 16; ++r) dst[r * 64] = kh ? acc0[r] : acc1[r]; }
    __syncthreads();
    const float* src = red + (size_t)wid * 1024 + lane;
    const int t = tt * 64 + trow, ft = ct * 4 + 2 * fh + kh;
    const float bias = bflo(bsp[t]);
    const u16* z1 = (const u16*)(ws + WS_Z1) + ((size_t)((t >> 5) * 28 + ft) * 1024) + (hi * 32 + r32) * 4;
    u16* hp = (u16*)(ws + WS_H) + ((size_t)((t >> 5) * 56 + ft * 2) * 512) + r32 * 8 + hi * 4;
#pragma unroll
    for (int q = 0; q < 4; ++q) {
        const u32x2 z = *(const u32x2*)(z1 + q * 256);
        float v[4];
#pragma unroll
        for (int r = 0; r < 4; ++r) v[r] = (kh ? acc1[4 * q + r] : acc0[4 * q + r]) + src[(4 * q + r) * 64] + bias;
        u32x2 w = {cvtpk(v[0] * bflo(z[0]), v[1] * bfhi(z[0])), cvtpk(v[2] * bflo(z[1]), v[3] * bfhi(z[1]))};
        *(u32x2*)(hp + (q >> 1) * 512 + (q & 1) * 256) = w;
    }
#undef G_DMA
}

__global__ __launch_bounds__(512, 4) void k2_mix(const u16* __restrict__ wsp, const u16* __restrict__ bsp, char* __restrict__ ws) {
    extern __shared__ __attribute__((aligned(16))) char lds[];
    if (blockIdx.x < 256) attn_role(blockIdx.x, ws, lds);
    else gate_role(blockIdx.x - 256, wsp, bsp, ws, lds);
}

constexpr int K3_STAGE = 32768;
__global__ __launch_bounds__(256, 1) void k3_out(const u16* __restrict__ x, const u16* __restrict__ wo, const u16* __restrict__ wout, const char* __restrict__ ws, u16* __restrict__ out) {
    extern __shared__ __attribute__((aligned(16))) char lds[];
    const int tid = threadIdx.x, lane = tid & 63, r32 = lane & 31, hi = lane >> 5;
    const int wid = __builtin_amdgcn_readfirstlane(tid >> 6);
    const int b = blockIdx.x, xcd = b & 7, idx = b >> 3;
    const int tt = xcd * 4 + (idx & 3), nt = idx >> 2;
    const int th = wid & 1, nh = wid >> 1;
    const unsigned lds0 = (unsigned)(uintptr_t)lds;
    const char* og = ws + WS_O + (size_t)tt * 2 * 32768;
    const char* hg = ws + WS_H + (size_t)tt * 2 * 57344;
    const unsigned tvoff = lane * 16;
    unsigned wvoff[4];
#pragma unroll
    for (int e = 0; e < 4; ++e) { const int row = wid * 32 + e * 8 + (lane >> 3); wvoff[e] = (unsigned)(row * 1024 + nt * 128 + (((lane & 7) ^ (((row >> 1) & 1) << 2)) * 16)); }
    auto dma = [&](int st) __attribute__((always_inline)) {
        const unsigned d = lds0 + (st & 3) * K3_STAGE;
        const bool first = st < 4; const int k0 = first ? st * 128 : (st - 4) * 128;
        const char* tg = (first ? og : hg) + (size_t)(k0 >> 4) * 1024; const int tstr = first ? 32768 : 57344;
#pragma unroll
        for (int e = 0; e < 4; ++e) glds16s(tvoff, tg + (size_t)th * tstr + (nh * 4 + e) * 1024, d + th * 8192 + (nh * 4 + e) * 1024);
        const char* wg = (const char*)(first ? wo : wout) + (size_t)k0 * 1024;
#pragma unroll
        for (int e = 0; e < 4; ++e) glds16s(wvoff[e], wg, d + 16384 + wid * 4096 + e * 1024);
    };
    dma(0); dma(1); dma(2);
    f32x16 accs = {}, accb = {};
    const int g = lane >> 4, qq = (lane & 15) >> 2, pp = lane & 3;
    const int kr = 8 * (g >> 1) + qq;
    const unsigned woff0 = (unsigned)(16384 + kr * 128 + ((nh ^ ((kr >> 1) & 1)) * 64) + (g & 1) * 32 + pp * 8);
    const unsigned woff1 = (unsigned)(16384 + (kr + 4) * 128 + ((nh ^ (((kr + 4) >> 1) & 1)) * 64) + (g & 1) * 32 + pp * 8);
    const unsigned toff = (unsigned)(th * 8192 + lane * 16);
    for (int st = 0; st < 11; ++st) {
        if (st < 9) WAIT_BAR(16); else if (st == 9) WAIT_BAR(8); else WAIT_BAR(0);
        if (st + 3 < 11) dma(st + 3);
        const lds_cptr sb = (lds_cptr)lds + (st & 3) * K3_STAGE;
        if (st < 4) {
#pragma unroll
            for (int ks = 0; ks < 8; ++ks) accs = MFMA32(cat(ldstr(sb + woff0 + ks * 2048), ldstr(sb + woff1 + ks * 2048)), ldsr128(sb + toff + ks * 1024), accs);
        } else {
#pragma unroll
            for (int ks = 0; ks < 8; ++ks) accb = MFMA32(cat(ldstr(sb + woff0 + ks * 2048), ldstr(sb + woff1 + ks * 2048)), ldsr128(sb + toff + ks * 1024), accb);
        }
    }
    const int t = tt * 64 + th * 32 + r32, n0 = nt * 64 + nh * 32 + 4 * hi;
#pragma unroll
    for (int q = 0; q < 4; ++q) {
        const u32x2 xv = *(const u32x2*)(x + (size_t)t * D + n0 + 8 * q);
        float v[4];
#pragma unroll
        for (int r = 0; r < 4; ++r) { const float bb = accb[4 * q + r]; const float sg = bb * __builtin_amdgcn_rcpf(1.0f + __builtin_amdgcn_exp2f(-LOG2E * bb)); v[r] = accs[4 * q + r] * sg; }
        u32x2 w = {cvtpk(bflo(xv[0]) + v[0], bfhi(xv[0]) + v[1]), cvtpk(bflo(xv[1]) + v[2], bfhi(xv[1]) + v[3])};
        *(u32x2*)(out + (size_t)t * D + n0 + 8 * q) = w;
    }
}

extern "C" void kernel_launch(void* const* d_in, const int* in_sizes, int n_in, void* d_out, int out_size, void* d_ws, size_t ws_size, hipStream_t stream) {
    const u16* x = (const u16*)d_in[0]; const u16* wqkv = (const u16*)d_in[1]; const u16* wo = (const u16*)d_in[2];
    const u16* win = (const u16*)d_in[4]; const u16* wsp = (const u16*)d_in[5]; const u16* bsp = (const u16*)d_in[6]; const u16* wout = (const u16*)d_in[7];
    char* ws = (char*)d_ws;
    (void)hipFuncSetAttribute((const void*)k1_proj, hipFuncAttributeMaxDynamicSharedMemorySize, K1_LDS);
    (void)hipFuncSetAttribute((const void*)k2_mix, hipFuncAttributeMaxDynamicSharedMemorySize, K2_LDS);
    (void)hipFuncSetAttribute((const void*)k3_out, hipFuncAttributeMaxDynamicSharedMemorySize, 4 * K3_STAGE);
    k1_proj<<<256, 256, K1_LDS, stream>>>(x, wqkv, win, ws);
    k1b_norm<<<512, 256, 0, stream>>>(ws);
    k2_mix<<<512, 512, K2_LDS, stream>>>(wsp, bsp, ws);
    k3_out<<<256, 256, 4 * K3_STAGE, stream>>>(x, wo, wout, ws, (u16*)d_out);
}
```

```cpp
#include <hip/hip_runtime.h>
#include <hip/hip_bf16.h>
#include <stdint.h>
#include <type_traits>

typedef unsigned short u16;
using bf16x8 = __attribute__((ext_vector_type(8))) short;
using s16x4 = __attribute__((ext_vector_type(4))) short;
using f32x16 = __attribute__((ext_vector_type(16))) float;
using f32x4 = __attribute__((ext_vector_type(4))) float;
using u32x4 = __attribute__((ext_vector_type(4))) unsigned;
using u32x2 = __attribute__((ext_vector_type(2))) unsigned;
typedef __bf16 bf16x2_t __attribute__((ext_vector_type(2)));
typedef float f32x2_t __attribute__((ext_vector_type(2)));
typedef short v4i16_t __attribute__((ext_vector_type(4)));
#define LDSAS __attribute__((address_space(3)))
typedef LDSAS const char* lds_cptr;
#define MFMA32(a, b, c) __builtin_amdgcn_mfma_f32_32x32x16_bf16(a, b, c, 0, 0, 0)
#define MFMA16(a, b, c) __builtin_amdgcn_mfma_f32_16x16x32_bf16(a, b, c, 0, 0, 0)
#define SBAR() __builtin_amdgcn_sched_barrier(0)
#define PIN(x) asm volatile("" : "+v"(x))
#define WAIT_BAR(N) asm volatile("s_waitcnt vmcnt(" #N ") lgkmcnt(0)\n\ts_barrier" ::: "memory")
#define LBAR() asm volatile("s_waitcnt lgkmcnt(0)\n\ts_barrier" ::: "memory")
#define DI __device__ __forceinline__
#define STAMP(kern, i) do { } while (0)

constexpr int S = 2048, D = 512, H = 32, DH = 16, FH = 896;
constexpr float LOG2E = 1.4426950408889634f;

constexpr size_t WS_Q = 0, WS_K = 2u << 20, WS_V = 4u << 20, WS_Z1 = 6u << 20, WS_Z2R = 10u << 20, WS_Z2N = 14u << 20, WS_H = 18u << 20, WS_O = 22u << 20, WS_KMAX = 24u << 20, WS_EPOCH = 25u << 20, WS_XCH = 26u << 20;

DI unsigned cvtpk(float lo, float hi) { f32x2_t v = {lo, hi}; return __builtin_bit_cast(unsigned, __builtin_convertvector(v, bf16x2_t)); }
DI float bflo(unsigned w) { return __uint_as_float(w << 16); }
DI float bfhi(unsigned w) { return __uint_as_float(w & 0xFFFF0000u); }
DI float dot2(unsigned a, unsigned b, float c) { return __builtin_amdgcn_fdot2_f32_bf16(__builtin_bit_cast(bf16x2_t, a), __builtin_bit_cast(bf16x2_t, b), c, false); }
DI void glds16s(unsigned voff, const void* sbase, unsigned lds_base) {
    unsigned sv; asm volatile("s_mov_b32 %0, m0\n\ts_mov_b32 m0, %3\n\ts_nop 0\n\tglobal_load_lds_dwordx4 %1, %2\n\ts_mov_b32 m0, %0" : "=&s"(sv) : "v"(voff), "s"(sbase), "s"(lds_base) : "memory"); }
DI bf16x8 ldsr128(lds_cptr p) { return *(const LDSAS bf16x8*)p; }
DI s16x4 ldstr(lds_cptr p) { return __builtin_bit_cast(s16x4, __builtin_amdgcn_ds_read_tr16_b64_v4i16((LDSAS v4i16_t*)p)); }
DI bf16x8 cat(s16x4 a, s16x4 b) { return __builtin_shufflevector(a, b, 0, 1, 2, 3, 4, 5, 6, 7); }
DI float swap32(float x, bool sum) { auto rr = __builtin_amdgcn_permlane32_swap(__float_as_uint(x), __float_as_uint(x), false, false); return sum ? __uint_as_float(rr[0]) + __uint_as_float(rr[1]) : __uint_as_float(rr[0]); }
DI float gelu_t(float x) {
    const float c1 = -2.0f * 0.7978845608028654f * LOG2E, c3 = c1 * 0.044715f;
    const float u = x * fmaf(x * x, c3, c1);
    return x * __builtin_amdgcn_rcpf(1.0f + __builtin_amdgcn_exp2f(u));
}

constexpr int K1_KSB = 13312;
constexpr int K1_XB = 16384;
constexpr int K1_STAGE = K1_XB + 2 * K1_KSB;
constexpr int K1_CS = 3 * K1_STAGE;
constexpr int K1_ST = K1_CS + 3328;
constexpr int K1_LDS = K1_ST + 1024;
typedef __attribute__((address_space(1))) unsigned gu32; typedef __attribute__((address_space(1))) unsigned long long gu64;
#define RLX_AGENT __ATOMIC_RELAXED, __HIP_MEMORY_SCOPE_AGENT

DI float xor16(float x) { auto r = __builtin_amdgcn_permlane16_swap(__float_as_uint(x), __float_as_uint(x), false, false); return __uint_as_float(r[0]) + __uint_as_float(r[1]); }
DI float xor32(float x) { auto r = __builtin_amdgcn_permlane32_swap(__float_as_uint(x), __float_as_uint(x), false, false); return __uint_as_float(r[0]) + __uint_as_float(r[1]); }
template <int CTRL> DI float dppf(float x) { return __uint_as_float(__builtin_amdgcn_update_dpp(0, __float_as_uint(x), CTRL, 0xF, 0xF, false)); }
DI float rowmax16(float x) { x = fmaxf(x, dppf<0x128>(x)); x = fmaxf(x, dppf<0x124>(x)); x = fmaxf(x, dppf<0x122>(x)); x = fmaxf(x, dppf<0x121>(x)); return x; }

__global__ __launch_bounds__(640, 3) void k1_proj(const u16* __restrict__ x, const u16* __restrict__ wqkv, const u16* __restrict__ win, char* __restrict__ ws) {
    extern __shared__ __attribute__((aligned(16))) char lds[];
    const int tid = threadIdx.x, lane = tid & 63, r16 = lane & 15, g = lane >> 4;
    const int wid = __builtin_amdgcn_readfirstlane(tid >> 6);
    const int mw = wid & 3, nh = wid >> 2;
    const int b = blockIdx.x, xcd = b & 7, idx = b >> 3;
    const int panel = 8 * (xcd >> 2) + (idx & 7), j = 4 * (xcd & 3) + (idx >> 3);
    const unsigned lds0 = (unsigned)(uintptr_t)lds;
    const int pi = j >> 1, odd = j & 1;
    const int n1 = odd ? 3 : 4;
    const int z1f0 = 7 * pi + (odd ? 4 : 0), z2f0 = 7 * pi + (odd ? 3 : 0);
    STAMP(0, 0);
    if (wid >= 8) {
        const int l = wid - 8;
        const char* xb = (const char*)x + (size_t)(panel * 128 + l * 64) * 1024;
        const unsigned xv0 = (unsigned)((lane >> 3) * 1024 + (((lane & 7) ^ (lane >> 4)) * 16)), xv1 = (unsigned)((lane >> 3) * 1024 + (((lane & 7) ^ (4 + (lane >> 4))) * 16));
        unsigned voq[2], voz[7];
#pragma unroll
        for (int e = 0; e < 2; ++e) { const int rho = 16 * e + (lane >> 2); const int k = 8 * ((rho >> 2) & 3) + 4 * (rho >> 4) + (rho & 3);
            voq[e] = (unsigned)(k * 3072 + j * 64 + (((lane & 3) ^ (((rho >> 2) & 1) << 1)) * 16)); }
        const char* wq = (const char*)wqkv + (size_t)l * 32 * 3072; const char* wi = (const char*)win + (size_t)l * 32 * 3584;
        auto dmax = [&](int st) __attribute__((always_inline)) { const unsigned d = lds0 + (st % 3) * K1_STAGE + l * 8192;
#pragma unroll
            for (int p = 0; p < 8; ++p) glds16s((p & 1) ? xv1 : xv0, xb + st * 128 + p * 8192, d + p * 1024); };
        auto dmaq = [&](int st) __attribute__((always_inline)) { const unsigned d = lds0 + (st % 3) * K1_STAGE + K1_XB + l * K1_KSB; const char* bq = wq + (size_t)st * 64 * 3072;
            glds16s(voq[0], bq + 2048, d); glds16s(voq[1], bq + 2048, d + 1024); glds16s(voq[0], bq, d + 2048); glds16s(voq[1], bq, d + 3072); glds16s(voq[0], bq + 1024, d + 4096); glds16s(voq[1], bq + 1024, d + 5120); };
        dmax(0); dmaq(0);
#pragma unroll
        for (int e = 0; e < 7; ++e) { const int ii = 64 * e + lane; const int rho = (ii * 4682) >> 16, c = ii - rho * 14; const int k = 8 * ((rho >> 2) & 3) + 4 * (rho >> 4) + (rho & 3);
            const int colb = (c < 2 * n1) ? (z1f0 * 32 + c * 16) : (FH * 2 + z2f0 * 32 + (c - 2 * n1) * 16);
            voz[e] = (unsigned)(k * 3584 + colb); }
        auto dmaz = [&](int st) __attribute__((always_inline)) { const unsigned d = lds0 + (st % 3) * K1_STAGE + K1_XB + l * K1_KSB + 6144; const char* bz = wi + (size_t)st * 64 * 3584;
#pragma unroll
            for (int e = 0; e < 7; ++e) glds16s(voz[e], bz, d + e * 1024); };
        dmaz(0);
        dmax(1); dmaq(1); dmaz(1); dmax(2); dmaq(2); dmaz(2);
        WAIT_BAR(42);
#pragma unroll
        for (int st = 1; st < 8; ++st) {
            if (st < 7) WAIT_BAR(21); else WAIT_BAR(0);
            if (st + 2 < 8) { dmax(st + 2); dmaq(st + 2); dmaz(st + 2); }
        }
        return;
    }
    gu32* epw = (gu32*)(ws + WS_EPOCH) + panel * 64;
    const unsigned tag = __hip_atomic_load(epw, RLX_AGENT) + 1u;

    f32x4 acc[2][7];
#pragma unroll
    for (int m = 0; m < 2; ++m)
#pragma unroll
        for (int s = 0; s < 7; ++s) acc[m][s] = (f32x4){0.f, 0.f, 0.f, 0.f};
    float cs[7];
#pragma unroll
    for (int s = 0; s < 7; ++s) cs[s] = 0.f;
    float sx[2] = {0.f, 0.f};
    const int qq = r16 >> 2, pp = lane & 3, rr = 4 * g + qq;
    const unsigned wq0 = (unsigned)(rr * 64 + ((0 ^ (g & 1)) * 32) + pp * 8), wq1 = (unsigned)(rr * 64 + ((1 ^ (g & 1)) * 32) + pp * 8);
    const unsigned wz = (unsigned)(6144 + rr * 224 + pp * 8);
    const unsigned xo0 = (unsigned)((32 * mw + r16) * 128 + ((g ^ ((r16 >> 1) & 7)) * 16)), xo1 = (unsigned)((32 * mw + r16) * 128 + (((4 + g) ^ ((r16 >> 1) & 7)) * 16));
    auto mainloop = [&](auto NHC) __attribute__((always_inline)) {
        constexpr int NHV = decltype(NHC)::value; constexpr int NS = NHV == 0 ? 7 : 6;
        bf16x8 xa[2][2], wf[2][NS];
        auto rd = [&](const int set, const lds_cptr sb, const int kk) __attribute__((always_inline)) {
            const lds_cptr wb = sb + K1_XB + kk * K1_KSB;
            xa[set][0] = ldsr128(sb + (kk ? xo1 : xo0)); xa[set][1] = ldsr128(sb + (kk ? xo1 : xo0) + 2048);
            if (NHV == 0) {
#pragma unroll
                for (int s = 0; s < 6; ++s) { const lds_cptr p = wb + ((s & 1) ? wq1 : wq0) + (s >> 1) * 2048; wf[set][s] = cat(ldstr(p), ldstr(p + 1024)); }
                { const lds_cptr p = wb + wz; wf[set][NS - 1] = cat(ldstr(p), ldstr(p + 16 * 224)); }
            } else {
#pragma unroll
                for (int s = 0; s < 6; ++s) { const lds_cptr p = wb + wz + (s + 1) * 32; wf[set][s] = cat(ldstr(p), ldstr(p + 16 * 224)); }
            }
        };
        auto mm = [&](const int set, const int ks) __attribute__((always_inline)) {
            const bf16x8 x0 = xa[set][0], x1 = xa[set][1];
#pragma unroll
            for (int s = 0; s < NS; ++s) {
                if (NHV == 0 && s < 2) { acc[0][s] = MFMA16(x0, wf[set][s], acc[0][s]); acc[1][s] = MFMA16(x1, wf[set][s], acc[1][s]); }
                else { acc[0][s] = MFMA16(wf[set][s], x0, acc[0][s]); acc[1][s] = MFMA16(wf[set][s], x1, acc[1][s]); }
            }
            const u32x4 a = __builtin_bit_cast(u32x4, x0), c = __builtin_bit_cast(u32x4, x1);
#pragma unroll
            for (int i = 0; i < 4; ++i) { sx[0] = dot2(a[i], NHV == 0 ? 0x3F803F80u : a[i], sx[0]); sx[1] = dot2(c[i], NHV == 0 ? 0x3F803F80u : c[i], sx[1]); }
#pragma unroll
            for (int i = 0; i < 2 * NS; ++i) { __builtin_amdgcn_sched_group_barrier(0x008, 1, 0); __builtin_amdgcn_sched_group_barrier(0x100, 1, 0); if (i < 8) __builtin_amdgcn_sched_group_barrier(0x002, 1, 0); }
            __builtin_amdgcn_sched_group_barrier(0x100, 2, 0);
            if ((ks & 3) == mw) {
#pragma unroll
                for (int s = 0; s < NS; ++s) { const u32x4 w = __builtin_bit_cast(u32x4, wf[set][s]);
#pragma unroll
                    for (int i = 0; i < 4; ++i) cs[s] = dot2(w[i], 0x3F803F80u, cs[s]); } }
        };
        int slot = 0;
        LBAR();
        STAMP(0, 1);
        rd(0, (lds_cptr)lds, 0);
        for (int st = 0; st < 8; ++st) {
            const lds_cptr sb = (lds_cptr)lds + slot * K1_STAGE;
            rd(1, sb, 1); mm(0, 2 * st);
            slot = slot == 2 ? 0 : slot + 1;
            if (st < 7) {
                LBAR();
                rd(0, (lds_cptr)lds + slot * K1_STAGE, 0);
            }
            mm(1, 2 * st + 1);
        }
    };
    if (nh == 0) mainloop(std::integral_constant<int, 0>{}); else mainloop(std::integral_constant<int, 1>{});
    STAMP(0, 2);
    float* csl = (float*)(lds + K1_CS); float* stl = (float*)(lds + K1_ST);
#pragma unroll
    for (int s = 0; s < 7; ++s) { const float v = xor32(xor16(cs[s])); if (g == 0 && (nh == 0 || s < 6)) csl[(mw * 13 + nh * 7 + s) * 16 + r16] = v; }
#pragma unroll
    for (int m = 0; m < 2; ++m) { const float v = xor32(xor16(sx[m])); if (g == 0) stl[nh * 128 + mw * 32 + m * 16 + r16] = v; }
    __syncthreads();
    STAMP(0, 3);
    float mu[2], rs[2];
#pragma unroll
    for (int m = 0; m < 2; ++m) { const float a = stl[mw * 32 + m * 16 + r16], q = stl[128 + mw * 32 + m * 16 + r16];
        mu[m] = a * (1.0f / 512.0f); rs[m] = __builtin_amdgcn_rsqf(fmaxf(q * (1.0f / 512.0f) - mu[m] * mu[m], 0.f) + 1e-5f); }
    const int tok0 = panel * 128 + mw * 32;
    auto cs4 = [&](int s) __attribute__((always_inline)) { f32x4 v = *(const f32x4*)(csl + s * 16 + 4 * g);
#pragma unroll
        for (int w = 1; w < 4; ++w) v += *(const f32x4*)(csl + (w * 13 + s) * 16 + 4 * g); return v; };
    u16* Z1 = (u16*)(ws + WS_Z1);
    auto z1store = [&](int zi, int m, u32x2 w) __attribute__((always_inline)) { const int f = z1f0 + zi, tok = tok0 + 16 * m + r16; const int q = 2 * (f & 1) + (g >> 1), hi = g & 1;
        *(u32x2*)(Z1 + ((size_t)((tok >> 5) * 28 + (f >> 1)) * 1024) + ((q * 2 + hi) * 32 + (tok & 31)) * 4) = w; };
    if (nh == 0) {
        u16* Qs = (u16*)(ws + WS_Q); u16* Kb = (u16*)(ws + WS_K); u16* Vt = (u16*)(ws + WS_V); float* kmax = (float*)(ws + WS_KMAX);
#pragma unroll
        for (int m = 0; m < 2; ++m) {
            const f32x4 a4 = *(const f32x4*)(stl + mw * 32 + m * 16 + 4 * g), q4 = *(const f32x4*)(stl + 128 + mw * 32 + m * 16 + 4 * g);
            float mm[4], rv[4];
#pragma unroll
            for (int r = 0; r < 4; ++r) { mm[r] = a4[r] * (1.0f / 512.0f); rv[r] = __builtin_amdgcn_rsqf(fmaxf(q4[r] * (1.0f / 512.0f) - mm[r] * mm[r], 0.f) + 1e-5f); }
#pragma unroll
            for (int s = 0; s < 2; ++s) {
                const float c = csl[s * 16 + r16] + csl[(13 + s) * 16 + r16] + csl[(26 + s) * 16 + r16] + csl[(39 + s) * 16 + r16];
                float v[4];
#pragma unroll
                for (int r = 0; r < 4; ++r) v[r] = rv[r] * (acc[m][s][r] - mm[r] * c);
                u32x2 w = {cvtpk(v[0], v[1]), cvtpk(v[2], v[3])};
                *(u32x2*)(Vt + ((size_t)(2 * j + s) * 128 + ((tok0 + 16 * m) >> 4)) * 256 + (g & 1) * 128 + r16 * 8 + (g >> 1) * 4) = w;
            }
        }
#pragma unroll
        for (int s = 2; s < 6; ++s) {
            const f32x4 c = cs4(s); const int head = 2 * j + (s & 1); const bool isq = s < 4;
            float kn = 0.f;
#pragma unroll
            for (int m = 0; m < 2; ++m) {
                const float sc = isq ? rs[m] * (0.25f * LOG2E) : rs[m];
                float v[4];
#pragma unroll
                for (int r = 0; r < 4; ++r) v[r] = sc * (acc[m][s][r] - mu[m] * c[r]);
                u32x2 w = {cvtpk(v[0], v[1]), cvtpk(v[2], v[3])};
                *(u32x2*)((isq ? Qs : Kb) + ((size_t)head * S + tok0 + 16 * m + r16) * 16 + 4 * g) = w;
                if (!isq) { float n = bflo(w[0]) * bflo(w[0]); n = fmaf(bfhi(w[0]), bfhi(w[0]), n); n = fmaf(bflo(w[1]), bflo(w[1]), n); n = fmaf(bfhi(w[1]), bfhi(w[1]), n);
                    kn = fmaxf(kn, xor32(xor16(n))); }
            }
            if (!isq) { kn = rowmax16(kn); if (lane == 0) kmax[(panel * 4 + mw) * H + head] = kn; }
        }
        { const f32x4 c = cs4(6);
#pragma unroll
          for (int m = 0; m < 2; ++m) { float v[4];
#pragma unroll
              for (int r = 0; r < 4; ++r) v[r] = gelu_t(rs[m] * (acc[m][6][r] - mu[m] * c[r]));
              z1store(0, m, (u32x2){cvtpk(v[0], v[1]), cvtpk(v[2], v[3])}); } }
    } else {
        u32x2 zw[2][6];
#pragma unroll
        for (int s = 5; s >= 0; --s) { const f32x4 c = cs4(7 + s);
#pragma unroll
            for (int m = 0; m < 2; ++m) { float v[4];
#pragma unroll
                for (int r = 0; r < 4; ++r) v[r] = gelu_t(rs[m] * (acc[m][s][r] - mu[m] * c[r]));
                zw[m][s] = (u32x2){cvtpk(v[0], v[1]), cvtpk(v[2], v[3])}; }
            if (s == 2) {
                gu64* xg = (gu64*)(ws + WS_XCH) + ((size_t)(panel * 16 + j) * 128 + mw * 32) * 2;
#pragma unroll
                for (int m = 0; m < 2; ++m) {
                    float sm = 0.f;
#pragma unroll
                    for (int t = 2; t < 6; ++t) { const float a = (bflo(zw[m][t][0]) + bfhi(zw[m][t][0])) + (bflo(zw[m][t][1]) + bfhi(zw[m][t][1])); sm += (t > 2 || odd) ? a : 0.f; }
                    sm = xor32(xor16(sm));
                    const float ml = sm * (odd ? (1.0f / 64.f) : (1.0f / 48.f)); float m2 = 0.f;
#pragma unroll
                    for (int t = 2; t < 6; ++t) { const float d0 = bflo(zw[m][t][0]) - ml, d1 = bfhi(zw[m][t][0]) - ml, d2 = bflo(zw[m][t][1]) - ml, d3 = bfhi(zw[m][t][1]) - ml;
                        const float a = (d0 * d0 + d1 * d1) + (d2 * d2 + d3 * d3); m2 += (t > 2 || odd) ? a : 0.f; }
                    m2 = xor32(xor16(m2));
                    if (g < 2) __hip_atomic_store(xg + (m * 16 + r16) * 2 + g, ((unsigned long long)tag << 32) | __float_as_uint(g ? m2 : sm), RLX_AGENT);
                }
            }
        }
        STAMP(0, 4);
#pragma unroll
        for (int s = 0; s < 3; ++s) if (s + 1 < n1) {
#pragma unroll
            for (int m = 0; m < 2; ++m) z1store(s + 1, m, zw[m][s]); }
        float mean[2], rstd[2];
        {
            gu64* xg = (gu64*)(ws + WS_XCH) + ((size_t)(panel * 16 + 4 * g) * 128 + mw * 32 + r16) * 2;
            float ps[2][4], pm[2][4];
            for (unsigned spins = 0;; ++spins) {
                bool ok = true;
#pragma unroll
                for (int m = 0; m < 2; ++m)
#pragma unroll
                    for (int u = 0; u < 4; ++u) {
                        const unsigned long long a = __hip_atomic_load(xg + (size_t)(u * 128 + m * 16) * 2, RLX_AGENT), c = __hip_atomic_load(xg + (size_t)(u * 128 + m * 16) * 2 + 1, RLX_AGENT);
                        ps[m][u] = __uint_as_float((unsigned)a); pm[m][u] = __uint_as_float((unsigned)c); ok &= ((unsigned)(a >> 32) == tag) & ((unsigned)(c >> 32) == tag); }
                if (__all(ok) || spins > 200000u) break;
                __builtin_amdgcn_s_sleep(1);
            }
#pragma unroll
            for (int m = 0; m < 2; ++m) {
                const float tot = xor32(xor16((ps[m][0] + ps[m][1]) + (ps[m][2] + ps[m][3])));
                mean[m] = tot * (1.0f / FH);
                float m2 = 0.f;
#pragma unroll
                for (int u = 0; u < 4; ++u) { const float nl = (u & 1) ? 64.f : 48.f; const float d = ps[m][u] * ((u & 1) ? (1.0f / 64.f) : (1.0f / 48.f)) - mean[m]; m2 += pm[m][u] + nl * d * d; }
                m2 = xor32(xor16(m2));
                rstd[m] = __builtin_amdgcn_rsqf(m2 * (1.0f / FH) + 1e-5f);
            }
        }
        STAMP(0, 5);
        if (wid == 4 && lane == 0) __hip_atomic_store(epw, tag, RLX_AGENT);
        u16* Z2N = (u16*)(ws + WS_Z2N);
#pragma unroll
        for (int s = 2; s < 6; ++s) if (s > 2 || odd) {
            const int f = z2f0 + (s + 1 - n1);
#pragma unroll
            for (int m = 0; m < 2; ++m) { const int tok = tok0 + 16 * m + r16;
                const u32x2 w = {cvtpk((bflo(zw[m][s][0]) - mean[m]) * rstd[m], (bfhi(zw[m][s][0]) - mean[m]) * rstd[m]), cvtpk((bflo(zw[m][s][1]) - mean[m]) * rstd[m], (bfhi(zw[m][s][1]) - mean[m]) * rstd[m])};
                *(u32x2*)(Z2N + ((size_t)(((f >> 3) * 32 + (tok >> 6)) * 4 + ((f >> 1) & 3)) * 64 + (tok & 63)) * 32 + (f & 1) * 16 + 4 * g) = w; }
        }
    }
    STAMP(0, 6);
}

constexpr int A_KR = 0, A_VR = 32768, A_CONST = 65536, K2_LDS = 81920;
constexpr float SAFE_T = 40.f;

DI void attn_role(int b, char* __restrict__ ws, char* lds) {
    const u16* Qs = (const u16*)(ws + WS_Q); const u16* Kb = (const u16*)(ws + WS_K); const u16* Vt = (const u16*)(ws + WS_V); const float* kmax = (const float*)(ws + WS_KMAX); u16* Ot = (u16*)(ws + WS_O);
    const int tid = threadIdx.x, lane = tid & 63, r32 = lane & 31, hi = lane >> 5;
    const int wid = __builtin_amdgcn_readfirstlane(tid >> 6);
    const int xcd = b & 7, idx = b >> 3, head = xcd * 4 + (idx & 3), qt = idx >> 2;
    const int qp = wid & 3, kh = wid >> 2;
    const unsigned lds0 = (unsigned)(uintptr_t)lds;
    const char* kg = (const char*)(Kb + (size_t)head * S * DH);
    const char* vg = (const char*)(Vt + (size_t)head * S * DH);
    const unsigned kvoff = wid * 1024 + ((lane ^ ((lane >> 5) & 1)) * 16);
    const unsigned vvoff = wid * 1024 + lane * 16;
#define DMA_CHUNK(c) do { glds16s(kvoff, kg + (c) * 8192, lds0 + A_KR + ((c) & 3) * 8192 + wid * 1024); glds16s(vvoff, vg + (c) * 8192, lds0 + A_VR + ((c) & 3) * 8192 + wid * 1024); } while (0)
    STAMP(1, 0);
    DMA_CHUNK(0); DMA_CHUNK(1);
    const int q0 = qt * 256 + qp * 64;
    const bf16x8 qa = *(const bf16x8*)(Qs + ((size_t)head * S + q0 + r32) * DH + hi * 8);
    const bf16x8 qb = *(const bf16x8*)(Qs + ((size_t)head * S + q0 + 32 + r32) * DH + hi * 8);
    float km = kmax[lane * H + head];
    DMA_CHUNK(2);
    if (tid < 256) { const unsigned w = (tid & 31) ? 0u : 0x3F803F80u; u32x4 v = {w, w, w, w}; *(u32x4*)(lds + A_CONST + tid * 16) = v; }
#pragma unroll
    for (int o = 1; o < 64; o <<= 1) km = fmaxf(km, __shfl_xor(km, o));
    float qna = 0.f, qnb = 0.f;
#pragma unroll
    for (int j = 0; j < 8; ++j) { const float va = bflo((u16)qa[j]), vb = bflo((u16)qb[j]); qna = fmaf(va, va, qna); qnb = fmaf(vb, vb, qnb); }
    qna = swap32(qna, true); qnb = swap32(qnb, true);
    const float mh = __builtin_sqrtf(fmaxf(qna, qnb) * km) * 1.001f;
    f32x16 cinit;
#pragma unroll
    for (int r = 0; r < 16; ++r) cinit[r] = -mh;
    PIN(cinit);
    f32x16 oa = {}, ob = {};
    const lds_cptr kp0 = (lds_cptr)lds + A_KR + kh * 4096 + r32 * 32 + ((hi ^ (r32 >> 4)) * 16);
    const bool vreal = r32 < 16;
    const unsigned vlane = vreal ? (unsigned)(A_VR + kh * 4096 + hi * 256 + r32 * 16) : (unsigned)(A_CONST + (r32 == 16 ? 0 : ((r32 >= 20 && r32 < 28) ? 64 : 16)));
    const unsigned vmask = vreal ? 0xFFFFFFFFu : 0u;
    WAIT_BAR(2);
    bf16x8 kf, v0, v1; f32x16 sA, sB; u32x4 p0, p1;
#define EX(x) __builtin_amdgcn_exp2f(x)
#define BF(p) __builtin_bit_cast(bf16x8, p)
    kf = ldsr128(kp0);
    { lds_cptr vp = (lds_cptr)lds + vlane; v0 = ldsr128(vp); v1 = ldsr128(vp + 512); }
    sA = MFMA32(kf, qa, cinit);
    p1 = (u32x4){0u, 0u, 0u, 0u};
#define HSTEP(SC, SN, QN, OP, OC, PRE1, PRE2, PRE3) do { \
        SN = MFMA32(kf, QN, cinit); PIN(SN); SBAR(); \
        PRE1; \
        SC[0] = EX(SC[0]); SC[1] = EX(SC[1]); SC[2] = EX(SC[2]); SC[3] = EX(SC[3]); SC[4] = EX(SC[4]); SC[5] = EX(SC[5]); PIN(SC); SBAR(); \
        OP = MFMA32(v1, BF(p1), OP); PIN(OP); SBAR(); \
        PRE2; \
        SC[6] = EX(SC[6]); SC[7] = EX(SC[7]); \
        p0[0] = cvtpk(SC[0], SC[1]); p0[1] = cvtpk(SC[2], SC[3]); p0[2] = cvtpk(SC[4], SC[5]); p0[3] = cvtpk(SC[6], SC[7]); PIN(p0); SBAR(); \
        SC[8] = EX(SC[8]); SC[9] = EX(SC[9]); SC[10] = EX(SC[10]); PIN(SC); SBAR(); \
        OC = MFMA32(v0, BF(p0), OC); PIN(OC); SBAR(); \
        PRE3; \
        SC[11] = EX(SC[11]); SC[12] = EX(SC[12]); SC[13] = EX(SC[13]); SC[14] = EX(SC[14]); SC[15] = EX(SC[15]); \
        p1[0] = cvtpk(SC[8], SC[9]); p1[1] = cvtpk(SC[10], SC[11]); p1[2] = cvtpk(SC[12], SC[13]); p1[3] = cvtpk(SC[14], SC[15]); PIN(p1); SBAR(); \
    } while (0)
#define BLOCK(kn, vc, vco, vn, vno) do { \
        HSTEP(sA, sB, qb, ob, oa, kf = ldsr128(kn), v1 = ldsr128((vc) + (vco) + 512), (void)0); \
        HSTEP(sB, sA, qa, oa, ob, (void)0, (void)0, v0 = ldsr128((vn) + (vno))); \
    } while (0)
    STAMP(1, 1);
    for (int c = 0; c < 8; ++c) {
        if (c > 0) { if (c < 6) WAIT_BAR(2); else if (c == 6) WAIT_BAR(0); else asm volatile("s_waitcnt lgkmcnt(0)\n\ts_barrier" ::: "memory"); }
        if (c + 3 < 8 && c > 0) DMA_CHUNK(c + 3);
        if (c == 0) DMA_CHUNK(3);
        const unsigned ro = (unsigned)((c & 3) * 8192), rn = (unsigned)(((c + 1) & 3) * 8192);
        const lds_cptr vp = (lds_cptr)lds + (vlane + (ro & vmask)), vpn = (lds_cptr)lds + (vlane + (rn & vmask));
        const lds_cptr kp = kp0 + ro, kpn = kp0 + rn;
        BLOCK(kp + 1024, vp, 0, vp, 1024);
        BLOCK(kp + 2048, vp, 1024, vp, 2048);
        BLOCK(kp + 3072, vp, 2048, vp, 3072);
        BLOCK(kpn, vp, 3072, vpn, 0);
    }
    ob = MFMA32(v1, BF(p1), ob);
    STAMP(1, 2);
    __syncthreads();
    float* red = (float*)lds;
    { float* dst = red + (size_t)(wid ^ 4) * 9 * 64 + lane;
#pragma unroll
      for (int r = 0; r < 9; ++r) dst[r * 64] = kh ? oa[r] : ob[r]; }
    __syncthreads();
    float o[9];
    { const float* src = red + (size_t)wid * 9 * 64 + lane;
#pragma unroll
      for (int r = 0; r < 9; ++r) o[r] = (kh ? ob[r] : oa[r]) + src[r * 64]; }
    const float rl = 1.0f / swap32(o[8], false);
    u16* op = Ot + ((size_t)(((q0 >> 5) + kh) * H + head) * 512) + r32 * 8 + hi * 4;
    u32x2 w0 = {cvtpk(o[0] * rl, o[1] * rl), cvtpk(o[2] * rl, o[3] * rl)};
    u32x2 w1 = {cvtpk(o[4] * rl, o[5] * rl), cvtpk(o[6] * rl, o[7] * rl)};
    *(u32x2*)op = w0; *(u32x2*)(op + 256) = w1;
    STAMP(1, 3);
#undef DMA_CHUNK
#undef HSTEP
#undef BLOCK
}

constexpr int G_STAGE = 24576;
DI void gate_role(int b, const u16* __restrict__ wsp, const u16* __restrict__ bsp, char* __restrict__ ws, char* lds) {
    const int xcd = b & 7, idx = b >> 3;
    if (idx >= 28) return;
    const int tt = xcd * 4 + (idx & 3), ct = idx >> 2;
    const int tid = threadIdx.x, lane = tid & 63, r32 = lane & 31, hi = lane >> 5;
    const int wid = __builtin_amdgcn_readfirstlane(tid >> 6);
    const unsigned lds0 = (unsigned)(uintptr_t)lds;
    STAMP(1, 0);
    if (wid >= 4) {
        const int l = wid - 4;
        const char* ag = ws + WS_Z2N + (size_t)ct * 32 * 16384 + l * 4096;
        const char* bg = (const char*)wsp + (size_t)(tt * 64 + l * 16) * 4096;
        const unsigned avoff = lane * 16;
        const unsigned bv0 = (unsigned)((lane >> 3) * 4096 + (((lane & 7) ^ (lane >> 4)) * 16)), bv1 = (unsigned)((8 + (lane >> 3)) * 4096 + (((lane & 7) ^ (4 + (lane >> 4))) * 16));
        auto dma = [&](int kt, int slot) __attribute__((always_inline)) { const unsigned d = lds0 + slot * G_STAGE; const char* a_ = ag + (size_t)kt * 16384;
#pragma unroll
            for (int e = 0; e < 4; ++e) glds16s(avoff, a_ + e * 1024, d + l * 4096 + e * 1024);
            glds16s(bv0, bg + kt * 128, d + 16384 + l * 2048); glds16s(bv1, bg + kt * 128, d + 16384 + l * 2048 + 1024); };
        dma(0, 0); dma(1, 1); dma(2, 2);
        WAIT_BAR(12);
        int slot = 0;
        for (int kt = 1; kt < 32; ++kt) {
            if (kt < 31) WAIT_BAR(6); else WAIT_BAR(0);
            if (kt + 2 < 32) dma(kt + 2, slot);
            slot = slot == 2 ? 0 : slot + 1;
        }
        return;
    }
#define GPRIO 2
    __builtin_amdgcn_s_setprio(GPRIO);
    const int th = wid & 1, fh = wid >> 1;
    f32x16 acc0 = {}, acc1 = {};
    const int g = lane >> 4, qq = (lane & 15) >> 2, pp = lane & 3;
    const unsigned aoff = (unsigned)(fh * 8192 + (8 * (g >> 1) + qq) * 64 + (g & 1) * 32 + pp * 8);
    const int trow = 32 * th + r32;
    unsigned boff[4];
#pragma unroll
    for (int ks = 0; ks < 4; ++ks) boff[ks] = (unsigned)(16384 + trow * 128 + (((2 * ks + hi) ^ ((trow >> 1) & 7)) * 16));
    bf16x8 fa0[2], fa1[2], fb[2];
    auto rd = [&](int set, lds_cptr sb, int ks) __attribute__((always_inline)) {
        fb[set] = ldsr128(sb + boff[ks]);
        fa0[set] = cat(ldstr(sb + aoff + ks * 1024), ldstr(sb + aoff + ks * 1024 + 256));
        fa1[set] = cat(ldstr(sb + aoff + 4096 + ks * 1024), ldstr(sb + aoff + 4096 + ks * 1024 + 256)); };
    auto mm = [&](int set) __attribute__((always_inline)) { acc0 = MFMA32(fa0[set], fb[set], acc0); acc1 = MFMA32(fa1[set], fb[set], acc1); };
    const int t = tt * 64 + trow;
    const float bias = bflo(bsp[t]);
    u32x2 z[2][4];
#pragma unroll
    for (int i = 0; i < 2; ++i)
#pragma unroll
        for (int q = 0; q < 4; ++q) z[i][q] = *(const u32x2*)((const u16*)(ws + WS_Z1) + ((size_t)((t >> 5) * 28 + ct * 4 + 2 * fh + i) * 1024) + (hi * 32 + r32) * 4 + q * 256);
    LBAR();
    rd(0, (lds_cptr)lds, 0);
    int slot = 0;
    for (int kt = 0; kt < 32; ++kt) {
        const lds_cptr sb = (lds_cptr)lds + slot * G_STAGE;
        rd(1, sb, 1); mm(0);
        rd(0, sb, 2); mm(1);
        rd(1, sb, 3); mm(0);
        slot = slot == 2 ? 0 : slot + 1;
        if (kt < 31) { LBAR(); rd(0, (lds_cptr)lds + slot * G_STAGE, 0); }
        mm(1);
    }
    STAMP(1, 2);
#pragma unroll
    for (int i = 0; i < 2; ++i) {
        u16* hp = (u16*)(ws + WS_H) + ((size_t)((t >> 5) * 56 + (ct * 4 + 2 * fh + i) * 2) * 512) + r32 * 8 + hi * 4;
#pragma unroll
        for (int q = 0; q < 4; ++q) {
            float v[4];
#pragma unroll
            for (int r = 0; r < 4; ++r) v[r] = (i ? acc1[4 * q + r] : acc0[4 * q + r]) + bias;
            u32x2 w = {cvtpk(v[0] * bflo(z[i][q][0]), v[1] * bfhi(z[i][q][0])), cvtpk(v[2] * bflo(z[i][q][1]), v[3] * bfhi(z[i][q][1]))};
            *(u32x2*)(hp + (q >> 1) * 512 + (q & 1) * 256) = w;
        }
    }
    STAMP(1, 3);
}

__global__ __launch_bounds__(512, 4) void k2_mix(const u16* __restrict__ wsp, const u16* __restrict__ bsp, char* __restrict__ ws) {
    extern __shared__ __attribute__((aligned(16))) char lds[];
    if (blockIdx.x < 256) attn_role(blockIdx.x, ws, lds);
    else gate_role(blockIdx.x - 256, wsp, bsp, ws, lds);
}

constexpr int K3_STAGE = 32768;
constexpr int K3_SLOTS = 4;
__global__ __launch_bounds__(512, 2) void k3_out(const u16* __restrict__ x, const u16* __restrict__ wo, const u16* __restrict__ wout, char* __restrict__ ws, u16* __restrict__ out) {
    extern __shared__ __attribute__((aligned(16))) char lds[];
    const int tid = threadIdx.x, lane = tid & 63, r32 = lane & 31, hi = lane >> 5;
    const int wid = __builtin_amdgcn_readfirstlane(tid >> 6);
    const int b = blockIdx.x, xcd = b & 7, idx = b >> 3;
    const int tt = xcd * 4 + (idx & 3), nt = idx >> 2;
    const unsigned lds0 = (unsigned)(uintptr_t)lds;
    STAMP(2, 0);
    if (wid >= 4) {
        const int l = wid - 4, th = l & 1, nh = l >> 1;
        const char* og = ws + WS_O + (size_t)tt * 2 * 32768;
        const char* hg = ws + WS_H + (size_t)tt * 2 * 57344;
        const unsigned tvoff = lane * 16;
        unsigned wvoff[4];
#pragma unroll
        for (int e = 0; e < 4; ++e) { const int row = l * 32 + e * 8 + (lane >> 3); wvoff[e] = (unsigned)(row * 1024 + nt * 128 + (((lane & 7) ^ (((row >> 1) & 1) << 2)) * 16)); }
        auto dma = [&](int st) __attribute__((always_inline)) {
            const unsigned d = lds0 + (st & 3) * K3_STAGE;
            const bool first = st < 4; const int k0 = first ? st * 128 : (st - 4) * 128;
            const char* tg = (first ? og : hg) + (size_t)(k0 >> 4) * 1024; const int tstr = first ? 32768 : 57344;
#pragma unroll
            for (int e = 0; e < 4; ++e) glds16s(tvoff, tg + (size_t)th * tstr + (nh * 4 + e) * 1024, d + th * 8192 + (nh * 4 + e) * 1024);
            const char* wg = (const char*)(first ? wo : wout) + (size_t)k0 * 1024;
#pragma unroll
            for (int e = 0; e < 4; ++e) glds16s(wvoff[e], wg, d + 16384 + l * 4096 + e * 1024);
        };
        dma(0); dma(1); dma(2); dma(3);
        WAIT_BAR(24);
#pragma unroll
        for (int st = 1; st < 11; ++st) {
            if (st < 9) WAIT_BAR(16); else if (st == 9) WAIT_BAR(8); else WAIT_BAR(0);
            if (st + 3 < 11) dma(st + 3);
        }
        return;
    }
    const int th = wid & 1, nh = wid >> 1;
    f32x16 accs = {}, accb = {};
    const int g = lane >> 4, qq = (lane & 15) >> 2, pp = lane & 3;
    const int kr = 8 * (g >> 1) + qq;
    const unsigned woff0 = (unsigned)(16384 + kr * 128 + ((nh ^ ((kr >> 1) & 1)) * 64) + (g & 1) * 32 + pp * 8);
    const unsigned woff1 = (unsigned)(16384 + (kr + 4) * 128 + ((nh ^ (((kr + 4) >> 1) & 1)) * 64) + (g & 1) * 32 + pp * 8);
    const unsigned toff = (unsigned)(th * 8192 + lane * 16);
    const int t = tt * 64 + th * 32 + r32, n0 = nt * 64 + nh * 32 + 4 * hi;
    u32x2 xv[4];
#pragma unroll
    for (int q = 0; q < 4; ++q) xv[q] = *(const u32x2*)(x + (size_t)t * D + n0 + 8 * q);
    for (int st = 0; st < 11; ++st) {
        LBAR();
        const lds_cptr sb = (lds_cptr)lds + (st & 3) * K3_STAGE;
        bf16x8 wf[8], tf[8];
#pragma unroll
        for (int ks = 0; ks < 8; ++ks) { wf[ks] = cat(ldstr(sb + woff0 + ks * 2048), ldstr(sb + woff1 + ks * 2048)); tf[ks] = ldsr128(sb + toff + ks * 1024); }
        if (st < 4) {
#pragma unroll
            for (int ks = 0; ks < 8; ++ks) accs = MFMA32(wf[ks], tf[ks], accs);
        } else {
#pragma unroll
            for (int ks = 0; ks < 8; ++ks) accb = MFMA32(wf[ks], tf[ks], accb);
        }
    }
    STAMP(2, 2);
#pragma unroll
    for (int q = 0; q < 4; ++q) {
        float v[4];
#pragma unroll
        for (int r = 0; r < 4; ++r) { const float bb = accb[4 * q + r]; const float sg = bb * __builtin_amdgcn_rcpf(1.0f + __builtin_amdgcn_exp2f(-LOG2E * bb)); v[r] = accs[4 * q + r] * sg; }
        u32x2 w = {cvtpk(bflo(xv[q][0]) + v[0], bfhi(xv[q][0]) + v[1]), cvtpk(bflo(xv[q][1]) + v[2], bfhi(xv[q][1]) + v[3])};
        *(u32x2*)(out + (size_t)t * D + n0 + 8 * q) = w;
    }
    STAMP(2, 3);
}

extern "C" void kernel_launch(void* const* d_in, const int* in_sizes, int n_in, void* d_out, int out_size, void* d_ws, size_t ws_size, hipStream_t stream) {
    const u16* x = (const u16*)d_in[0]; const u16* wqkv = (const u16*)d_in[1]; const u16* wo = (const u16*)d_in[2];
    const u16* win = (const u16*)d_in[4]; const u16* wsp = (const u16*)d_in[5]; const u16* bsp = (const u16*)d_in[6]; const u16* wout = (const u16*)d_in[7];
    char* ws = (char*)d_ws;
    (void)hipFuncSetAttribute((const void*)k1_proj, hipFuncAttributeMaxDynamicSharedMemorySize, K1_LDS);
    (void)hipFuncSetAttribute((const void*)k2_mix, hipFuncAttributeMaxDynamicSharedMemorySize, K2_LDS);
    (void)hipFuncSetAttribute((const void*)k3_out, hipFuncAttributeMaxDynamicSharedMemorySize, 4 * K3_STAGE);
    k1_proj<<<256, 640, K1_LDS, stream>>>(x, wqkv, win, ws);
    k2_mix<<<512, 512, K2_LDS, stream>>>(wsp, bsp, ws);
    k3_out<<<256, 512, 4 * K3_STAGE, stream>>>(x, wo, wout, ws, (u16*)d_out);
}
```

```cpp
#include <hip/hip_runtime.h>
#include <hip/hip_bf16.h>
#include <stdint.h>
#include <type_traits>

typedef unsigned short u16;
using bf16x8 = __attribute__((ext_vector_type(8))) short;
using s16x4 = __attribute__((ext_vector_type(4))) short;
using f32x16 = __attribute__((ext_vector_type(16))) float;
using f32x4 = __attribute__((ext_vector_type(4))) float;
using u32x4 = __attribute__((ext_vector_type(4))) unsigned;
using u32x2 = __attribute__((ext_vector_type(2))) unsigned;
typedef __bf16 bf16x2_t __attribute__((ext_vector_type(2)));
typedef float f32x2_t __attribute__((ext_vector_type(2)));
typedef short v4i16_t __attribute__((ext_vector_type(4)));
#define LDSAS __attribute__((address_space(3)))
typedef LDSAS const char* lds_cptr;
#define MFMA32(a, b, c) __builtin_amdgcn_mfma_f32_32x32x16_bf16(a, b, c, 0, 0, 0)
#define MFMA16(a, b, c) __builtin_amdgcn_mfma_f32_16x16x32_bf16(a, b, c, 0, 0, 0)
#define SBAR() __builtin_amdgcn_sched_barrier(0)
#define PIN(x) asm volatile("" : "+v"(x))
#define WAIT_BAR(N) asm volatile("s_waitcnt vmcnt(" #N ") lgkmcnt(0)\n\ts_barrier" ::: "memory")
#define LBAR() asm volatile("s_waitcnt lgkmcnt(0)\n\ts_barrier" ::: "memory")
#define DI __device__ __forceinline__
#define STAMP(kern, i) do { } while (0)

constexpr int S = 2048, D = 512, H = 32, DH = 16, FH = 896;
constexpr float LOG2E = 1.4426950408889634f;

constexpr size_t WS_Q = 0, WS_K = 2u << 20, WS_V = 4u << 20, WS_Z1 = 6u << 20, WS_Z2R = 10u << 20, WS_Z2N = 14u << 20, WS_H = 18u << 20, WS_O = 22u << 20, WS_KMAX = 24u << 20, WS_EPOCH = 25u << 20, WS_XCH = 26u << 20, WS_XST = 27u << 20, WS_CNT = 28u << 20;

DI unsigned cvtpk(float lo, float hi) { f32x2_t v = {lo, hi}; return __builtin_bit_cast(unsigned, __builtin_convertvector(v, bf16x2_t)); }
DI float bflo(unsigned w) { return __uint_as_float(w << 16); }
DI float bfhi(unsigned w) { return __uint_as_float(w & 0xFFFF0000u); }
DI float dot2(unsigned a, unsigned b, float c) { return __builtin_amdgcn_fdot2_f32_bf16(__builtin_bit_cast(bf16x2_t, a), __builtin_bit_cast(bf16x2_t, b), c, false); }
DI void glds16s(unsigned voff, const void* sbase, unsigned lds_base) {
    lds_base = __builtin_amdgcn_readfirstlane(lds_base);
    unsigned sv; asm volatile("s_mov_b32 %0, m0\n\ts_mov_b32 m0, %3\n\ts_nop 0\n\tglobal_load_lds_dwordx4 %1, %2\n\ts_mov_b32 m0, %0" : "=&s"(sv) : "v"(voff), "s"(sbase), "s"(lds_base) : "memory"); }
DI bf16x8 ldsr128(lds_cptr p) { return *(const LDSAS bf16x8*)p; }
DI s16x4 ldstr(lds_cptr p) { return __builtin_bit_cast(s16x4, __builtin_amdgcn_ds_read_tr16_b64_v4i16((LDSAS v4i16_t*)p)); }
DI bf16x8 cat(s16x4 a, s16x4 b) { return __builtin_shufflevector(a, b, 0, 1, 2, 3, 4, 5, 6, 7); }
DI float swap32(float x, bool sum) { auto rr = __builtin_amdgcn_permlane32_swap(__float_as_uint(x), __float_as_uint(x), false, false); return sum ? __uint_as_float(rr[0]) + __uint_as_float(rr[1]) : __uint_as_float(rr[0]); }
DI float gelu_t(float x) {
    const float c1 = -2.0f * 0.7978845608028654f * LOG2E, c3 = c1 * 0.044715f;
    const float u = x * fmaf(x * x, c3, c1);
    return x * __builtin_amdgcn_rcpf(1.0f + __builtin_amdgcn_exp2f(u));
}

constexpr int K1_XB = 16384;
constexpr int K1_STAGE = K1_XB + 7 * 4096;
constexpr int K1_CS = 3 * K1_STAGE;
constexpr int K1_LDS = K1_CS + 1024;
typedef __attribute__((address_space(1))) unsigned gu32; typedef __attribute__((address_space(1))) unsigned long long gu64;
#define RLX_AGENT __ATOMIC_RELAXED, __HIP_MEMORY_SCOPE_AGENT

DI float xor16(float x) { auto r = __builtin_amdgcn_permlane16_swap(__float_as_uint(x), __float_as_uint(x), false, false); return __uint_as_float(r[0]) + __uint_as_float(r[1]); }
DI float xor32(float x) { auto r = __builtin_amdgcn_permlane32_swap(__float_as_uint(x), __float_as_uint(x), false, false); return __uint_as_float(r[0]) + __uint_as_float(r[1]); }
DI float max16(float x) { auto r = __builtin_amdgcn_permlane16_swap(__float_as_uint(x), __float_as_uint(x), false, false); return fmaxf(__uint_as_float(r[0]), __uint_as_float(r[1])); }
DI float max32(float x) { auto r = __builtin_amdgcn_permlane32_swap(__float_as_uint(x), __float_as_uint(x), false, false); return fmaxf(__uint_as_float(r[0]), __uint_as_float(r[1])); }
template <int CTRL> DI float dppf(float x) { return __uint_as_float(__builtin_amdgcn_update_dpp(0, __float_as_uint(x), CTRL, 0xF, 0xF, false)); }
DI float rowmax16(float x) { x = fmaxf(x, dppf<0x128>(x)); x = fmaxf(x, dppf<0x124>(x)); x = fmaxf(x, dppf<0x122>(x)); x = fmaxf(x, dppf<0x121>(x)); return x; }
DI float wavesum(float x) { x += dppf<0x128>(x); x += dppf<0x124>(x); x += dppf<0x122>(x); x += dppf<0x121>(x); return xor32(xor16(x)); }
DI u32x4 widen(u32x2 a, u32x2 b) { auto r0 = __builtin_amdgcn_permlane32_swap(a[0], b[0], false, false); auto r1 = __builtin_amdgcn_permlane32_swap(a[1], b[1], false, false); return (u32x4){r0[0], r1[0], r0[1], r1[1]}; }

__global__ __launch_bounds__(768, 3) void k1_proj(const u16* __restrict__ x, const u16* __restrict__ wqkv, const u16* __restrict__ win, char* __restrict__ ws) {
    extern __shared__ __attribute__((aligned(16))) char lds[];
    const int tid = threadIdx.x, lane = tid & 63, r32 = lane & 31, hi = lane >> 5;
    const int wid = __builtin_amdgcn_readfirstlane(tid >> 6);
    const int b = blockIdx.x, xcd = b & 7, idx = b >> 3;
    const int panel = 8 * (xcd >> 2) + (idx & 7), j = 4 * (xcd & 3) + (idx >> 3);
    const unsigned lds0 = (unsigned)(uintptr_t)lds;
    const int jq = j & 3, u7 = 7 * (j >> 2);
    const int n1 = jq == 3 ? 1 : 2, n2 = jq == 1 ? 1 : 2;
    const int z1t0 = u7 + 2 * jq, z2t0 = u7 + (jq == 0 ? 0 : (jq == 1 ? 2 : (jq == 2 ? 3 : 5)));
    STAMP(0, 0);
    if (b == 0 && tid < 40) ((unsigned*)(ws + WS_CNT))[tid * 64] = 0u;
    if (wid >= 8) {
        const int l = wid - 8;
        const char* xb = (const char*)x + (size_t)(panel * 128 + l * 32) * 1024;
        const unsigned xv0 = (unsigned)((lane >> 3) * 1024 + (((lane & 7) ^ (lane >> 4)) * 16)), xv1 = (unsigned)((lane >> 3) * 1024 + (((lane & 7) ^ (4 + (lane >> 4))) * 16));
        const unsigned vq = (unsigned)((16 * l + (lane >> 2)) * 3072 + (lane & 3) * 16), vz = (unsigned)((16 * l + (lane >> 2)) * 3584 + (lane & 3) * 16);
        const char* wq = (const char*)wqkv + j * 64;
        int zc[4];
#pragma unroll
        for (int i = 0; i < 4; ++i) { const int ii = i < n1 + n2 ? i : n1 + n2 - 1; zc[i] = ii < n1 ? (z1t0 + ii) * 64 : FH * 2 + (z2t0 + ii - n1) * 64; }
        auto dma = [&](int st) __attribute__((always_inline)) {
            const unsigned d = lds0 + (st % 3) * K1_STAGE;
#pragma unroll
            for (int p = 0; p < 4; ++p) glds16s((p & 1) ? xv1 : xv0, xb + st * 128 + p * 8192, d + l * 4096 + p * 1024);
            const char* bq = wq + (size_t)st * 64 * 3072; const char* bz = (const char*)win + (size_t)st * 64 * 3584; const unsigned dw = d + K1_XB + l * 1024;
            glds16s(vq, bq + 2048, dw); glds16s(vq, bq, dw + 4096); glds16s(vq, bq + 1024, dw + 8192);
#pragma unroll
            for (int i = 0; i < 4; ++i) glds16s(vz, bz + zc[i], dw + (3 + i) * 4096);
        };
        dma(0);
        WAIT_BAR(0);
        dma(1); dma(2);
#pragma unroll
        for (int st = 1; st < 8; ++st) {
            if (st < 7) WAIT_BAR(11); else WAIT_BAR(0);
            if (st + 2 < 8) dma(st + 2);
        }
        return;
    }
    const int mw = wid & 3, nh = wid >> 2;
    gu32* epw = (gu32*)(ws + WS_EPOCH) + panel * 64;
    unsigned tag = __hip_atomic_load(epw, RLX_AGENT);
    u32x4 xr = *(const u32x4*)(x + (size_t)(panel * 128 + 8 * j + wid) * D + lane * 8);
    auto rowstats = [&]() __attribute__((always_inline)) {
        PIN(xr); PIN(tag); tag += 1u;
        float v[8];
#pragma unroll
        for (int i = 0; i < 4; ++i) { v[2 * i] = bflo(xr[i]); v[2 * i + 1] = bfhi(xr[i]); }
        const float m = wavesum(((v[0] + v[1]) + (v[2] + v[3])) + ((v[4] + v[5]) + (v[6] + v[7]))) * (1.0f / 512.0f);
        float q = 0.f;
#pragma unroll
        for (int i = 0; i < 8; ++i) q = fmaf(v[i] - m, v[i] - m, q);
        const float r = __builtin_amdgcn_rsqf(wavesum(q) * (1.0f / 512.0f) + 1e-5f);
        if (lane < 2) __hip_atomic_store((gu64*)(ws + WS_XST) + (size_t)(panel * 128 + 8 * j + wid) * 2 + lane, ((unsigned long long)tag << 32) | __float_as_uint(lane ? r : m), RLX_AGENT);
    };
    f32x16 acc[4];
#pragma unroll
    for (int s = 0; s < 4; ++s) acc[s] = (f32x16){};
    float cs0 = 0.f, cs1 = 0.f;
    const int g = lane >> 4, qq = (lane & 15) >> 2, pp = lane & 3;
    const int trow = 32 * mw + r32;
    unsigned xo[4];
#pragma unroll
    for (int ks = 0; ks < 4; ++ks) xo[ks] = (unsigned)(trow * 128 + (((2 * ks + hi) ^ ((trow >> 1) & 7)) * 16));
    const unsigned wlane = (unsigned)(K1_XB + (8 * (g >> 1) + qq) * 64 + (g & 1) * 32 + pp * 8);
    const __amdgpu_buffer_rsrc_t sr = __builtin_amdgcn_make_buffer_rsrc((void*)(ws + WS_XST + (size_t)panel * 128 * 16), 0, 128 * 16, 0x00020000);
    u32x4 pst;
    int tile[4];
    auto mainloop = [&](auto NHC) __attribute__((always_inline)) {
        constexpr int NHV = decltype(NHC)::value; constexpr int NS = NHV == 0 ? 4 : 3;
        unsigned wo[NS];
#pragma unroll
        for (int s = 0; s < NS; ++s) { int t = s + mw; t = t >= NS ? t - NS : t; t = t >= NS ? t - NS : t; tile[s] = 4 * NHV + t; wo[s] = wlane + tile[s] * 4096; }
        bf16x8 xa[2], wf[2][NS];
        auto rd = [&](const int set, const lds_cptr sb, const int ks) __attribute__((always_inline)) {
            xa[set] = ldsr128(sb + xo[ks]);
#pragma unroll
            for (int s = 0; s < NS; ++s) wf[set][s] = cat(ldstr(sb + wo[s] + ks * 1024), ldstr(sb + wo[s] + ks * 1024 + 256));
        };
        auto mm = [&](const int set) __attribute__((always_inline)) {
            SBAR();
#pragma unroll
            for (int s = 0; s < NS; ++s) acc[s] = MFMA32(wf[set][s], xa[set], acc[s]);
            const u32x4 w = __builtin_bit_cast(u32x4, wf[set][0]);
#pragma unroll
            for (int i = 0; i < 4; ++i) { cs0 += bflo(w[i]); cs1 += bfhi(w[i]); }
            PIN(cs0); PIN(cs1);
#pragma unroll
            for (int i = 0; i < NS; ++i) { __builtin_amdgcn_sched_group_barrier(0x008, 1, 0); __builtin_amdgcn_sched_group_barrier(0x002, NS == 4 ? 4 : 6, 0); }
            SBAR();
        };
        LBAR();
        STAMP(0, 1);
        rd(0, (lds_cptr)lds, 0);
#pragma unroll
        for (int st = 0; st < 8; ++st) {
            const lds_cptr sb = (lds_cptr)lds + (st % 3) * K1_STAGE;
            rd(1, sb, 1); mm(0);
            rd(0, sb, 2); mm(1);
            rd(1, sb, 3); mm(0);
            if (st < 7) { LBAR(); rd(0, (lds_cptr)lds + ((st + 1) % 3) * K1_STAGE, 0); }
            if (st == 1) rowstats();
            if (st == 5) pst = __builtin_amdgcn_raw_buffer_load_b128(sr, trow * 16, 0, 16);
            mm(1);
        }
    };
    if (nh == 0) mainloop(std::integral_constant<int, 0>{}); else mainloop(std::integral_constant<int, 1>{});
    STAMP(0, 2);
    float* csl = (float*)(lds + K1_CS);
    { const float c = xor32(cs0 + cs1); if (hi == 0) csl[tile[0] * 32 + r32] = c; }
    const int tok = panel * 128 + trow;
    float mu, rs;
    {   u32x4 p = pst;
        for (unsigned spins = 0; !__all((p[1] == tag) & (p[3] == tag)) && spins < 200000u; ++spins) {
            __builtin_amdgcn_s_sleep(1);
            p = __builtin_amdgcn_raw_buffer_load_b128(sr, trow * 16, 0, 16);
        }
        mu = __uint_as_float(p[0]); rs = __uint_as_float(p[2]); }
    LBAR();
    STAMP(0, 3);
    auto lnv = [&](int s, float (&v)[16], float sc) __attribute__((always_inline)) {
#pragma unroll
        for (int q = 0; q < 4; ++q) { const f32x4 c = *(const f32x4*)(csl + tile[s] * 32 + 8 * q + 4 * hi);
#pragma unroll
            for (int r = 0; r < 4; ++r) v[4 * q + r] = sc * (acc[s][4 * q + r] - mu * c[r]); } };
    u16* Z1 = (u16*)(ws + WS_Z1);
    auto z1store = [&](int zt, const u32x2 (&w)[4]) __attribute__((always_inline)) {
        u16* p = Z1 + ((size_t)((tok >> 5) * 28 + zt) * 1024) + (hi * 32 + r32) * 4;
#pragma unroll
        for (int q = 0; q < 4; ++q) *(u32x2*)(p + q * 256) = w[q]; };
    auto gelu16 = [&](int s, u32x2 (&w)[4]) __attribute__((always_inline)) { float v[16]; lnv(s, v, rs);
#pragma unroll
        for (int q = 0; q < 4; ++q) w[q] = (u32x2){cvtpk(gelu_t(v[4 * q]), gelu_t(v[4 * q + 1])), cvtpk(gelu_t(v[4 * q + 2]), gelu_t(v[4 * q + 3]))}; };
    if (nh == 0) {
#pragma unroll
        for (int s = 0; s < 4; ++s) {
            const int T = tile[s];
            if (T == 3) { u32x2 w[4]; gelu16(s, w); z1store(z1t0, w); }
            else {
                float v[16]; lnv(s, v, T == 1 ? rs * (0.25f * LOG2E) : rs);
                u32x2 w[4];
#pragma unroll
                for (int q = 0; q < 4; ++q) w[q] = (u32x2){cvtpk(v[4 * q], v[4 * q + 1]), cvtpk(v[4 * q + 2], v[4 * q + 3])};
                if (T == 0) {
                    char* sc = lds + mw * 2048;
#pragma unroll
                    for (int q = 0; q < 4; ++q) *(u32x2*)(sc + r32 * 64 + (8 * q + 4 * hi) * 2) = w[q];
                    asm volatile("s_waitcnt lgkmcnt(0)" ::: "memory");
                    u16* Vt = (u16*)(ws + WS_V);
#pragma unroll
                    for (int hh = 0; hh < 2; ++hh)
#pragma unroll
                        for (int ks = 0; ks < 2; ++ks) {
                            const s16x4 t4 = ldstr((lds_cptr)lds + mw * 2048 + (16 * ks + 8 * (g >> 1) + 4 * (g & 1) + qq) * 64 + hh * 32 + pp * 8);
                            *(s16x4*)(Vt + ((size_t)(2 * j + hh) * 128 + ((panel * 128 + 32 * mw) >> 4) + ks) * 256 + (g & 1) * 128 + (lane & 15) * 8 + (g >> 1) * 4) = t4; }
                } else {
                    u16* dst = (u16*)(ws + (T == 1 ? WS_Q : WS_K));
#pragma unroll
                    for (int hh = 0; hh < 2; ++hh) *(u32x4*)(dst + ((size_t)(2 * j + hh) * S + tok) * 16 + 8 * hi) = widen(w[2 * hh], w[2 * hh + 1]);
                    if (T == 2) {
                        float* kmax = (float*)(ws + WS_KMAX);
#pragma unroll
                        for (int hh = 0; hh < 2; ++hh) { float n = 0.f;
#pragma unroll
                            for (int q = 0; q < 2; ++q) { const u32x2 ww = w[2 * hh + q]; n = fmaf(bflo(ww[0]), bflo(ww[0]), n); n = fmaf(bfhi(ww[0]), bfhi(ww[0]), n); n = fmaf(bflo(ww[1]), bflo(ww[1]), n); n = fmaf(bfhi(ww[1]), bfhi(ww[1]), n); }
                            n = max16(rowmax16(xor32(n)));
                            if (lane == 0) kmax[(panel * 4 + mw) * H + 2 * j + hh] = n; }
                    }
                }
            }
        }
    } else {
        __builtin_amdgcn_s_setprio(2);
        float sm = 0.f;
#pragma unroll
        for (int s = 0; s < 3; ++s) { const int zi = tile[s] - 3;
            if (zi >= n1 && zi < n1 + n2) { float v[16]; lnv(s, v, rs);
#pragma unroll
                for (int r = 0; r < 16; ++r) { acc[s][r] = gelu_t(v[r]); sm += acc[s][r]; } } }
        sm = xor32(sm);
        const float ml = sm * (n2 == 2 ? (1.0f / 64.f) : (1.0f / 32.f)); float m2 = 0.f;
#pragma unroll
        for (int s = 0; s < 3; ++s) { const int zi = tile[s] - 3;
            if (zi >= n1 && zi < n1 + n2) {
#pragma unroll
                for (int r = 0; r < 16; ++r) { const float d = acc[s][r] - ml; m2 = fmaf(d, d, m2); } } }
        m2 = xor32(m2);
        __hip_atomic_store((gu64*)(ws + WS_XCH) + ((size_t)(panel * 16 + j) * 128 + trow) * 2 + hi, ((unsigned long long)tag << 32) | __float_as_uint(hi ? m2 : sm), RLX_AGENT);
        __builtin_amdgcn_s_setprio(0);
        STAMP(0, 4);
#pragma unroll
        for (int s = 0; s < 3; ++s) { const int zi = tile[s] - 3; if (zi < n1) { u32x2 w[4]; gelu16(s, w); z1store(z1t0 + zi, w); } }
        float mean, rstd;
        {
            const __amdgpu_buffer_rsrc_t xr = __builtin_amdgcn_make_buffer_rsrc((void*)(ws + WS_XCH + (size_t)panel * 16 * 128 * 16), 0, 16 * 128 * 16, 0x00020000);
            u32x4 p[8];
            for (unsigned spins = 0;; ++spins) {
                bool ok = true;
#pragma unroll
                for (int u = 0; u < 8; ++u) p[u] = __builtin_amdgcn_raw_buffer_load_b128(xr, ((8 * hi + u) * 128 + trow) * 16, 0, 16);
#pragma unroll
                for (int u = 0; u < 8; ++u) ok &= (p[u][1] == tag) & (p[u][3] == tag);
                if (__all(ok) || spins > 200000u) break;
                __builtin_amdgcn_s_sleep(1);
            }
            float tot = 0.f;
#pragma unroll
            for (int u = 0; u < 8; ++u) tot += __uint_as_float(p[u][0]);
            mean = xor32(tot) * (1.0f / FH);
            float mm2 = 0.f;
#pragma unroll
            for (int u = 0; u < 8; ++u) { const float nl = (u & 3) == 1 ? 32.f : 64.f; const float d = __uint_as_float(p[u][0]) * ((u & 3) == 1 ? (1.0f / 32.f) : (1.0f / 64.f)) - mean; mm2 += __uint_as_float(p[u][2]) + nl * d * d; }
            rstd = __builtin_amdgcn_rsqf(xor32(mm2) * (1.0f / FH) + 1e-5f);
        }
        STAMP(0, 5);
        if (wid == 4 && lane == 0) __hip_atomic_store(epw, tag, RLX_AGENT);
        u16* Z2N = (u16*)(ws + WS_Z2N); const float nmr = -mean * rstd;
#pragma unroll
        for (int s = 0; s < 3; ++s) { const int zi = tile[s] - 3;
            if (zi >= n1 && zi < n1 + n2) {
                const int ft = z2t0 + zi - n1;
                u32x2 w[4];
#pragma unroll
                for (int q = 0; q < 4; ++q) w[q] = (u32x2){cvtpk(fmaf(acc[s][4 * q], rstd, nmr), fmaf(acc[s][4 * q + 1], rstd, nmr)), cvtpk(fmaf(acc[s][4 * q + 2], rstd, nmr), fmaf(acc[s][4 * q + 3], rstd, nmr))};
                u16* p = Z2N + ((size_t)(((ft >> 2) * 32 + (tok >> 6)) * 4 + (ft & 3)) * 64 + (tok & 63)) * 32 + 8 * hi;
                *(u32x4*)p = widen(w[0], w[1]); *(u32x4*)(p + 16) = widen(w[2], w[3]);
            } }
    }
    STAMP(0, 6);
}

constexpr int A_KR = 0, A_VR = 32768, A_CONST = 65536, A_FLAG = A_CONST + 4096, A_MX = A_FLAG + 64, K2_LDS = 81920;
constexpr float SAFE_T = 48.f;
#define EX(x) __builtin_amdgcn_exp2f(x)
#define BF(p) __builtin_bit_cast(bf16x8, p)
DI void st8_wt(void* p, u32x2 w) { __hip_atomic_store((gu64*)p, ((unsigned long long)w[1] << 32) | w[0], RLX_AGENT); }

DI void attn_role(int b, char* __restrict__ ws, char* lds) {
    const u16* Qs = (const u16*)(ws + WS_Q); const u16* Kb = (const u16*)(ws + WS_K); const u16* Vt = (const u16*)(ws + WS_V); const float* kmax = (const float*)(ws + WS_KMAX); u16* Ot = (u16*)(ws + WS_O);
    const int tid = threadIdx.x, lane = tid & 63, r32 = lane & 31, hi = lane >> 5;
    const int wid = __builtin_amdgcn_readfirstlane(tid >> 6);
    const int xcd = b & 7, idx = b >> 3, head = xcd * 4 + (idx & 3), qt = idx >> 2;
    const int qp = wid & 3, kh = wid >> 2;
    const unsigned lds0 = (unsigned)(uintptr_t)lds;
    const char* kg = (const char*)(Kb + (size_t)head * S * DH);
    const char* vg = (const char*)(Vt + (size_t)head * S * DH);
    const unsigned kvoff = wid * 1024 + ((lane ^ ((lane >> 5) & 1)) * 16);
    const unsigned vvoff = wid * 1024 + lane * 16;
#define DMA_CHUNK(c) do { glds16s(kvoff, kg + (c) * 8192, lds0 + A_KR + ((c) & 3) * 8192 + wid * 1024); glds16s(vvoff, vg + (c) * 8192, lds0 + A_VR + ((c) & 3) * 8192 + wid * 1024); } while (0)
    STAMP(1, 0);
    const int q0 = qt * 256 + qp * 64;
    const bf16x8 qa = *(const bf16x8*)(Qs + ((size_t)head * S + q0 + r32) * DH + hi * 8);
    const bf16x8 qb = *(const bf16x8*)(Qs + ((size_t)head * S + q0 + 32 + r32) * DH + hi * 8);
    float km = kmax[lane * H + head];
    DMA_CHUNK(0);
    if (tid < 256) { const unsigned w = (tid & 31) ? 0u : 0x3F803F80u; u32x4 v = {w, w, w, w}; *(u32x4*)(lds + A_CONST + tid * 16) = v; }
    km = max32(max16(rowmax16(km)));
    float qna = 0.f, qnb = 0.f;
#pragma unroll
    for (int j = 0; j < 8; ++j) { const float va = bflo((u16)qa[j]), vb = bflo((u16)qb[j]); qna = fmaf(va, va, qna); qnb = fmaf(vb, vb, qnb); }
    qna = xor32(qna); qnb = xor32(qnb);
    const float mh = __builtin_amdgcn_sqrtf(fmaxf(qna, qnb) * km) * 1.001f;
    { const bool unsafe = __any(!(mh <= SAFE_T)); if (lane == 0) ((unsigned*)(lds + A_FLAG))[wid] = unsafe ? 1u : 0u; }
    const lds_cptr kp0 = (lds_cptr)lds + A_KR + kh * 4096 + r32 * 32 + ((hi ^ (r32 >> 4)) * 16);
    const bool vreal = r32 < 16;
    const unsigned vlane = vreal ? (unsigned)(A_VR + kh * 4096 + hi * 256 + r32 * 16) : (unsigned)(A_CONST + (r32 == 16 ? 0 : ((r32 >= 20 && r32 < 28) ? 64 : 16)));
    const unsigned vmask = vreal ? 0xFFFFFFFFu : 0u;
    f32x16 oa = {}, ob = {};
    WAIT_BAR(0);
    bool slow;
    { const u32x4 f0 = *(const u32x4*)(lds + A_FLAG), f1 = *(const u32x4*)(lds + A_FLAG + 16); slow = __builtin_amdgcn_readfirstlane(f0[0] | f0[1] | f0[2] | f0[3] | f1[0] | f1[1] | f1[2] | f1[3]) != 0; }
    if (!slow) {
        DMA_CHUNK(1); DMA_CHUNK(2); DMA_CHUNK(3);
        f32x16 cinit;
#pragma unroll
        for (int r = 0; r < 16; ++r) cinit[r] = -mh;
        PIN(cinit);
        bf16x8 kf, v0, v1; f32x16 sA, sB; u32x4 p0, p1;
        kf = ldsr128(kp0);
        { lds_cptr vp = (lds_cptr)lds + vlane; v0 = ldsr128(vp); v1 = ldsr128(vp + 512); }
        sA = MFMA32(kf, qa, cinit);
        p1 = (u32x4){0u, 0u, 0u, 0u};
#define HSTEP(SC, SN, QN, OP, OC, PRE1, PRE2, PRE3) do { \
        SN = MFMA32(kf, QN, cinit); PIN(SN); SBAR(); \
        PRE1; \
        SC[0] = EX(SC[0]); SC[1] = EX(SC[1]); SC[2] = EX(SC[2]); SC[3] = EX(SC[3]); SC[4] = EX(SC[4]); SC[5] = EX(SC[5]); PIN(SC); SBAR(); \
        OP = MFMA32(v1, BF(p1), OP); PIN(OP); SBAR(); \
        PRE2; \
        SC[6] = EX(SC[6]); SC[7] = EX(SC[7]); \
        p0[0] = cvtpk(SC[0], SC[1]); p0[1] = cvtpk(SC[2], SC[3]); p0[2] = cvtpk(SC[4], SC[5]); p0[3] = cvtpk(SC[6], SC[7]); PIN(p0); SBAR(); \
        SC[8] = EX(SC[8]); SC[9] = EX(SC[9]); SC[10] = EX(SC[10]); PIN(SC); SBAR(); \
        OC = MFMA32(v0, BF(p0), OC); PIN(OC); SBAR(); \
        PRE3; \
        SC[11] = EX(SC[11]); SC[12] = EX(SC[12]); SC[13] = EX(SC[13]); SC[14] = EX(SC[14]); SC[15] = EX(SC[15]); \
        p1[0] = cvtpk(SC[8], SC[9]); p1[1] = cvtpk(SC[10], SC[11]); p1[2] = cvtpk(SC[12], SC[13]); p1[3] = cvtpk(SC[14], SC[15]); PIN(p1); SBAR(); \
    } while (0)
#define BLOCK(kn, vc, vco, vn, vno) do { \
        HSTEP(sA, sB, qb, ob, oa, kf = ldsr128(kn), v1 = ldsr128((vc) + (vco) + 512), (void)0); \
        HSTEP(sB, sA, qa, oa, ob, (void)0, (void)0, v0 = ldsr128((vn) + (vno))); \
    } while (0)
        STAMP(1, 1);
        for (int c = 0; c < 8; ++c) {
            const unsigned ro = (unsigned)((c & 3) * 8192), rn = (unsigned)(((c + 1) & 3) * 8192);
            const lds_cptr vp = (lds_cptr)lds + (vlane + (ro & vmask)), vpn = (lds_cptr)lds + (vlane + (rn & vmask));
            const lds_cptr kp = kp0 + ro, kpn = kp0 + rn;
            BLOCK(kp + 1024, vp, 0, vp, 1024);
            BLOCK(kp + 2048, vp, 1024, vp, 2048);
            BLOCK(kp + 3072, vp, 2048, vp, 3072);
            if (c == 0) WAIT_BAR(4); else if (c < 6) WAIT_BAR(2); else if (c == 6) WAIT_BAR(0);
            if (c >= 1 && c + 3 < 8) DMA_CHUNK(c + 3);
            BLOCK(kpn, vp, 3072, vpn, 0);
        }
        ob = MFMA32(v1, BF(p1), ob);
#undef HSTEP
#undef BLOCK
    } else {
        float ma = -3.0e38f, mb = -3.0e38f;
        for (int pass = 0; pass < 2; ++pass) {
            for (int c = 0; c < 8; ++c) {
                LBAR();
                DMA_CHUNK(c);
                WAIT_BAR(0);
                const unsigned ro = (unsigned)((c & 3) * 8192);
                const lds_cptr vp = (lds_cptr)lds + (vlane + (ro & vmask)), kp = kp0 + ro;
                for (int j = 0; j < 4; ++j) {
                    const bf16x8 kf = ldsr128(kp + j * 1024);
                    const bf16x8 v0 = ldsr128(vp + j * 1024), v1 = ldsr128(vp + j * 1024 + 512);
#pragma unroll
                    for (int u = 0; u < 2; ++u) {
                        const f32x16 sc = MFMA32(kf, u ? qb : qa, (f32x16){});
                        float& m = u ? mb : ma; f32x16& o = u ? ob : oa;
                        if (pass == 0) {
#pragma unroll
                            for (int r = 0; r < 16; ++r) m = fmaxf(m, sc[r]);
                        } else {
                            u32x4 p0, p1;
#pragma unroll
                            for (int i = 0; i < 4; ++i) { p0[i] = cvtpk(EX(sc[2 * i] - m), EX(sc[2 * i + 1] - m)); p1[i] = cvtpk(EX(sc[8 + 2 * i] - m), EX(sc[9 + 2 * i] - m)); }
                            o = MFMA32(v0, BF(p0), o); o = MFMA32(v1, BF(p1), o);
                        }
                        SBAR();
                    }
                }
            }
            if (pass == 0) {
                ma = max32(ma); mb = max32(mb);
                float* mx = (float*)(lds + A_MX);
                if (hi == 0) { mx[(wid * 2) * 32 + r32] = ma; mx[(wid * 2 + 1) * 32 + r32] = mb; }
                LBAR();
                ma = fmaxf(ma, mx[((wid ^ 4) * 2) * 32 + r32]); mb = fmaxf(mb, mx[((wid ^ 4) * 2 + 1) * 32 + r32]);
            }
        }
    }
    STAMP(1, 2);
    __syncthreads();
    int tz = threadIdx.x; asm volatile("" : "+v"(tz));
    const int lz = tz & 63;
    float* red = (float*)lds;
    { float* dst = red + (size_t)(wid ^ 4) * 9 * 64 + lz;
#pragma unroll
      for (int r = 0; r < 9; ++r) dst[r * 64] = kh ? oa[r] : ob[r]; }
    __syncthreads();
    float o[9];
    { const float* src = red + (size_t)wid * 9 * 64 + lz;
#pragma unroll
      for (int r = 0; r < 9; ++r) o[r] = (kh ? ob[r] : oa[r]) + src[r * 64]; }
    const float rl = __builtin_amdgcn_rcpf(swap32(o[8], false));
    u16* op = Ot + ((size_t)(((q0 >> 5) + kh) * H + head) * 512) + (lz & 31) * 8 + (lz >> 5) * 4;
    st8_wt(op, (u32x2){cvtpk(o[0] * rl, o[1] * rl), cvtpk(o[2] * rl, o[3] * rl)});
    st8_wt(op + 256, (u32x2){cvtpk(o[4] * rl, o[5] * rl), cvtpk(o[6] * rl, o[7] * rl)});
    asm volatile("s_waitcnt vmcnt(0)\n\ts_barrier" ::: "memory");
    if (tz == 0) __hip_atomic_fetch_add((gu32*)(ws + WS_CNT) + (32 + qt) * 64, 1u, RLX_AGENT);
    STAMP(1, 3);
#undef DMA_CHUNK
}

DI void wait_cnt(gu32* cnt, unsigned want) {
    for (unsigned spins = 0; spins < 400000u; ++spins) {
        if ((unsigned)__builtin_amdgcn_readfirstlane(__hip_atomic_load(cnt, RLX_AGENT)) >= want) break;
        __builtin_amdgcn_s_sleep(2);
    }
}

constexpr int G_STAGE = 24576;
constexpr int W_STAGE = 16384;
constexpr int G_RED = 4 * W_STAGE;
DI void gate_role(int b, const u16* __restrict__ x, const u16* __restrict__ wo, const u16* __restrict__ wsp, const u16* __restrict__ bsp, const u16* __restrict__ wout, char* __restrict__ ws, u16* __restrict__ out, char* lds) {
    const int xcd = b & 7, idx = b >> 3;
    const int tt = xcd * 4 + (idx & 3), ct = idx >> 2;
    const int tid = threadIdx.x, lane = tid & 63, r32 = lane & 31, hi = lane >> 5;
    const int wid = __builtin_amdgcn_readfirstlane(tid >> 6);
    const unsigned lds0 = (unsigned)(uintptr_t)lds;
    gu32* hcnt = (gu32*)(ws + WS_CNT) + tt * 64; gu32* ocnt = (gu32*)(ws + WS_CNT) + (32 + (tt >> 2)) * 64;
    STAMP(1, 0);
    if (wid >= 4) {
        const int l = wid - 4;
        if (ct < 7) {
            const char* ag = ws + WS_Z2N + (size_t)ct * 32 * 16384 + l * 4096;
            const char* bg = (const char*)wsp + (size_t)(tt * 64 + l * 16) * 4096;
            const unsigned avoff = lane * 16;
            const unsigned bv0 = (unsigned)((lane >> 3) * 4096 + (((lane & 7) ^ (lane >> 4)) * 16)), bv1 = (unsigned)((8 + (lane >> 3)) * 4096 + (((lane & 7) ^ (4 + (lane >> 4))) * 16));
            auto dma = [&](int kt, int slot) __attribute__((always_inline)) { const unsigned d = lds0 + slot * G_STAGE; const char* a_ = ag + (size_t)kt * 16384;
#pragma unroll
                for (int e = 0; e < 4; ++e) glds16s(avoff, a_ + e * 1024, d + l * 4096 + e * 1024);
                glds16s(bv0, bg + kt * 128, d + 16384 + l * 2048); glds16s(bv1, bg + kt * 128, d + 16384 + l * 2048 + 1024); };
            dma(0, 0);
            WAIT_BAR(0);
            dma(1, 1); dma(2, 2);
            int slot = 0;
            for (int kt = 1; kt < 32; ++kt) {
                if (kt < 31) WAIT_BAR(6); else WAIT_BAR(0);
                if (kt + 2 < 32) dma(kt + 2, slot);
                slot = slot == 2 ? 0 : slot + 1;
            }
            LBAR();
        }
        unsigned wv[4];
#pragma unroll
        for (int e = 0; e < 4; ++e) { const int row = l * 32 + e * 8 + (lane >> 3); wv[e] = (unsigned)(row * 1024 + ct * 128 + (((lane & 7) ^ (((row >> 1) & 1) << 2)) * 16)); }
        auto dmw = [&](const u16* w, int st) __attribute__((always_inline)) { const char* wg = (const char*)w + (size_t)st * 128 * 1024; const unsigned d = lds0 + (st & 3) * W_STAGE + l * 4096;
#pragma unroll
            for (int e = 0; e < 4; ++e) glds16s(wv[e], wg, d + e * 1024); };
        dmw(wout, 0); dmw(wout, 1); dmw(wout, 2); dmw(wout, 3);
        if (l == 0) wait_cnt(hcnt, 7);
        WAIT_BAR(12);
#pragma unroll
        for (int st = 1; st < 7; ++st) {
            if (st < 4) WAIT_BAR(8); else if (st == 4) WAIT_BAR(8); else if (st == 5) WAIT_BAR(4); else WAIT_BAR(0);
            if (st + 3 < 7) dmw(wout, st + 3);
        }
        LBAR();
        dmw(wo, 0); dmw(wo, 1); dmw(wo, 2); dmw(wo, 3);
        if (l == 0) wait_cnt(ocnt, 32);
        WAIT_BAR(0);
        return;
    }
#define GPRIO 1
    __builtin_amdgcn_s_setprio(GPRIO);
    const int g = lane >> 4, qq = (lane & 15) >> 2, pp = lane & 3;
    const int th = wid & 1, fh = wid >> 1;
    const int trow = 32 * th + r32, t = tt * 64 + trow;
    if (ct < 7) {
        f32x16 acc0 = {}, acc1 = {};
        const unsigned aoff = (unsigned)(fh * 8192 + (8 * (g >> 1) + qq) * 64 + (g & 1) * 32 + pp * 8);
        unsigned boff[4];
#pragma unroll
        for (int ks = 0; ks < 4; ++ks) boff[ks] = (unsigned)(16384 + trow * 128 + (((2 * ks + hi) ^ ((trow >> 1) & 7)) * 16));
        bf16x8 fa0[2], fa1[2], fb[2];
        auto rd = [&](int set, lds_cptr sb, int ks) __attribute__((always_inline)) {
            fb[set] = ldsr128(sb + boff[ks]);
            fa0[set] = cat(ldstr(sb + aoff + ks * 1024), ldstr(sb + aoff + ks * 1024 + 256));
            fa1[set] = cat(ldstr(sb + aoff + 4096 + ks * 1024), ldstr(sb + aoff + 4096 + ks * 1024 + 256)); };
        auto mm = [&](int set) __attribute__((always_inline)) { SBAR(); acc0 = MFMA32(fa0[set], fb[set], acc0); acc1 = MFMA32(fa1[set], fb[set], acc1); SBAR(); };
        const float bias = bflo(bsp[t]);
        u32x2 z[2][4];
#pragma unroll
        for (int i = 0; i < 2; ++i)
#pragma unroll
            for (int q = 0; q < 4; ++q) z[i][q] = *(const u32x2*)((const u16*)(ws + WS_Z1) + ((size_t)((t >> 5) * 28 + ct * 4 + 2 * fh + i) * 1024) + (hi * 32 + r32) * 4 + q * 256);
        LBAR();
        rd(0, (lds_cptr)lds, 0);
        int slot = 0;
        for (int kt = 0; kt < 32; ++kt) {
            const lds_cptr sb = (lds_cptr)lds + slot * G_STAGE;
            rd(1, sb, 1); mm(0);
            rd(0, sb, 2); mm(1);
            rd(1, sb, 3); mm(0);
            slot = slot == 2 ? 0 : slot + 1;
            if (kt < 31) { LBAR(); rd(0, (lds_cptr)lds + slot * G_STAGE, 0); }
            mm(1);
        }
        STAMP(1, 2);
#pragma unroll
        for (int i = 0; i < 2; ++i) {
            u16* hp = (u16*)(ws + WS_H) + ((size_t)((t >> 5) * 56 + (ct * 4 + 2 * fh + i) * 2) * 512) + r32 * 8 + hi * 4;
#pragma unroll
            for (int q = 0; q < 4; ++q) {
                float v[4];
#pragma unroll
                for (int r = 0; r < 4; ++r) v[r] = (i ? acc1[4 * q + r] : acc0[4 * q + r]) + bias;
                st8_wt(hp + (q >> 1) * 512 + (q & 1) * 256, (u32x2){cvtpk(v[0] * bflo(z[i][q][0]), v[1] * bfhi(z[i][q][0])), cvtpk(v[2] * bflo(z[i][q][1]), v[3] * bfhi(z[i][q][1]))});
            }
        }
        asm volatile("s_waitcnt vmcnt(0) lgkmcnt(0)\n\ts_barrier" ::: "memory");
        if (tid == 0) __hip_atomic_fetch_add(hcnt, 1u, RLX_AGENT);
    }
    STAMP(1, 3);
    const int kh = fh;
    const int kr = 8 * (g >> 1) + qq;
    unsigned wof[2][2];
#pragma unroll
    for (int n = 0; n < 2; ++n) { wof[n][0] = (unsigned)((64 * kh + kr) * 128 + ((n ^ ((kr >> 1) & 1)) * 64) + (g & 1) * 32 + pp * 8); wof[n][1] = wof[n][0] + 512; }
    auto gemm = [&](const __amdgpu_buffer_rsrc_t tr, auto NST, bool bars, f32x16& c0, f32x16& c1) __attribute__((always_inline)) {
        constexpr int N = decltype(NST)::value;
        u32x4 tf[2][4];
#pragma unroll
        for (int i = 0; i < 4; ++i) tf[0][i] = __builtin_amdgcn_raw_buffer_load_b128(tr, lane * 16 + (4 * kh + i) * 1024, 0, 16);
#pragma unroll
        for (int st = 0; st < N; ++st) {
            if (st + 1 < N) {
#pragma unroll
                for (int i = 0; i < 4; ++i) tf[(st + 1) & 1][i] = __builtin_amdgcn_raw_buffer_load_b128(tr, lane * 16 + ((st + 1) * 8 + 4 * kh + i) * 1024, 0, 16); }
            if (bars && st > 0) LBAR();
            const lds_cptr sb = (lds_cptr)lds + (st & 3) * W_STAGE;
#pragma unroll
            for (int i = 0; i < 4; ++i) {
                const bf16x8 w0 = cat(ldstr(sb + wof[0][0] + i * 2048), ldstr(sb + wof[0][1] + i * 2048)), w1 = cat(ldstr(sb + wof[1][0] + i * 2048), ldstr(sb + wof[1][1] + i * 2048));
                c0 = MFMA32(w0, BF(tf[st & 1][i]), c0); c1 = MFMA32(w1, BF(tf[st & 1][i]), c1);
            }
        }
    };
    auto merge = [&](f32x16& c0, f32x16& c1) __attribute__((always_inline)) {
        float* red = (float*)(lds + G_RED);
        float* dst = red + (size_t)(wid ^ 2) * 1024 + lane;
#pragma unroll
        for (int r = 0; r < 16; ++r) dst[r * 64] = kh ? c0[r] : c1[r];
    };
    f32x16 gb0 = {}, gb1 = {};
    LBAR();
    gemm(__builtin_amdgcn_make_buffer_rsrc((void*)(ws + WS_H + (size_t)(tt * 2 + th) * 57344), 0, 57344, 0x00020000), std::integral_constant<int, 7>{}, true, gb0, gb1);
    merge(gb0, gb1);
    LBAR();
    f32x16 gate;
    { const float* src = (const float*)(lds + G_RED) + (size_t)wid * 1024 + lane;
#pragma unroll
      for (int r = 0; r < 16; ++r) { const float bb = (kh ? gb1[r] : gb0[r]) + src[r * 64]; gate[r] = bb * __builtin_amdgcn_rcpf(1.0f + EX(-LOG2E * bb)); } }
    PIN(gate);
    u32x2 xv[4];
    { const int n0 = ct * 64 + kh * 32 + 4 * hi;
#pragma unroll
      for (int q = 0; q < 4; ++q) xv[q] = *(const u32x2*)(x + (size_t)t * D + n0 + 8 * q); }
    STAMP(1, 4);
    LBAR();
    STAMP(1, 5);
    f32x16 s0 = {}, s1 = {};
    gemm(__builtin_amdgcn_make_buffer_rsrc((void*)(ws + WS_O + (size_t)(tt * 2 + th) * 32768), 0, 32768, 0x00020000), std::integral_constant<int, 4>{}, false, s0, s1);
    merge(s0, s1);
    LBAR();
    { const float* src = (const float*)(lds + G_RED) + (size_t)wid * 1024 + lane;
      const int n0 = ct * 64 + kh * 32 + 4 * hi;
#pragma unroll
      for (int q = 0; q < 4; ++q) { float v[4];
#pragma unroll
          for (int r = 0; r < 4; ++r) v[r] = ((kh ? s1[4 * q + r] : s0[4 * q + r]) + src[(4 * q + r) * 64]) * gate[4 * q + r];
          *(u32x2*)(out + (size_t)t * D + n0 + 8 * q) = (u32x2){cvtpk(bflo(xv[q][0]) + v[0], bfhi(xv[q][0]) + v[1]), cvtpk(bflo(xv[q][1]) + v[2], bfhi(xv[q][1]) + v[3])}; } }
    STAMP(1, 6);
}

__global__ __launch_bounds__(512, 4) void k2_mix(const u16* __restrict__ x, const u16* __restrict__ wo, const u16* __restrict__ wsp, const u16* __restrict__ bsp, const u16* __restrict__ wout, char* __restrict__ ws, u16* __restrict__ out) {
    extern __shared__ __attribute__((aligned(16))) char lds[];
    if (blockIdx.x < 256) attn_role(blockIdx.x, ws, lds);
    if (blockIdx.x >= 256) gate_role(blockIdx.x - 256, x, wo, wsp, bsp, wout, ws, out, lds);
}

extern "C" void kernel_launch(void* const* d_in, const int* in_sizes, int n_in, void* d_out, int out_size, void* d_ws, size_t ws_size, hipStream_t stream) {
    const u16* x = (const u16*)d_in[0]; const u16* wqkv = (const u16*)d_in[1]; const u16* wo = (const u16*)d_in[2];
    const u16* win = (const u16*)d_in[4]; const u16* wsp = (const u16*)d_in[5]; const u16* bsp = (const u16*)d_in[6]; const u16* wout = (const u16*)d_in[7];
    char* ws = (char*)d_ws;
    (void)hipFuncSetAttribute((const void*)k1_proj, hipFuncAttributeMaxDynamicSharedMemorySize, K1_LDS);
    (void)hipFuncSetAttribute((const void*)k2_mix, hipFuncAttributeMaxDynamicSharedMemorySize, K2_LDS);
    k1_proj<<<256, 768, K1_LDS, stream>>>(x, wqkv, win, ws);
    k2_mix<<<512, 512, K2_LDS, stream>>>(x, wo, wsp, bsp, wout, ws, (u16*)d_out);
}
```

```cpp
#include <hip/hip_runtime.h>
#include <hip/hip_bf16.h>
#include <stdint.h>
#include <type_traits>

typedef unsigned short u16;
using bf16x8 = __attribute__((ext_vector_type(8))) short;
using s16x4 = __attribute__((ext_vector_type(4))) short;
using f32x16 = __attribute__((ext_vector_type(16))) float;
using f32x4 = __attribute__((ext_vector_type(4))) float;
using u32x4 = __attribute__((ext_vector_type(4))) unsigned;
using u32x2 = __attribute__((ext_vector_type(2))) unsigned;
typedef __bf16 bf16x2_t __attribute__((ext_vector_type(2)));
typedef float f32x2_t __attribute__((ext_vector_type(2)));
typedef short v4i16_t __attribute__((ext_vector_type(4)));
#define LDSAS __attribute__((address_space(3)))
typedef LDSAS const char* lds_cptr;
#define MFMA32(a, b, c) __builtin_amdgcn_mfma_f32_32x32x16_bf16(a, b, c, 0, 0, 0)
#define MFMA16(a, b, c) __builtin_amdgcn_mfma_f32_16x16x32_bf16(a, b, c, 0, 0, 0)
#define SBAR() __builtin_amdgcn_sched_barrier(0)
#define PIN(x) asm volatile("" : "+v"(x))
#define WAIT_BAR(N) asm volatile("s_waitcnt vmcnt(" #N ") lgkmcnt(0)\n\ts_barrier" ::: "memory")
#define LBAR() asm volatile("s_waitcnt lgkmcnt(0)\n\ts_barrier" ::: "memory")
#define DI __device__ __forceinline__
#define STAMP(kern, i) do { } while (0)

constexpr int S = 2048, D = 512, H = 32, DH = 16, FH = 896;
constexpr float LOG2E = 1.4426950408889634f;

constexpr size_t WS_Q = 0, WS_K = 2u << 20, WS_V = 4u << 20, WS_Z1 = 6u << 20, WS_Z2R = 10u << 20, WS_Z2N = 14u << 20, WS_H = 18u << 20, WS_O = 22u << 20, WS_KMAX = 24u << 20, WS_EPOCH = 25u << 20, WS_XCH = 26u << 20, WS_XST = 27u << 20, WS_CNT = 28u << 20;

DI unsigned cvtpk(float lo, float hi) { f32x2_t v = {lo, hi}; return __builtin_bit_cast(unsigned, __builtin_convertvector(v, bf16x2_t)); }
DI float bflo(unsigned w) { return __uint_as_float(w << 16); }
DI float bfhi(unsigned w) { return __uint_as_float(w & 0xFFFF0000u); }
DI float dot2(unsigned a, unsigned b, float c) { return __builtin_amdgcn_fdot2_f32_bf16(__builtin_bit_cast(bf16x2_t, a), __builtin_bit_cast(bf16x2_t, b), c, false); }
DI void glds16s(unsigned voff, const void* sbase, unsigned lds_base) {
    lds_base = __builtin_amdgcn_readfirstlane(lds_base);
    unsigned sv; asm volatile("s_mov_b32 %0, m0\n\ts_mov_b32 m0, %3\n\ts_nop 0\n\tglobal_load_lds_dwordx4 %1, %2\n\ts_mov_b32 m0, %0" : "=&s"(sv) : "v"(voff), "s"(sbase), "s"(lds_base) : "memory"); }
DI bf16x8 ldsr128(lds_cptr p) { return *(const LDSAS bf16x8*)p; }
DI s16x4 ldstr(lds_cptr p) { return __builtin_bit_cast(s16x4, __builtin_amdgcn_ds_read_tr16_b64_v4i16((LDSAS v4i16_t*)p)); }
DI bf16x8 cat(s16x4 a, s16x4 b) { return __builtin_shufflevector(a, b, 0, 1, 2, 3, 4, 5, 6, 7); }
DI float swap32(float x, bool sum) { auto rr = __builtin_amdgcn_permlane32_swap(__float_as_uint(x), __float_as_uint(x), false, false); return sum ? __uint_as_float(rr[0]) + __uint_as_float(rr[1]) : __uint_as_float(rr[0]); }
DI float gelu_t(float x) {
    const float c1 = -2.0f * 0.7978845608028654f * LOG2E, c3 = c1 * 0.044715f;
    const float u = x * fmaf(x * x, c3, c1);
    return x * __builtin_amdgcn_rcpf(1.0f + __builtin_amdgcn_exp2f(u));
}

constexpr int K1_XB = 16384;
constexpr int K1_STAGE = K1_XB + 7 * 4096;
constexpr int K1_CS = 3 * K1_STAGE;
constexpr int K1_LDS = K1_CS + 1024;
typedef __attribute__((address_space(1))) unsigned gu32; typedef __attribute__((address_space(1))) unsigned long long gu64;
#define RLX_AGENT __ATOMIC_RELAXED, __HIP_MEMORY_SCOPE_AGENT

DI float xor16(float x) { auto r = __builtin_amdgcn_permlane16_swap(__float_as_uint(x), __float_as_uint(x), false, false); return __uint_as_float(r[0]) + __uint_as_float(r[1]); }
DI float xor32(float x) { auto r = __builtin_amdgcn_permlane32_swap(__float_as_uint(x), __float_as_uint(x), false, false); return __uint_as_float(r[0]) + __uint_as_float(r[1]); }
DI float max16(float x) { auto r = __builtin_amdgcn_permlane16_swap(__float_as_uint(x), __float_as_uint(x), false, false); return fmaxf(__uint_as_float(r[0]), __uint_as_float(r[1])); }
DI float max32(float x) { auto r = __builtin_amdgcn_permlane32_swap(__float_as_uint(x), __float_as_uint(x), false, false); return fmaxf(__uint_as_float(r[0]), __uint_as_float(r[1])); }
template <int CTRL> DI float dppf(float x) { return __uint_as_float(__builtin_amdgcn_update_dpp(0, __float_as_uint(x), CTRL, 0xF, 0xF, false)); }
DI float rowmax16(float x) { x = fmaxf(x, dppf<0x128>(x)); x = fmaxf(x, dppf<0x124>(x)); x = fmaxf(x, dppf<0x122>(x)); x = fmaxf(x, dppf<0x121>(x)); return x; }
DI float wavesum(float x) { x += dppf<0x128>(x); x += dppf<0x124>(x); x += dppf<0x122>(x); x += dppf<0x121>(x); return xor32(xor16(x)); }
DI u32x4 widen(u32x2 a, u32x2 b) { auto r0 = __builtin_amdgcn_permlane32_swap(a[0], b[0], false, false); auto r1 = __builtin_amdgcn_permlane32_swap(a[1], b[1], false, false); return (u32x4){r0[0], r1[0], r0[1], r1[1]}; }

__global__ __launch_bounds__(768, 3) void k1_proj(const u16* __restrict__ x, const u16* __restrict__ wqkv, const u16* __restrict__ win, char* __restrict__ ws) {
    extern __shared__ __attribute__((aligned(16))) char lds[];
    const int tid = threadIdx.x, lane = tid & 63, r32 = lane & 31, hi = lane >> 5;
    const int wid = __builtin_amdgcn_readfirstlane(tid >> 6);
    const int b = blockIdx.x, xcd = b & 7, idx = b >> 3;
    const int panel = 8 * (xcd >> 2) + (idx & 7), j = 4 * (xcd & 3) + (idx >> 3);
    const unsigned lds0 = (unsigned)(uintptr_t)lds;
    const int jq = j & 3, u7 = 7 * (j >> 2);
    const int n1 = jq == 3 ? 1 : 2, n2 = jq == 1 ? 1 : 2;
    const int z1t0 = u7 + 2 * jq, z2t0 = u7 + (jq == 0 ? 0 : (jq == 1 ? 2 : (jq == 2 ? 3 : 5)));
    STAMP(0, 0);
    if (b == 0 && tid < 40) ((unsigned*)(ws + WS_CNT))[tid * 64] = 0u;
    if (wid >= 8) {
        const int l = wid - 8;
        const char* xb = (const char*)x + (size_t)(panel * 128 + l * 32) * 1024;
        const unsigned xv0 = (unsigned)((lane >> 3) * 1024 + (((lane & 7) ^ (lane >> 4)) * 16)), xv1 = (unsigned)((lane >> 3) * 1024 + (((lane & 7) ^ (4 + (lane >> 4))) * 16));
        const unsigned vq = (unsigned)((16 * l + (lane >> 2)) * 3072 + (lane & 3) * 16), vz = (unsigned)((16 * l + (lane >> 2)) * 3584 + (lane & 3) * 16);
        const char* wq = (const char*)wqkv + j * 64;
        int zc[4];
#pragma unroll
        for (int i = 0; i < 4; ++i) { const int ii = i < n1 + n2 ? i : n1 + n2 - 1; zc[i] = ii < n1 ? (z1t0 + ii) * 64 : FH * 2 + (z2t0 + ii - n1) * 64; }
        auto dma = [&](int st) __attribute__((always_inline)) {
            const unsigned d = lds0 + (st % 3) * K1_STAGE;
#pragma unroll
            for (int p = 0; p < 4; ++p) glds16s((p & 1) ? xv1 : xv0, xb + st * 128 + p * 8192, d + l * 4096 + p * 1024);
            const char* bq = wq + (size_t)st * 64 * 3072; const char* bz = (const char*)win + (size_t)st * 64 * 3584; const unsigned dw = d + K1_XB + l * 1024;
            glds16s(vq, bq + 2048, dw); glds16s(vq, bq, dw + 4096); glds16s(vq, bq + 1024, dw + 8192);
#pragma unroll
            for (int i = 0; i < 4; ++i) glds16s(vz, bz + zc[i], dw + (3 + i) * 4096);
        };
        dma(0);
        WAIT_BAR(0);
        dma(1); dma(2);
#pragma unroll
        for (int st = 1; st < 8; ++st) {
            if (st < 7) WAIT_BAR(11); else WAIT_BAR(0);
            if (st + 2 < 8) dma(st + 2);
        }
        return;
    }
    const int mw = wid & 3, nh = wid >> 2;
    gu32* epw = (gu32*)(ws + WS_EPOCH) + panel * 64;
    unsigned tag = __hip_atomic_load(epw, RLX_AGENT);
    u32x4 xr = *(const u32x4*)(x + (size_t)(panel * 128 + 8 * j + wid) * D + lane * 8);
    auto rowstats = [&]() __attribute__((always_inline)) {
        PIN(xr); PIN(tag); tag += 1u;
        float v[8];
#pragma unroll
        for (int i = 0; i < 4; ++i) { v[2 * i] = bflo(xr[i]); v[2 * i + 1] = bfhi(xr[i]); }
        const float m = wavesum(((v[0] + v[1]) + (v[2] + v[3])) + ((v[4] + v[5]) + (v[6] + v[7]))) * (1.0f / 512.0f);
        float q = 0.f;
#pragma unroll
        for (int i = 0; i < 8; ++i) q = fmaf(v[i] - m, v[i] - m, q);
        const float r = __builtin_amdgcn_rsqf(wavesum(q) * (1.0f / 512.0f) + 1e-5f);
        if (lane < 2) __hip_atomic_store((gu64*)(ws + WS_XST) + (size_t)(panel * 128 + 8 * j + wid) * 2 + lane, ((unsigned long long)tag << 32) | __float_as_uint(lane ? r : m), RLX_AGENT);
    };
    f32x16 acc[4];
#pragma unroll
    for (int s = 0; s < 4; ++s) acc[s] = (f32x16){};
    float cs0 = 0.f, cs1 = 0.f;
    const int g = lane >> 4, qq = (lane & 15) >> 2, pp = lane & 3;
    const int trow = 32 * mw + r32;
    unsigned xo[4];
#pragma unroll
    for (int ks = 0; ks < 4; ++ks) xo[ks] = (unsigned)(trow * 128 + (((2 * ks + hi) ^ ((trow >> 1) & 7)) * 16));
    const unsigned wlane = (unsigned)(K1_XB + (8 * (g >> 1) + qq) * 64 + (g & 1) * 32 + pp * 8);
    const __amdgpu_buffer_rsrc_t sr = __builtin_amdgcn_make_buffer_rsrc((void*)(ws + WS_XST + (size_t)panel * 128 * 16), 0, 128 * 16, 0x00020000);
    u32x4 pst;
    int tile[4];
    auto mainloop = [&](auto NHC) __attribute__((always_inline)) {
        constexpr int NHV = decltype(NHC)::value; constexpr int NS = NHV == 0 ? 4 : 3;
        unsigned wo[NS];
#pragma unroll
        for (int s = 0; s < NS; ++s) { int t = s + mw; t = t >= NS ? t - NS : t; t = t >= NS ? t - NS : t; tile[s] = 4 * NHV + t; wo[s] = wlane + tile[s] * 4096; }
        bf16x8 xa[2], wf[2][NS];
        auto rd = [&](const int set, const lds_cptr sb, const int ks) __attribute__((always_inline)) {
            xa[set] = ldsr128(sb + xo[ks]);
#pragma unroll
            for (int s = 0; s < NS; ++s) wf[set][s] = cat(ldstr(sb + wo[s] + ks * 1024), ldstr(sb + wo[s] + ks * 1024 + 256));
        };
        auto mm = [&](const int set) __attribute__((always_inline)) {
            SBAR();
#pragma unroll
            for (int s = 0; s < NS; ++s) acc[s] = MFMA32(wf[set][s], xa[set], acc[s]);
            const u32x4 w = __builtin_bit_cast(u32x4, wf[set][0]);
#pragma unroll
            for (int i = 0; i < 4; ++i) { cs0 += bflo(w[i]); cs1 += bfhi(w[i]); }
            PIN(cs0); PIN(cs1);
#pragma unroll
            for (int i = 0; i < NS; ++i) { __builtin_amdgcn_sched_group_barrier(0x008, 1, 0); __builtin_amdgcn_sched_group_barrier(0x002, NS == 4 ? 4 : 6, 0); }
            SBAR();
        };
        LBAR();
        STAMP(0, 1);
        rd(0, (lds_cptr)lds, 0);
#pragma unroll
        for (int st = 0; st < 8; ++st) {
            const lds_cptr sb = (lds_cptr)lds + (st % 3) * K1_STAGE;
            rd(1, sb, 1); mm(0);
            rd(0, sb, 2); mm(1);
            rd(1, sb, 3); mm(0);
            if (st < 7) { LBAR(); rd(0, (lds_cptr)lds + ((st + 1) % 3) * K1_STAGE, 0); }
            if (st == 1) rowstats();
            if (st == 5) pst = __builtin_amdgcn_raw_buffer_load_b128(sr, trow * 16, 0, 16);
            mm(1);
        }
    };
    if (nh == 0) mainloop(std::integral_constant<int, 0>{}); else mainloop(std::integral_constant<int, 1>{});
    STAMP(0, 2);
    float* csl = (float*)(lds + K1_CS);
    { const float c = xor32(cs0 + cs1); if (hi == 0) csl[tile[0] * 32 + r32] = c; }
    const int tok = panel * 128 + trow;
    float mu, rs;
    {   u32x4 p = pst;
        for (unsigned spins = 0; !__all((p[1] == tag) & (p[3] == tag)) && spins < 200000u; ++spins) {
            __builtin_amdgcn_s_sleep(1);
            p = __builtin_amdgcn_raw_buffer_load_b128(sr, trow * 16, 0, 16);
        }
        mu = __uint_as_float(p[0]); rs = __uint_as_float(p[2]); }
    LBAR();
    STAMP(0, 3);
    auto lnv = [&](int s, float (&v)[16], float sc) __attribute__((always_inline)) {
#pragma unroll
        for (int q = 0; q < 4; ++q) { const f32x4 c = *(const f32x4*)(csl + tile[s] * 32 + 8 * q + 4 * hi);
#pragma unroll
            for (int r = 0; r < 4; ++r) v[4 * q + r] = sc * (acc[s][4 * q + r] - mu * c[r]); } };
    u16* Z1 = (u16*)(ws + WS_Z1);
    auto z1store = [&](int zt, const u32x2 (&w)[4]) __attribute__((always_inline)) {
        u16* p = Z1 + ((size_t)((tok >> 5) * 28 + zt) * 1024) + (hi * 32 + r32) * 4;
#pragma unroll
        for (int q = 0; q < 4; ++q) *(u32x2*)(p + q * 256) = w[q]; };
    auto gelu16 = [&](int s, u32x2 (&w)[4]) __attribute__((always_inline)) { float v[16]; lnv(s, v, rs);
#pragma unroll
        for (int q = 0; q < 4; ++q) w[q] = (u32x2){cvtpk(gelu_t(v[4 * q]), gelu_t(v[4 * q + 1])), cvtpk(gelu_t(v[4 * q + 2]), gelu_t(v[4 * q + 3]))}; };
    if (nh == 0) {
#pragma unroll
        for (int s = 0; s < 4; ++s) {
            const int T = tile[s];
            if (T == 3) { u32x2 w[4]; gelu16(s, w); z1store(z1t0, w); }
            else {
                float v[16]; lnv(s, v, T == 1 ? rs * (0.25f * LOG2E) : rs);
                u32x2 w[4];
#pragma unroll
                for (int q = 0; q < 4; ++q) w[q] = (u32x2){cvtpk(v[4 * q], v[4 * q + 1]), cvtpk(v[4 * q + 2], v[4 * q + 3])};
                if (T == 0) {
                    char* sc = lds + mw * 2048;
#pragma unroll
                    for (int q = 0; q < 4; ++q) *(u32x2*)(sc + r32 * 64 + (8 * q + 4 * hi) * 2) = w[q];
                    asm volatile("s_waitcnt lgkmcnt(0)" ::: "memory");
                    u16* Vt = (u16*)(ws + WS_V);
#pragma unroll
                    for (int hh = 0; hh < 2; ++hh)
#pragma unroll
                        for (int ks = 0; ks < 2; ++ks) {
                            const s16x4 t4 = ldstr((lds_cptr)lds + mw * 2048 + (16 * ks + 8 * (g >> 1) + 4 * (g & 1) + qq) * 64 + hh * 32 + pp * 8);
                            *(s16x4*)(Vt + ((size_t)(2 * j + hh) * 128 + ((panel * 128 + 32 * mw) >> 4) + ks) * 256 + (g & 1) * 128 + (lane & 15) * 8 + (g >> 1) * 4) = t4; }
                } else {
                    u16* dst = (u16*)(ws + (T == 1 ? WS_Q : WS_K));
#pragma unroll
                    for (int hh = 0; hh < 2; ++hh) *(u32x4*)(dst + ((size_t)(2 * j + hh) * S + tok) * 16 + 8 * hi) = widen(w[2 * hh], w[2 * hh + 1]);
                    if (T == 2) {
                        float* kmax = (float*)(ws + WS_KMAX);
#pragma unroll
                        for (int hh = 0; hh < 2; ++hh) { float n = 0.f;
#pragma unroll
                            for (int q = 0; q < 2; ++q) { const u32x2 ww = w[2 * hh + q]; n = fmaf(bflo(ww[0]), bflo(ww[0]), n); n = fmaf(bfhi(ww[0]), bfhi(ww[0]), n); n = fmaf(bflo(ww[1]), bflo(ww[1]), n); n = fmaf(bfhi(ww[1]), bfhi(ww[1]), n); }
                            n = max16(rowmax16(xor32(n)));
                            if (lane == 0) kmax[(panel * 4 + mw) * H + 2 * j + hh] = n; }
                    }
                }
            }
        }
    } else {
        __builtin_amdgcn_s_setprio(2);
        float sm = 0.f;
#pragma unroll
        for (int s = 0; s < 3; ++s) { const int zi = tile[s] - 3;
            if (zi >= n1 && zi < n1 + n2) { float v[16]; lnv(s, v, rs);
#pragma unroll
                for (int r = 0; r < 16; ++r) { acc[s][r] = gelu_t(v[r]); sm += acc[s][r]; } } }
        sm = xor32(sm);
        const float ml = sm * (n2 == 2 ? (1.0f / 64.f) : (1.0f / 32.f)); float m2 = 0.f;
#pragma unroll
        for (int s = 0; s < 3; ++s) { const int zi = tile[s] - 3;
            if (zi >= n1 && zi < n1 + n2) {
#pragma unroll
                for (int r = 0; r < 16; ++r) { const float d = acc[s][r] - ml; m2 = fmaf(d, d, m2); } } }
        m2 = xor32(m2);
        __hip_atomic_store((gu64*)(ws + WS_XCH) + ((size_t)(panel * 16 + j) * 128 + trow) * 2 + hi, ((unsigned long long)tag << 32) | __float_as_uint(hi ? m2 : sm), RLX_AGENT);
        __builtin_amdgcn_s_setprio(0);
        STAMP(0, 4);
#pragma unroll
        for (int s = 0; s < 3; ++s) { const int zi = tile[s] - 3; if (zi < n1) { u32x2 w[4]; gelu16(s, w); z1store(z1t0 + zi, w); } }
        float mean, rstd;
        {
            const __amdgpu_buffer_rsrc_t xr = __builtin_amdgcn_make_buffer_rsrc((void*)(ws + WS_XCH + (size_t)panel * 16 * 128 * 16), 0, 16 * 128 * 16, 0x00020000);
            u32x4 p[8];
            for (unsigned spins = 0;; ++spins) {
                bool ok = true;
#pragma unroll
                for (int u = 0; u < 8; ++u) p[u] = __builtin_amdgcn_raw_buffer_load_b128(xr, ((8 * hi + u) * 128 + trow) * 16, 0, 16);
#pragma unroll
                for (int u = 0; u < 8; ++u) ok &= (p[u][1] == tag) & (p[u][3] == tag);
                if (__all(ok) || spins > 200000u) break;
                __builtin_amdgcn_s_sleep(1);
            }
            float tot = 0.f;
#pragma unroll
            for (int u = 0; u < 8; ++u) tot += __uint_as_float(p[u][0]);
            mean = xor32(tot) * (1.0f / FH);
            float mm2 = 0.f;
#pragma unroll
            for (int u = 0; u < 8; ++u) { const float nl = (u & 3) == 1 ? 32.f : 64.f; const float d = __uint_as_float(p[u][0]) * ((u & 3) == 1 ? (1.0f / 32.f) : (1.0f / 64.f)) - mean; mm2 += __uint_as_float(p[u][2]) + nl * d * d; }
            rstd = __builtin_amdgcn_rsqf(xor32(mm2) * (1.0f / FH) + 1e-5f);
        }
        STAMP(0, 5);
        if (wid == 4 && lane == 0) __hip_atomic_store(epw, tag, RLX_AGENT);
        u16* Z2N = (u16*)(ws + WS_Z2N); const float nmr = -mean * rstd;
#pragma unroll
        for (int s = 0; s < 3; ++s) { const int zi = tile[s] - 3;
            if (zi >= n1 && zi < n1 + n2) {
                const int ft = z2t0 + zi - n1;
                u32x2 w[4];
#pragma unroll
                for (int q = 0; q < 4; ++q) w[q] = (u32x2){cvtpk(fmaf(acc[s][4 * q], rstd, nmr), fmaf(acc[s][4 * q + 1], rstd, nmr)), cvtpk(fmaf(acc[s][4 * q + 2], rstd, nmr), fmaf(acc[s][4 * q + 3], rstd, nmr))};
                u16* p = Z2N + ((size_t)(((ft >> 2) * 32 + (tok >> 6)) * 4 + (ft & 3)) * 64 + (tok & 63)) * 32 + 8 * hi;
                *(u32x4*)p = widen(w[0], w[1]); *(u32x4*)(p + 16) = widen(w[2], w[3]);
            } }
    }
    STAMP(0, 6);
}

constexpr int A_KR = 0, A_VR = 32768, A_CONST = 65536, A_FLAG = A_CONST + 4096, A_MX = A_FLAG + 64, K2_LDS = 81920;
constexpr float SAFE_T = 48.f;
#define EX(x) __builtin_amdgcn_exp2f(x)
#define BF(p) __builtin_bit_cast(bf16x8, p)
DI void st8_wt(void* p, u32x2 w) { __hip_atomic_store((gu64*)p, ((unsigned long long)w[1] << 32) | w[0], RLX_AGENT); }

DI void attn_role(int b, char* __restrict__ ws, char* lds) {
    const u16* Qs = (const u16*)(ws + WS_Q); const u16* Kb = (const u16*)(ws + WS_K); const u16* Vt = (const u16*)(ws + WS_V); const float* kmax = (const float*)(ws + WS_KMAX); u16* Ot = (u16*)(ws + WS_O);
    const int tid = threadIdx.x, lane = tid & 63, r32 = lane & 31, hi = lane >> 5;
    const int wid = __builtin_amdgcn_readfirstlane(tid >> 6);
    const int xcd = b & 7, idx = b >> 3, head = xcd * 4 + (idx & 3), qt = idx >> 2;
    const int qp = wid & 3, kh = wid >> 2;
    const unsigned lds0 = (unsigned)(uintptr_t)lds;
    const char* kg = (const char*)(Kb + (size_t)head * S * DH);
    const char* vg = (const char*)(Vt + (size_t)head * S * DH);
    const unsigned kvoff = wid * 1024 + ((lane ^ ((lane >> 5) & 1)) * 16);
    const unsigned vvoff = wid * 1024 + lane * 16;
#define DMA_CHUNK(c) do { glds16s(kvoff, kg + (c) * 8192, lds0 + A_KR + ((c) & 3) * 8192 + wid * 1024); glds16s(vvoff, vg + (c) * 8192, lds0 + A_VR + ((c) & 3) * 8192 + wid * 1024); } while (0)
    STAMP(1, 0);
    const int q0 = qt * 256 + qp * 64;
    const bf16x8 qa = *(const bf16x8*)(Qs + ((size_t)head * S + q0 + r32) * DH + hi * 8);
    const bf16x8 qb = *(const bf16x8*)(Qs + ((size_t)head * S + q0 + 32 + r32) * DH + hi * 8);
    float km = kmax[lane * H + head];
    DMA_CHUNK(0);
    if (tid < 256) { const unsigned w = (tid & 31) ? 0u : 0x3F803F80u; u32x4 v = {w, w, w, w}; *(u32x4*)(lds + A_CONST + tid * 16) = v; }
    km = max32(max16(rowmax16(km)));
    float qna = 0.f, qnb = 0.f;
#pragma unroll
    for (int j = 0; j < 8; ++j) { const float va = bflo((u16)qa[j]), vb = bflo((u16)qb[j]); qna = fmaf(va, va, qna); qnb = fmaf(vb, vb, qnb); }
    qna = xor32(qna); qnb = xor32(qnb);
    const float mh = __builtin_amdgcn_sqrtf(fmaxf(qna, qnb) * km) * 1.001f;
    { const bool unsafe = __any(!(mh <= SAFE_T)); if (lane == 0) ((unsigned*)(lds + A_FLAG))[wid] = unsafe ? 1u : 0u; }
    const lds_cptr kp0 = (lds_cptr)lds + A_KR + kh * 4096 + r32 * 32 + ((hi ^ (r32 >> 4)) * 16);
    const bool vreal = r32 < 16;
    const unsigned vlane = vreal ? (unsigned)(A_VR + kh * 4096 + hi * 256 + r32 * 16) : (unsigned)(A_CONST + (r32 == 16 ? 0 : ((r32 >= 20 && r32 < 28) ? 64 : 16)));
    const unsigned vmask = vreal ? 0xFFFFFFFFu : 0u;
    f32x16 oa = {}, ob = {};
    WAIT_BAR(0);
    bool slow;
    { const u32x4 f0 = *(const u32x4*)(lds + A_FLAG), f1 = *(const u32x4*)(lds + A_FLAG + 16); slow = __builtin_amdgcn_readfirstlane(f0[0] | f0[1] | f0[2] | f0[3] | f1[0] | f1[1] | f1[2] | f1[3]) != 0; }
    if (!slow) {
        DMA_CHUNK(1); DMA_CHUNK(2); DMA_CHUNK(3);
        f32x16 cinit;
#pragma unroll
        for (int r = 0; r < 16; ++r) cinit[r] = -mh;
        PIN(cinit);
        bf16x8 kf, v0, v1; f32x16 sA, sB; u32x4 p0, p1;
        kf = ldsr128(kp0);
        { lds_cptr vp = (lds_cptr)lds + vlane; v0 = ldsr128(vp); v1 = ldsr128(vp + 512); }
        sA = MFMA32(kf, qa, cinit);
        p1 = (u32x4){0u, 0u, 0u, 0u};
#define HSTEP(SC, SN, QN, OP, OC, PRE1, PRE2, PRE3) do { \
        SN = MFMA32(kf, QN, cinit); PIN(SN); SBAR(); \
        PRE1; \
        SC[0] = EX(SC[0]); SC[1] = EX(SC[1]); SC[2] = EX(SC[2]); SC[3] = EX(SC[3]); SC[4] = EX(SC[4]); SC[5] = EX(SC[5]); PIN(SC); SBAR(); \
        OP = MFMA32(v1, BF(p1), OP); PIN(OP); SBAR(); \
        PRE2; \
        SC[6] = EX(SC[6]); SC[7] = EX(SC[7]); \
        p0[0] = cvtpk(SC[0], SC[1]); p0[1] = cvtpk(SC[2], SC[3]); p0[2] = cvtpk(SC[4], SC[5]); p0[3] = cvtpk(SC[6], SC[7]); PIN(p0); SBAR(); \
        SC[8] = EX(SC[8]); SC[9] = EX(SC[9]); SC[10] = EX(SC[10]); PIN(SC); SBAR(); \
        OC = MFMA32(v0, BF(p0), OC); PIN(OC); SBAR(); \
        PRE3; \
        SC[11] = EX(SC[11]); SC[12] = EX(SC[12]); SC[13] = EX(SC[13]); SC[14] = EX(SC[14]); SC[15] = EX(SC[15]); \
        p1[0] = cvtpk(SC[8], SC[9]); p1[1] = cvtpk(SC[10], SC[11]); p1[2] = cvtpk(SC[12], SC[13]); p1[3] = cvtpk(SC[14], SC[15]); PIN(p1); SBAR(); \
    } while (0)
#define BLOCK(kn, vc, vco, vn, vno) do { \
        HSTEP(sA, sB, qb, ob, oa, kf = ldsr128(kn), v1 = ldsr128((vc) + (vco) + 512), (void)0); \
        HSTEP(sB, sA, qa, oa, ob, (void)0, (void)0, v0 = ldsr128((vn) + (vno))); \
    } while (0)
        STAMP(1, 1);
        for (int c = 0; c < 8; ++c) {
            const unsigned ro = (unsigned)((c & 3) * 8192), rn = (unsigned)(((c + 1) & 3) * 8192);
            const lds_cptr vp = (lds_cptr)lds + (vlane + (ro & vmask)), vpn = (lds_cptr)lds + (vlane + (rn & vmask));
            const lds_cptr kp = kp0 + ro, kpn = kp0 + rn;
            BLOCK(kp + 1024, vp, 0, vp, 1024);
            BLOCK(kp + 2048, vp, 1024, vp, 2048);
            BLOCK(kp + 3072, vp, 2048, vp, 3072);
            if (c == 0) WAIT_BAR(4); else if (c < 6) WAIT_BAR(2); else if (c == 6) WAIT_BAR(0);
            if (c >= 1 && c + 3 < 8) DMA_CHUNK(c + 3);
            BLOCK(kpn, vp, 3072, vpn, 0);
        }
        ob = MFMA32(v1, BF(p1), ob);
#undef HSTEP
#undef BLOCK
    } else {
        float ma = -3.0e38f, mb = -3.0e38f;
        for (int pass = 0; pass < 2; ++pass) {
            for (int c = 0; c < 8; ++c) {
                LBAR();
                DMA_CHUNK(c);
                WAIT_BAR(0);
                const unsigned ro = (unsigned)((c & 3) * 8192);
                const lds_cptr vp = (lds_cptr)lds + (vlane + (ro & vmask)), kp = kp0 + ro;
                for (int j = 0; j < 4; ++j) {
                    const bf16x8 kf = ldsr128(kp + j * 1024);
                    const bf16x8 v0 = ldsr128(vp + j * 1024), v1 = ldsr128(vp + j * 1024 + 512);
#pragma unroll
                    for (int u = 0; u < 2; ++u) {
                        const f32x16 sc = MFMA32(kf, u ? qb : qa, (f32x16){});
                        float& m = u ? mb : ma; f32x16& o = u ? ob : oa;
                        if (pass == 0) {
#pragma unroll
                            for (int r = 0; r < 16; ++r) m = fmaxf(m, sc[r]);
                        } else {
                            u32x4 p0, p1;
#pragma unroll
                            for (int i = 0; i < 4; ++i) { p0[i] = cvtpk(EX(sc[2 * i] - m), EX(sc[2 * i + 1] - m)); p1[i] = cvtpk(EX(sc[8 + 2 * i] - m), EX(sc[9 + 2 * i] - m)); }
                            o = MFMA32(v0, BF(p0), o); o = MFMA32(v1, BF(p1), o);
                        }
                        SBAR();
                    }
                }
            }
            if (pass == 0) {
                ma = max32(ma); mb = max32(mb);
                float* mx = (float*)(lds + A_MX);
                if (hi == 0) { mx[(wid * 2) * 32 + r32] = ma; mx[(wid * 2 + 1) * 32 + r32] = mb; }
                LBAR();
                ma = fmaxf(ma, mx[((wid ^ 4) * 2) * 32 + r32]); mb = fmaxf(mb, mx[((wid ^ 4) * 2 + 1) * 32 + r32]);
            }
        }
    }
    STAMP(1, 2);
    __syncthreads();
    int tz = threadIdx.x; asm volatile("" : "+v"(tz));
    const int lz = tz & 63;
    float* red = (float*)lds;
    { float* dst = red + (size_t)(wid ^ 4) * 9 * 64 + lz;
#pragma unroll
      for (int r = 0; r < 9; ++r) dst[r * 64] = kh ? oa[r] : ob[r]; }
    __syncthreads();
    float o[9];
    { const float* src = red + (size_t)wid * 9 * 64 + lz;
#pragma unroll
      for (int r = 0; r < 9; ++r) o[r] = (kh ? ob[r] : oa[r]) + src[r * 64]; }
    const float rl = __builtin_amdgcn_rcpf(swap32(o[8], false));
    u16* op = Ot + ((size_t)(((q0 >> 5) + kh) * H + head) * 512) + (lz & 31) * 8 + (lz >> 5) * 4;
    st8_wt(op, (u32x2){cvtpk(o[0] * rl, o[1] * rl), cvtpk(o[2] * rl, o[3] * rl)});
    st8_wt(op + 256, (u32x2){cvtpk(o[4] * rl, o[5] * rl), cvtpk(o[6] * rl, o[7] * rl)});
    asm volatile("s_waitcnt vmcnt(0)\n\ts_barrier" ::: "memory");
    if (tz == 0) __hip_atomic_fetch_add((gu32*)(ws + WS_CNT) + (32 + qt) * 64, 1u, RLX_AGENT);
    STAMP(1, 3);
#undef DMA_CHUNK
}

DI void wait_cnt(gu32* cnt, unsigned want) {
    for (unsigned spins = 0; spins < 400000u; ++spins) {
        if ((unsigned)__builtin_amdgcn_readfirstlane(__hip_atomic_load(cnt, RLX_AGENT)) >= want) break;
        __builtin_amdgcn_s_sleep(2);
    }
}

constexpr int G_STAGE = 24576;
constexpr int W_STAGE = 16384;
constexpr int G_RED = 4 * W_STAGE;
DI void gate_role(int b, const u16* __restrict__ x, const u16* __restrict__ wo, const u16* __restrict__ wsp, const u16* __restrict__ bsp, const u16* __restrict__ wout, char* __restrict__ ws, u16* __restrict__ out, char* lds) {
    const int xcd = b & 7, idx = b >> 3;
    const int tt = xcd * 4 + (idx & 3), ct = idx >> 2;
    const int tid = threadIdx.x, lane = tid & 63, r32 = lane & 31, hi = lane >> 5;
    const int wid = __builtin_amdgcn_readfirstlane(tid >> 6);
    const unsigned lds0 = (unsigned)(uintptr_t)lds;
    gu32* hcnt = (gu32*)(ws + WS_CNT) + tt * 64; gu32* ocnt = (gu32*)(ws + WS_CNT) + (32 + (tt >> 2)) * 64;
    STAMP(1, 0);
    if (wid >= 4) {
        const int l = wid - 4;
        if (ct < 7) {
            const char* ag = ws + WS_Z2N + (size_t)ct * 32 * 16384 + l * 4096;
            const char* bg = (const char*)wsp + (size_t)(tt * 64 + l * 16) * 4096;
            const unsigned avoff = lane * 16;
            const unsigned bv0 = (unsigned)((lane >> 3) * 4096 + (((lane & 7) ^ (lane >> 4)) * 16)), bv1 = (unsigned)((8 + (lane >> 3)) * 4096 + (((lane & 7) ^ (4 + (lane >> 4))) * 16));
            auto dma = [&](int kt, int slot) __attribute__((always_inline)) { const unsigned d = lds0 + slot * G_STAGE; const char* a_ = ag + (size_t)kt * 16384;
#pragma unroll
                for (int e = 0; e < 4; ++e) glds16s(avoff, a_ + e * 1024, d + l * 4096 + e * 1024);
                glds16s(bv0, bg + kt * 128, d + 16384 + l * 2048); glds16s(bv1, bg + kt * 128, d + 16384 + l * 2048 + 1024); };
            dma(0, 0);
            WAIT_BAR(0);
            dma(1, 1); dma(2, 2);
            int slot = 0;
            for (int kt = 1; kt < 32; ++kt) {
                if (kt < 31) WAIT_BAR(6); else WAIT_BAR(0);
                if (kt + 2 < 32) dma(kt + 2, slot);
                slot = slot == 2 ? 0 : slot + 1;
            }
            LBAR();
        }
        unsigned wv[4];
#pragma unroll
        for (int e = 0; e < 4; ++e) { const int row = l * 32 + e * 8 + (lane >> 3); wv[e] = (unsigned)(row * 1024 + ct * 128 + (((lane & 7) ^ (((row >> 1) & 1) << 2)) * 16)); }
        auto dmw = [&](const u16* w, int st) __attribute__((always_inline)) { const char* wg = (const char*)w + (size_t)st * 128 * 1024; const unsigned d = lds0 + (st & 3) * W_STAGE + l * 4096;
#pragma unroll
            for (int e = 0; e < 4; ++e) glds16s(wv[e], wg, d + e * 1024); };
        dmw(wout, 0); dmw(wout, 1); dmw(wout, 2); dmw(wout, 3);
        if (l == 0) wait_cnt(hcnt, 7);
        WAIT_BAR(12);
#pragma unroll
        for (int st = 1; st < 7; ++st) {
            if (st < 4) WAIT_BAR(8); else if (st == 4) WAIT_BAR(8); else if (st == 5) WAIT_BAR(4); else WAIT_BAR(0);
            if (st + 3 < 7) dmw(wout, st + 3);
        }
        LBAR();
        dmw(wo, 0); dmw(wo, 1); dmw(wo, 2); dmw(wo, 3);
        if (l == 0) wait_cnt(ocnt, 32);
        WAIT_BAR(0);
        return;
    }
#define GPRIO 2
    __builtin_amdgcn_s_setprio(GPRIO);
    const int g = lane >> 4, qq = (lane & 15) >> 2, pp = lane & 3;
    const int th = wid & 1, fh = wid >> 1;
    const int trow = 32 * th + r32, t = tt * 64 + trow;
    if (ct < 7) {
        f32x16 acc0 = {}, acc1 = {};
        const unsigned aoff = (unsigned)(fh * 8192 + (8 * (g >> 1) + qq) * 64 + (g & 1) * 32 + pp * 8);
        unsigned boff[4];
#pragma unroll
        for (int ks = 0; ks < 4; ++ks) boff[ks] = (unsigned)(16384 + trow * 128 + (((2 * ks + hi) ^ ((trow >> 1) & 7)) * 16));
        bf16x8 fa0[2], fa1[2], fb[2];
        auto rd = [&](int set, lds_cptr sb, int ks) __attribute__((always_inline)) {
            fb[set] = ldsr128(sb + boff[ks]);
            fa0[set] = cat(ldstr(sb + aoff + ks * 1024), ldstr(sb + aoff + ks * 1024 + 256));
            fa1[set] = cat(ldstr(sb + aoff + 4096 + ks * 1024), ldstr(sb + aoff + 4096 + ks * 1024 + 256)); };
        auto mm = [&](int set) __attribute__((always_inline)) { SBAR(); acc0 = MFMA32(fa0[set], fb[set], acc0); acc1 = MFMA32(fa1[set], fb[set], acc1); SBAR(); };
#define GSTEP(cur, nxt, sbn, ksn) do { rd(nxt, sbn, ksn); mm(cur); } while (0)
        const float bias = bflo(bsp[t]);
        u32x2 z[2][4];
#pragma unroll
        for (int i = 0; i < 2; ++i)
#pragma unroll
            for (int q = 0; q < 4; ++q) z[i][q] = *(const u32x2*)((const u16*)(ws + WS_Z1) + ((size_t)((t >> 5) * 28 + ct * 4 + 2 * fh + i) * 1024) + (hi * 32 + r32) * 4 + q * 256);
        LBAR();
        rd(0, (lds_cptr)lds, 0);
        int slot = 0;
        for (int kt = 0; kt < 32; ++kt) {
            const lds_cptr sb = (lds_cptr)lds + slot * G_STAGE;
            GSTEP(0, 1, sb, 1);
            GSTEP(1, 0, sb, 2);
            GSTEP(0, 1, sb, 3);
            slot = slot == 2 ? 0 : slot + 1;
            if (kt < 31) { LBAR(); GSTEP(1, 0, (lds_cptr)lds + slot * G_STAGE, 0); }
            else { acc0 = MFMA32(fa0[1], fb[1], acc0); acc1 = MFMA32(fa1[1], fb[1], acc1); }
        }
        STAMP(1, 2);
#pragma unroll
        for (int i = 0; i < 2; ++i) {
            u16* hp = (u16*)(ws + WS_H) + ((size_t)((t >> 5) * 56 + (ct * 4 + 2 * fh + i) * 2) * 512) + r32 * 8 + hi * 4;
#pragma unroll
            for (int q = 0; q < 4; ++q) {
                float v[4];
#pragma unroll
                for (int r = 0; r < 4; ++r) v[r] = (i ? acc1[4 * q + r] : acc0[4 * q + r]) + bias;
                st8_wt(hp + (q >> 1) * 512 + (q & 1) * 256, (u32x2){cvtpk(v[0] * bflo(z[i][q][0]), v[1] * bfhi(z[i][q][0])), cvtpk(v[2] * bflo(z[i][q][1]), v[3] * bfhi(z[i][q][1]))});
            }
        }
        asm volatile("s_waitcnt vmcnt(0) lgkmcnt(0)\n\ts_barrier" ::: "memory");
        if (tid == 0) __hip_atomic_fetch_add(hcnt, 1u, RLX_AGENT);
    }
    STAMP(1, 3);
    const int kh = fh;
    const int kr = 8 * (g >> 1) + qq;
    unsigned wof[2][2];
#pragma unroll
    for (int n = 0; n < 2; ++n) { wof[n][0] = (unsigned)((64 * kh + kr) * 128 + ((n ^ ((kr >> 1) & 1)) * 64) + (g & 1) * 32 + pp * 8); wof[n][1] = wof[n][0] + 512; }
    auto gemm = [&](const __amdgpu_buffer_rsrc_t tr, auto NST, bool bars, f32x16& c0, f32x16& c1) __attribute__((always_inline)) {
        constexpr int N = decltype(NST)::value;
        u32x4 tf[2][4];
#pragma unroll
        for (int i = 0; i < 4; ++i) tf[0][i] = __builtin_amdgcn_raw_buffer_load_b128(tr, lane * 16 + (4 * kh + i) * 1024, 0, 16);
#pragma unroll
        for (int st = 0; st < N; ++st) {
            if (st + 1 < N) {
#pragma unroll
                for (int i = 0; i < 4; ++i) tf[(st + 1) & 1][i] = __builtin_amdgcn_raw_buffer_load_b128(tr, lane * 16 + ((st + 1) * 8 + 4 * kh + i) * 1024, 0, 16); }
            if (bars && st > 0) LBAR();
            const lds_cptr sb = (lds_cptr)lds + (st & 3) * W_STAGE;
#pragma unroll
            for (int i = 0; i < 4; ++i) {
                const bf16x8 w0 = cat(ldstr(sb + wof[0][0] + i * 2048), ldstr(sb + wof[0][1] + i * 2048)), w1 = cat(ldstr(sb + wof[1][0] + i * 2048), ldstr(sb + wof[1][1] + i * 2048));
                c0 = MFMA32(w0, BF(tf[st & 1][i]), c0); c1 = MFMA32(w1, BF(tf[st & 1][i]), c1);
            }
        }
    };
    auto merge = [&](f32x16& c0, f32x16& c1) __attribute__((always_inline)) {
        float* red = (float*)(lds + G_RED);
        float* dst = red + (size_t)(wid ^ 2) * 1024 + lane;
#pragma unroll
        for (int r = 0; r < 16; ++r) dst[r * 64] = kh ? c0[r] : c1[r];
    };
    f32x16 gb0 = {}, gb1 = {};
    LBAR();
    gemm(__builtin_amdgcn_make_buffer_rsrc((void*)(ws + WS_H + (size_t)(tt * 2 + th) * 57344), 0, 57344, 0x00020000), std::integral_constant<int, 7>{}, true, gb0, gb1);
    merge(gb0, gb1);
    LBAR();
    f32x16 gate;
    { const float* src = (const float*)(lds + G_RED) + (size_t)wid * 1024 + lane;
#pragma unroll
      for (int r = 0; r < 16; ++r) { const float bb = (kh ? gb1[r] : gb0[r]) + src[r * 64]; gate[r] = bb * __builtin_amdgcn_rcpf(1.0f + EX(-LOG2E * bb)); } }
    PIN(gate);
    u32x2 xv[4];
    { const int n0 = ct * 64 + kh * 32 + 4 * hi;
#pragma unroll
      for (int q = 0; q < 4; ++q) xv[q] = *(const u32x2*)(x + (size_t)t * D + n0 + 8 * q); }
    STAMP(1, 4);
    LBAR();
    STAMP(1, 5);
    f32x16 s0 = {}, s1 = {};
    gemm(__builtin_amdgcn_make_buffer_rsrc((void*)(ws + WS_O + (size_t)(tt * 2 + th) * 32768), 0, 32768, 0x00020000), std::integral_constant<int, 4>{}, false, s0, s1);
    merge(s0, s1);
    LBAR();
    { const float* src = (const float*)(lds + G_RED) + (size_t)wid * 1024 + lane;
      const int n0 = ct * 64 + kh * 32 + 4 * hi;
#pragma unroll
      for (int q = 0; q < 4; ++q) { float v[4];
#pragma unroll
          for (int r = 0; r < 4; ++r) v[r] = ((kh ? s1[4 * q + r] : s0[4 * q + r]) + src[(4 * q + r) * 64]) * gate[4 * q + r];
          *(u32x2*)(out + (size_t)t * D + n0 + 8 * q) = (u32x2){cvtpk(bflo(xv[q][0]) + v[0], bfhi(xv[q][0]) + v[1]), cvtpk(bflo(xv[q][1]) + v[2], bfhi(xv[q][1]) + v[3])}; } }
    STAMP(1, 6);
}

__global__ __launch_bounds__(512, 4) void k2_mix(const u16* __restrict__ x, const u16* __restrict__ wo, const u16* __restrict__ wsp, const u16* __restrict__ bsp, const u16* __restrict__ wout, char* __restrict__ ws, u16* __restrict__ out) {
    extern __shared__ __attribute__((aligned(16))) char lds[];
    if (blockIdx.x < 256) attn_role(blockIdx.x, ws, lds);
    if (blockIdx.x >= 256) gate_role(blockIdx.x - 256, x, wo, wsp, bsp, wout, ws, out, lds);
}

extern "C" void kernel_launch(void* const* d_in, const int* in_sizes, int n_in, void* d_out, int out_size, void* d_ws, size_t ws_size, hipStream_t stream) {
    const u16* x = (const u16*)d_in[0]; const u16* wqkv = (const u16*)d_in[1]; const u16* wo = (const u16*)d_in[2];
    const u16* win = (const u16*)d_in[4]; const u16* wsp = (const u16*)d_in[5]; const u16* bsp = (const u16*)d_in[6]; const u16* wout = (const u16*)d_in[7];
    char* ws = (char*)d_ws;
    (void)hipFuncSetAttribute((const void*)k1_proj, hipFuncAttributeMaxDynamicSharedMemorySize, K1_LDS);
    (void)hipFuncSetAttribute((const void*)k2_mix, hipFuncAttributeMaxDynamicSharedMemorySize, K2_LDS);
    k1_proj<<<256, 768, K1_LDS, stream>>>(x, wqkv, win, ws);
    k2_mix<<<512, 512, K2_LDS, stream>>>(x, wo, wsp, bsp, wout, ws, (u16*)d_out);
}
```

```cpp
#include <hip/hip_runtime.h>
#include <hip/hip_bf16.h>
#include <stdint.h>
#include <type_traits>

typedef unsigned short u16;
using bf16x8 = __attribute__((ext_vector_type(8))) short;
using s16x4 = __attribute__((ext_vector_type(4))) short;
using f32x16 = __attribute__((ext_vector_type(16))) float;
using f32x4 = __attribute__((ext_vector_type(4))) float;
using u32x4 = __attribute__((ext_vector_type(4))) unsigned;
using u32x2 = __attribute__((ext_vector_type(2))) unsigned;
typedef __bf16 bf16x2_t __attribute__((ext_vector_type(2)));
typedef float f32x2_t __attribute__((ext_vector_type(2)));
typedef short v4i16_t __attribute__((ext_vector_type(4)));
#define LDSAS __attribute__((address_space(3)))
typedef LDSAS const char* lds_cptr;
#define MFMA32(a, b, c) __builtin_amdgcn_mfma_f32_32x32x16_bf16(a, b, c, 0, 0, 0)
#define MFMA16(a, b, c) __builtin_amdgcn_mfma_f32_16x16x32_bf16(a, b, c, 0, 0, 0)
#define SBAR() __builtin_amdgcn_sched_barrier(0)
#define PIN(x) asm volatile("" : "+v"(x))
#define WAIT_BAR(N) asm volatile("s_waitcnt vmcnt(" #N ") lgkmcnt(0)\n\ts_barrier" ::: "memory")
#define LBAR() asm volatile("s_waitcnt lgkmcnt(0)\n\ts_barrier" ::: "memory")
#define DI __device__ __forceinline__
#define ST8(p, w) st8_wt((void*)(p), (w))
#define ST16(off, v) __builtin_amdgcn_raw_buffer_store_b128((v), wsr, (off), 0, 16)
#define STAMP(kern, i) do { } while (0)

constexpr int S = 2048, D = 512, H = 32, DH = 16, FH = 896;
constexpr float LOG2E = 1.4426950408889634f;

constexpr size_t WS_Q = 0, WS_K = 2u << 20, WS_V = 4u << 20, WS_Z1 = 6u << 20, WS_Z2R = 10u << 20, WS_Z2N = 14u << 20, WS_H = 18u << 20, WS_O = 22u << 20, WS_KMAX = 24u << 20, WS_EPOCH = 25u << 20, WS_XCH = 26u << 20, WS_XST = 27u << 20, WS_CNT = 28u << 20;

DI unsigned cvtpk(float lo, float hi) { f32x2_t v = {lo, hi}; return __builtin_bit_cast(unsigned, __builtin_convertvector(v, bf16x2_t)); }
DI float bflo(unsigned w) { return __uint_as_float(w << 16); }
DI float bfhi(unsigned w) { return __uint_as_float(w & 0xFFFF0000u); }
DI float dot2(unsigned a, unsigned b, float c) { return __builtin_amdgcn_fdot2_f32_bf16(__builtin_bit_cast(bf16x2_t, a), __builtin_bit_cast(bf16x2_t, b), c, false); }
DI void glds16s(unsigned voff, const void* sbase, unsigned lds_base) {
    lds_base = __builtin_amdgcn_readfirstlane(lds_base);
    unsigned sv; asm volatile("s_mov_b32 %0, m0\n\ts_mov_b32 m0, %3\n\ts_nop 0\n\tglobal_load_lds_dwordx4 %1, %2\n\ts_mov_b32 m0, %0" : "=&s"(sv) : "v"(voff), "s"(sbase), "s"(lds_base) : "memory"); }
DI bf16x8 ldsr128(lds_cptr p) { return *(const LDSAS bf16x8*)p; }
DI s16x4 ldstr(lds_cptr p) { return __builtin_bit_cast(s16x4, __builtin_amdgcn_ds_read_tr16_b64_v4i16((LDSAS v4i16_t*)p)); }
DI bf16x8 cat(s16x4 a, s16x4 b) { return __builtin_shufflevector(a, b, 0, 1, 2, 3, 4, 5, 6, 7); }
DI float swap32(float x, bool sum) { auto rr = __builtin_amdgcn_permlane32_swap(__float_as_uint(x), __float_as_uint(x), false, false); return sum ? __uint_as_float(rr[0]) + __uint_as_float(rr[1]) : __uint_as_float(rr[0]); }
DI float gelu_t(float x) {
    const float c1 = -2.0f * 0.7978845608028654f * LOG2E, c3 = c1 * 0.044715f;
    const float u = x * fmaf(x * x, c3, c1);
    return x * __builtin_amdgcn_rcpf(1.0f + __builtin_amdgcn_exp2f(u));
}

constexpr int K1_XB = 16384;
constexpr int K1_STAGE = K1_XB + 7 * 4096;
constexpr int K1_CS = 3 * K1_STAGE;
constexpr int K1_LDS = K1_CS + 1024;
typedef __attribute__((address_space(1))) unsigned gu32; typedef __attribute__((address_space(1))) unsigned long long gu64;
#define RLX_AGENT __ATOMIC_RELAXED, __HIP_MEMORY_SCOPE_AGENT

DI float xor16(float x) { auto r = __builtin_amdgcn_permlane16_swap(__float_as_uint(x), __float_as_uint(x), false, false); return __uint_as_float(r[0]) + __uint_as_float(r[1]); }
DI float xor32(float x) { auto r = __builtin_amdgcn_permlane32_swap(__float_as_uint(x), __float_as_uint(x), false, false); return __uint_as_float(r[0]) + __uint_as_float(r[1]); }
DI float max16(float x) { auto r = __builtin_amdgcn_permlane16_swap(__float_as_uint(x), __float_as_uint(x), false, false); return fmaxf(__uint_as_float(r[0]), __uint_as_float(r[1])); }
DI float max32(float x) { auto r = __builtin_amdgcn_permlane32_swap(__float_as_uint(x), __float_as_uint(x), false, false); return fmaxf(__uint_as_float(r[0]), __uint_as_float(r[1])); }
template <int CTRL> DI float dppf(float x) { return __uint_as_float(__builtin_amdgcn_update_dpp(0, __float_as_uint(x), CTRL, 0xF, 0xF, false)); }
DI float rowmax16(float x) { x = fmaxf(x, dppf<0x128>(x)); x = fmaxf(x, dppf<0x124>(x)); x = fmaxf(x, dppf<0x122>(x)); x = fmaxf(x, dppf<0x121>(x)); return x; }
DI float wavesum(float x) { x += dppf<0x128>(x); x += dppf<0x124>(x); x += dppf<0x122>(x); x += dppf<0x121>(x); return xor32(xor16(x)); }
DI u32x4 widen(u32x2 a, u32x2 b) { auto r0 = __builtin_amdgcn_permlane32_swap(a[0], b[0], false, false); auto r1 = __builtin_amdgcn_permlane32_swap(a[1], b[1], false, false); return (u32x4){r0[0], r1[0], r0[1], r1[1]}; }

DI void st8_wt(void* p, u32x2 w) { __hip_atomic_store((gu64*)p, ((unsigned long long)w[1] << 32) | w[0], RLX_AGENT); }
__global__ __launch_bounds__(768, 3) void k1_proj(const u16* __restrict__ x, const u16* __restrict__ wqkv, const u16* __restrict__ win, char* __restrict__ ws) {
    extern __shared__ __attribute__((aligned(16))) char lds[];
    const int tid = threadIdx.x, lane = tid & 63, r32 = lane & 31, hi = lane >> 5;
    const int wid = __builtin_amdgcn_readfirstlane(tid >> 6);
    const int b = blockIdx.x, xcd = b & 7, idx = b >> 3;
    const int panel = 8 * (xcd >> 2) + (idx & 7), j = 4 * (xcd & 3) + (idx >> 3);
    const unsigned lds0 = (unsigned)(uintptr_t)lds;
    const int jq = j & 3, u7 = 7 * (j >> 2);
    const int n1 = jq == 3 ? 1 : 2, n2 = jq == 1 ? 1 : 2;
    const int z1t0 = u7 + 2 * jq, z2t0 = u7 + (jq == 0 ? 0 : (jq == 1 ? 2 : (jq == 2 ? 3 : 5)));
    STAMP(0, 0);
    if (b == 0 && tid < 40) ((unsigned*)(ws + WS_CNT))[tid * 64] = 0u;
    if (wid >= 8) {
        const int l = wid - 8;
        const char* xb = (const char*)x + (size_t)(panel * 128 + l * 32) * 1024;
        const unsigned xv0 = (unsigned)((lane >> 3) * 1024 + (((lane & 7) ^ (lane >> 4)) * 16)), xv1 = (unsigned)((lane >> 3) * 1024 + (((lane & 7) ^ (4 + (lane >> 4))) * 16));
        const unsigned vq = (unsigned)((16 * l + (lane >> 2)) * 3072 + (lane & 3) * 16), vz = (unsigned)((16 * l + (lane >> 2)) * 3584 + (lane & 3) * 16);
        const char* wq = (const char*)wqkv + j * 64;
        int zc[4];
#pragma unroll
        for (int i = 0; i < 4; ++i) { const int ii = i < n1 + n2 ? i : n1 + n2 - 1; zc[i] = ii < n1 ? (z1t0 + ii) * 64 : FH * 2 + (z2t0 + ii - n1) * 64; }
        auto dma = [&](int st) __attribute__((always_inline)) {
            const unsigned d = lds0 + (st % 3) * K1_STAGE;
#pragma unroll
            for (int p = 0; p < 4; ++p) glds16s((p & 1) ? xv1 : xv0, xb + st * 128 + p * 8192, d + l * 4096 + p * 1024);
            const char* bq = wq + (size_t)st * 64 * 3072; const char* bz = (const char*)win + (size_t)st * 64 * 3584; const unsigned dw = d + K1_XB + l * 1024;
            glds16s(vq, bq + 2048, dw); glds16s(vq, bq, dw + 4096); glds16s(vq, bq + 1024, dw + 8192);
#pragma unroll
            for (int i = 0; i < 4; ++i) glds16s(vz, bz + zc[i], dw + (3 + i) * 4096);
        };
        dma(0);
        WAIT_BAR(0);
        dma(1); dma(2);
#pragma unroll
        for (int st = 1; st < 8; ++st) {
            if (st < 7) WAIT_BAR(11); else WAIT_BAR(0);
            if (st + 2 < 8) dma(st + 2);
        }
        return;
    }
    const int mw = wid & 3, nh = wid >> 2;
    gu32* epw = (gu32*)(ws + WS_EPOCH) + panel * 64;
    unsigned tag = __hip_atomic_load(epw, RLX_AGENT);
    u32x4 xr = *(const u32x4*)(x + (size_t)(panel * 128 + 8 * j + wid) * D + lane * 8);
    auto rowstats = [&]() __attribute__((always_inline)) {
        PIN(xr); PIN(tag); tag += 1u;
        float v[8];
#pragma unroll
        for (int i = 0; i < 4; ++i) { v[2 * i] = bflo(xr[i]); v[2 * i + 1] = bfhi(xr[i]); }
        const float m = wavesum(((v[0] + v[1]) + (v[2] + v[3])) + ((v[4] + v[5]) + (v[6] + v[7]))) * (1.0f / 512.0f);
        float q = 0.f;
#pragma unroll
        for (int i = 0; i < 8; ++i) q = fmaf(v[i] - m, v[i] - m, q);
        const float r = __builtin_amdgcn_rsqf(wavesum(q) * (1.0f / 512.0f) + 1e-5f);
        if (lane < 2) __hip_atomic_store((gu64*)(ws + WS_XST) + (size_t)(panel * 128 + 8 * j + wid) * 2 + lane, ((unsigned long long)tag << 32) | __float_as_uint(lane ? r : m), RLX_AGENT);
    };
    f32x16 acc[4];
#pragma unroll
    for (int s = 0; s < 4; ++s) acc[s] = (f32x16){};
    float cs0 = 0.f, cs1 = 0.f;
    const int g = lane >> 4, qq = (lane & 15) >> 2, pp = lane & 3;
    const int trow = 32 * mw + r32;
    unsigned xo[4];
#pragma unroll
    for (int ks = 0; ks < 4; ++ks) xo[ks] = (unsigned)(trow * 128 + (((2 * ks + hi) ^ ((trow >> 1) & 7)) * 16));
    const unsigned wlane = (unsigned)(K1_XB + (8 * (g >> 1) + qq) * 64 + (g & 1) * 32 + pp * 8);
    const __amdgpu_buffer_rsrc_t sr = __builtin_amdgcn_make_buffer_rsrc((void*)(ws + WS_XST + (size_t)panel * 128 * 16), 0, 128 * 16, 0x00020000);
    u32x4 pst;
    int tile[4];
    auto mainloop = [&](auto NHC) __attribute__((always_inline)) {
        constexpr int NHV = decltype(NHC)::value; constexpr int NS = NHV == 0 ? 4 : 3;
        unsigned wo[NS];
#pragma unroll
        for (int s = 0; s < NS; ++s) { int t = s + mw; t = t >= NS ? t - NS : t; t = t >= NS ? t - NS : t; tile[s] = 4 * NHV + t; wo[s] = wlane + tile[s] * 4096; }
        bf16x8 xa[2], wf[2][NS];
        auto rd = [&](const int set, const lds_cptr sb, const int ks) __attribute__((always_inline)) {
            xa[set] = ldsr128(sb + xo[ks]);
#pragma unroll
            for (int s = 0; s < NS; ++s) wf[set][s] = cat(ldstr(sb + wo[s] + ks * 1024), ldstr(sb + wo[s] + ks * 1024 + 256));
        };
        auto mm = [&](const int set) __attribute__((always_inline)) {
            SBAR();
#pragma unroll
            for (int s = 0; s < NS; ++s) acc[s] = MFMA32(wf[set][s], xa[set], acc[s]);
            const u32x4 w = __builtin_bit_cast(u32x4, wf[set][0]);
#pragma unroll
            for (int i = 0; i < 4; ++i) { cs0 += bflo(w[i]); cs1 += bfhi(w[i]); }
            PIN(cs0); PIN(cs1);
#pragma unroll
            for (int i = 0; i < NS; ++i) { __builtin_amdgcn_sched_group_barrier(0x008, 1, 0); __builtin_amdgcn_sched_group_barrier(0x002, NS == 4 ? 4 : 6, 0); }
            SBAR();
        };
        LBAR();
        STAMP(0, 1);
        rd(0, (lds_cptr)lds, 0);
#pragma unroll
        for (int st = 0; st < 8; ++st) {
            const lds_cptr sb = (lds_cptr)lds + (st % 3) * K1_STAGE;
            rd(1, sb, 1); mm(0);
            rd(0, sb, 2); mm(1);
            rd(1, sb, 3); mm(0);
            if (st < 7) { LBAR(); rd(0, (lds_cptr)lds + ((st + 1) % 3) * K1_STAGE, 0); }
            if (st == 1) rowstats();
            if (st == 5) pst = __builtin_amdgcn_raw_buffer_load_b128(sr, trow * 16, 0, 16);
            mm(1);
        }
    };
    if (nh == 0) mainloop(std::integral_constant<int, 0>{}); else mainloop(std::integral_constant<int, 1>{});
    STAMP(0, 2);
    float* csl = (float*)(lds + K1_CS);
    { const float c = xor32(cs0 + cs1); if (hi == 0) csl[tile[0] * 32 + r32] = c; }
    const int tok = panel * 128 + trow;
    float mu, rs;
    {   u32x4 p = pst;
        for (unsigned spins = 0; !__all((p[1] == tag) & (p[3] == tag)) && spins < 200000u; ++spins) {
            __builtin_amdgcn_s_sleep(1);
            p = __builtin_amdgcn_raw_buffer_load_b128(sr, trow * 16, 0, 16);
        }
        mu = __uint_as_float(p[0]); rs = __uint_as_float(p[2]); }
    LBAR();
    STAMP(0, 3);
    auto lnv = [&](int s, float (&v)[16], float sc) __attribute__((always_inline)) {
#pragma unroll
        for (int q = 0; q < 4; ++q) { const f32x4 c = *(const f32x4*)(csl + tile[s] * 32 + 8 * q + 4 * hi);
#pragma unroll
            for (int r = 0; r < 4; ++r) v[4 * q + r] = sc * (acc[s][4 * q + r] - mu * c[r]); } };
    const __amdgpu_buffer_rsrc_t wsr = __builtin_amdgcn_make_buffer_rsrc((void*)ws, 0, 32u << 20, 0x00020000);
    u16* Z1 = (u16*)(ws + WS_Z1);
    auto z1store = [&](int zt, const u32x2 (&w)[4]) __attribute__((always_inline)) {
        u16* p = Z1 + ((size_t)((tok >> 5) * 28 + zt) * 1024) + (hi * 32 + r32) * 4;
#pragma unroll
        for (int q = 0; q < 4; ++q) ST8(p + q * 256, w[q]); };
    auto gelu16 = [&](int s, u32x2 (&w)[4]) __attribute__((always_inline)) { float v[16]; lnv(s, v, rs);
#pragma unroll
        for (int q = 0; q < 4; ++q) w[q] = (u32x2){cvtpk(gelu_t(v[4 * q]), gelu_t(v[4 * q + 1])), cvtpk(gelu_t(v[4 * q + 2]), gelu_t(v[4 * q + 3]))}; };
    if (nh == 0) {
#pragma unroll
        for (int s = 0; s < 4; ++s) {
            const int T = tile[s];
            if (T == 3) { u32x2 w[4]; gelu16(s, w); z1store(z1t0, w); }
            else {
                float v[16]; lnv(s, v, T == 1 ? rs * (0.25f * LOG2E) : rs);
                u32x2 w[4];
#pragma unroll
                for (int q = 0; q < 4; ++q) w[q] = (u32x2){cvtpk(v[4 * q], v[4 * q + 1]), cvtpk(v[4 * q + 2], v[4 * q + 3])};
                if (T == 0) {
                    char* sc = lds + mw * 2048;
#pragma unroll
                    for (int q = 0; q < 4; ++q) *(u32x2*)(sc + r32 * 64 + (8 * q + 4 * hi) * 2) = w[q];
                    asm volatile("s_waitcnt lgkmcnt(0)" ::: "memory");
                    u16* Vt = (u16*)(ws + WS_V);
#pragma unroll
                    for (int hh = 0; hh < 2; ++hh)
#pragma unroll
                        for (int ks = 0; ks < 2; ++ks) {
                            const s16x4 t4 = ldstr((lds_cptr)lds + mw * 2048 + (16 * ks + 8 * (g >> 1) + 4 * (g & 1) + qq) * 64 + hh * 32 + pp * 8);
                            ST8(Vt + ((size_t)(2 * j + hh) * 128 + ((panel * 128 + 32 * mw) >> 4) + ks) * 256 + (g & 1) * 128 + (lane & 15) * 8 + (g >> 1) * 4, __builtin_bit_cast(u32x2, t4)); }
                } else {
                    const unsigned dsto = (unsigned)(T == 1 ? WS_Q : WS_K);
#pragma unroll
                    for (int hh = 0; hh < 2; ++hh) ST16(dsto + (((2 * j + hh) * S + tok) * 16 + 8 * hi) * 2, widen(w[2 * hh], w[2 * hh + 1]));
                    if (T == 2) {
                        float* kmax = (float*)(ws + WS_KMAX);
#pragma unroll
                        for (int hh = 0; hh < 2; ++hh) { float n = 0.f;
#pragma unroll
                            for (int q = 0; q < 2; ++q) { const u32x2 ww = w[2 * hh + q]; n = fmaf(bflo(ww[0]), bflo(ww[0]), n); n = fmaf(bfhi(ww[0]), bfhi(ww[0]), n); n = fmaf(bflo(ww[1]), bflo(ww[1]), n); n = fmaf(bfhi(ww[1]), bfhi(ww[1]), n); }
                            n = max16(rowmax16(xor32(n)));
                            if (lane == 0) kmax[(panel * 4 + mw) * H + 2 * j + hh] = n; }
                    }
                }
            }
        }
    } else {
        __builtin_amdgcn_s_setprio(2);
        float sm = 0.f;
#pragma unroll
        for (int s = 0; s < 3; ++s) { const int zi = tile[s] - 3;
            if (zi >= n1 && zi < n1 + n2) { float v[16]; lnv(s, v, rs);
#pragma unroll
                for (int r = 0; r < 16; ++r) { acc[s][r] = gelu_t(v[r]); sm += acc[s][r]; } } }
        sm = xor32(sm);
        const float ml = sm * (n2 == 2 ? (1.0f / 64.f) : (1.0f / 32.f)); float m2 = 0.f;
#pragma unroll
        for (int s = 0; s < 3; ++s) { const int zi = tile[s] - 3;
            if (zi >= n1 && zi < n1 + n2) {
#pragma unroll
                for (int r = 0; r < 16; ++r) { const float d = acc[s][r] - ml; m2 = fmaf(d, d, m2); } } }
        m2 = xor32(m2);
        __hip_atomic_store((gu64*)(ws + WS_XCH) + ((size_t)(panel * 16 + j) * 128 + trow) * 2 + hi, ((unsigned long long)tag << 32) | __float_as_uint(hi ? m2 : sm), RLX_AGENT);
        __builtin_amdgcn_s_setprio(0);
        STAMP(0, 4);
#pragma unroll
        for (int s = 0; s < 3; ++s) { const int zi = tile[s] - 3; if (zi < n1) { u32x2 w[4]; gelu16(s, w); z1store(z1t0 + zi, w); } }
        float mean, rstd;
        {
            const __amdgpu_buffer_rsrc_t xr = __builtin_amdgcn_make_buffer_rsrc((void*)(ws + WS_XCH + (size_t)panel * 16 * 128 * 16), 0, 16 * 128 * 16, 0x00020000);
            u32x4 p[8];
            for (unsigned spins = 0;; ++spins) {
                bool ok = true;
#pragma unroll
                for (int u = 0; u < 8; ++u) p[u] = __builtin_amdgcn_raw_buffer_load_b128(xr, ((8 * hi + u) * 128 + trow) * 16, 0, 16);
#pragma unroll
                for (int u = 0; u < 8; ++u) ok &= (p[u][1] == tag) & (p[u][3] == tag);
                if (__all(ok) || spins > 200000u) break;
                __builtin_amdgcn_s_sleep(1);
            }
            float tot = 0.f;
#pragma unroll
            for (int u = 0; u < 8; ++u) tot += __uint_as_float(p[u][0]);
            mean = xor32(tot) * (1.0f / FH);
            float mm2 = 0.f;
#pragma unroll
            for (int u = 0; u < 8; ++u) { const float nl = (u & 3) == 1 ? 32.f : 64.f; const float d = __uint_as_float(p[u][0]) * ((u & 3) == 1 ? (1.0f / 32.f) : (1.0f / 64.f)) - mean; mm2 += __uint_as_float(p[u][2]) + nl * d * d; }
            rstd = __builtin_amdgcn_rsqf(xor32(mm2) * (1.0f / FH) + 1e-5f);
        }
        STAMP(0, 5);
        if (wid == 4 && lane == 0) __hip_atomic_store(epw, tag, RLX_AGENT);
        const float nmr = -mean * rstd;
#pragma unroll
        for (int s = 0; s < 3; ++s) { const int zi = tile[s] - 3;
            if (zi >= n1 && zi < n1 + n2) {
                const int ft = z2t0 + zi - n1;
                u32x2 w[4];
#pragma unroll
                for (int q = 0; q < 4; ++q) w[q] = (u32x2){cvtpk(fmaf(acc[s][4 * q], rstd, nmr), fmaf(acc[s][4 * q + 1], rstd, nmr)), cvtpk(fmaf(acc[s][4 * q + 2], rstd, nmr), fmaf(acc[s][4 * q + 3], rstd, nmr))};
                char* sc = lds + 8192 + mw * 2048;
#pragma unroll
                for (int q = 0; q < 4; ++q) *(u32x2*)(sc + r32 * 64 + (8 * q + 4 * hi) * 2) = w[q];
                asm volatile("s_waitcnt lgkmcnt(0)" ::: "memory");
                const int tk0 = panel * 128 + 32 * mw;
#pragma unroll
                for (int ks = 0; ks < 2; ++ks) {
                    const lds_cptr rp = (lds_cptr)lds + 8192 + mw * 2048 + (16 * ks + 8 * (g >> 1) + qq) * 64 + (g & 1) * 32 + pp * 8;
                    const bf16x8 fr = cat(ldstr(rp), ldstr(rp + 256));
                    ST16((unsigned)WS_Z2N + (unsigned)(((((ft >> 2) * 32 + (tk0 >> 6)) * 4 + (ft & 3)) * 4 + ((tk0 >> 4) & 3) + ks) * 1024 + lane * 16), __builtin_bit_cast(u32x4, fr));
                }
                asm volatile("s_waitcnt lgkmcnt(0)" ::: "memory");
            } }
    }
    STAMP(0, 6);
}

constexpr int A_KR = 0, A_VR = 32768, A_CONST = 65536, A_FLAG = A_CONST + 4096, A_MX = A_FLAG + 64, K2_LDS = 81920;
constexpr float SAFE_T = 48.f;
#define EX(x) __builtin_amdgcn_exp2f(x)
#define BF(p) __builtin_bit_cast(bf16x8, p)

DI void attn_role(int b, char* __restrict__ ws, char* lds) {
    const u16* Qs = (const u16*)(ws + WS_Q); const u16* Kb = (const u16*)(ws + WS_K); const u16* Vt = (const u16*)(ws + WS_V); const float* kmax = (const float*)(ws + WS_KMAX); u16* Ot = (u16*)(ws + WS_O);
    const int tid = threadIdx.x, lane = tid & 63, r32 = lane & 31, hi = lane >> 5;
    const int wid = __builtin_amdgcn_readfirstlane(tid >> 6);
    const int xcd = b & 7, idx = b >> 3, head = xcd * 4 + (idx & 3), qt = idx >> 2;
    const int qp = wid & 3, kh = wid >> 2;
    const unsigned lds0 = (unsigned)(uintptr_t)lds;
    const char* kg = (const char*)(Kb + (size_t)head * S * DH);
    const char* vg = (const char*)(Vt + (size_t)head * S * DH);
    const unsigned kvoff = wid * 1024 + ((lane ^ ((lane >> 5) & 1)) * 16);
    const unsigned vvoff = wid * 1024 + lane * 16;
#define DMA_CHUNK(c) do { glds16s(kvoff, kg + (c) * 8192, lds0 + A_KR + ((c) & 3) * 8192 + wid * 1024); glds16s(vvoff, vg + (c) * 8192, lds0 + A_VR + ((c) & 3) * 8192 + wid * 1024); } while (0)
    STAMP(1, 0);
    const int q0 = qt * 256 + qp * 64;
    const bf16x8 qa = *(const bf16x8*)(Qs + ((size_t)head * S + q0 + r32) * DH + hi * 8);
    const bf16x8 qb = *(const bf16x8*)(Qs + ((size_t)head * S + q0 + 32 + r32) * DH + hi * 8);
    float km = kmax[lane * H + head];
    DMA_CHUNK(0);
    if (tid < 256) { const unsigned w = (tid & 31) ? 0u : 0x3F803F80u; u32x4 v = {w, w, w, w}; *(u32x4*)(lds + A_CONST + tid * 16) = v; }
    km = max32(max16(rowmax16(km)));
    float qna = 0.f, qnb = 0.f;
#pragma unroll
    for (int j = 0; j < 8; ++j) { const float va = bflo((u16)qa[j]), vb = bflo((u16)qb[j]); qna = fmaf(va, va, qna); qnb = fmaf(vb, vb, qnb); }
    qna = xor32(qna); qnb = xor32(qnb);
    const float mh = __builtin_amdgcn_sqrtf(fmaxf(qna, qnb) * km) * 1.001f;
    { const bool unsafe = __any(!(mh <= SAFE_T)); if (lane == 0) ((unsigned*)(lds + A_FLAG))[wid] = unsafe ? 1u : 0u; }
    const lds_cptr kp0 = (lds_cptr)lds + A_KR + kh * 4096 + r32 * 32 + ((hi ^ (r32 >> 4)) * 16);
    const bool vreal = r32 < 16;
    const unsigned vlane = vreal ? (unsigned)(A_VR + kh * 4096 + hi * 256 + r32 * 16) : (unsigned)(A_CONST + (r32 == 16 ? 0 : ((r32 >= 20 && r32 < 28) ? 64 : 16)));
    const unsigned vmask = vreal ? 0xFFFFFFFFu : 0u;
    f32x16 oa = {}, ob = {};
    WAIT_BAR(0);
    bool slow;
    { const u32x4 f0 = *(const u32x4*)(lds + A_FLAG), f1 = *(const u32x4*)(lds + A_FLAG + 16); slow = __builtin_amdgcn_readfirstlane(f0[0] | f0[1] | f0[2] | f0[3] | f1[0] | f1[1] | f1[2] | f1[3]) != 0; }
    if (!slow) {
        DMA_CHUNK(1); DMA_CHUNK(2); DMA_CHUNK(3);
        f32x16 cinit;
#pragma unroll
        for (int r = 0; r < 16; ++r) cinit[r] = -mh;
        PIN(cinit);
        bf16x8 kf, v0, v1; f32x16 sA, sB; u32x4 p0, p1;
        kf = ldsr128(kp0);
        { lds_cptr vp = (lds_cptr)lds + vlane; v0 = ldsr128(vp); v1 = ldsr128(vp + 512); }
        sA = MFMA32(kf, qa, cinit);
        p1 = (u32x4){0u, 0u, 0u, 0u};
#define HSTEP(SC, SN, QN, OP, OC, PRE1, PRE2, PRE3) do { \
        SN = MFMA32(kf, QN, cinit); PIN(SN); SBAR(); \
        PRE1; \
        SC[0] = EX(SC[0]); SC[1] = EX(SC[1]); SC[2] = EX(SC[2]); SC[3] = EX(SC[3]); SC[4] = EX(SC[4]); SC[5] = EX(SC[5]); PIN(SC); SBAR(); \
        OP = MFMA32(v1, BF(p1), OP); PIN(OP); SBAR(); \
        PRE2; \
        SC[6] = EX(SC[6]); SC[7] = EX(SC[7]); \
        p0[0] = cvtpk(SC[0], SC[1]); p0[1] = cvtpk(SC[2], SC[3]); p0[2] = cvtpk(SC[4], SC[5]); p0[3] = cvtpk(SC[6], SC[7]); PIN(p0); SBAR(); \
        SC[8] = EX(SC[8]); SC[9] = EX(SC[9]); SC[10] = EX(SC[10]); PIN(SC); SBAR(); \
        OC = MFMA32(v0, BF(p0), OC); PIN(OC); SBAR(); \
        PRE3; \
        SC[11] = EX(SC[11]); SC[12] = EX(SC[12]); SC[13] = EX(SC[13]); SC[14] = EX(SC[14]); SC[15] = EX(SC[15]); \
        p1[0] = cvtpk(SC[8], SC[9]); p1[1] = cvtpk(SC[10], SC[11]); p1[2] = cvtpk(SC[12], SC[13]); p1[3] = cvtpk(SC[14], SC[15]); PIN(p1); SBAR(); \
    } while (0)
#define BLOCK(kn, vc, vco, vn, vno) do { \
        HSTEP(sA, sB, qb, ob, oa, kf = ldsr128(kn), v1 = ldsr128((vc) + (vco) + 512), (void)0); \
        HSTEP(sB, sA, qa, oa, ob, (void)0, (void)0, v0 = ldsr128((vn) + (vno))); \
    } while (0)
        STAMP(1, 1);
        for (int c = 0; c < 8; ++c) {
            const unsigned ro = (unsigned)((c & 3) * 8192), rn = (unsigned)(((c + 1) & 3) * 8192);
            const lds_cptr vp = (lds_cptr)lds + (vlane + (ro & vmask)), vpn = (lds_cptr)lds + (vlane + (rn & vmask));
            const lds_cptr kp = kp0 + ro, kpn = kp0 + rn;
            BLOCK(kp + 1024, vp, 0, vp, 1024);
            BLOCK(kp + 2048, vp, 1024, vp, 2048);
            BLOCK(kp + 3072, vp, 2048, vp, 3072);
            if (c == 0) WAIT_BAR(4); else if (c < 6) WAIT_BAR(2); else if (c == 6) WAIT_BAR(0);
            if (c >= 1 && c + 3 < 8) DMA_CHUNK(c + 3);
            BLOCK(kpn, vp, 3072, vpn, 0);
        }
        ob = MFMA32(v1, BF(p1), ob);
#undef HSTEP
#undef BLOCK
    } else {
        float ma = -3.0e38f, mb = -3.0e38f;
        for (int pass = 0; pass < 2; ++pass) {
            for (int c = 0; c < 8; ++c) {
                LBAR();
                DMA_CHUNK(c);
                WAIT_BAR(0);
                const unsigned ro = (unsigned)((c & 3) * 8192);
                const lds_cptr vp = (lds_cptr)lds + (vlane + (ro & vmask)), kp = kp0 + ro;
                for (int j = 0; j < 4; ++j) {
                    const bf16x8 kf = ldsr128(kp + j * 1024);
                    const bf16x8 v0 = ldsr128(vp + j * 1024), v1 = ldsr128(vp + j * 1024 + 512);
#pragma unroll
                    for (int u = 0; u < 2; ++u) {
                        const f32x16 sc = MFMA32(kf, u ? qb : qa, (f32x16){});
                        float& m = u ? mb : ma; f32x16& o = u ? ob : oa;
                        if (pass == 0) {
#pragma unroll
                            for (int r = 0; r < 16; ++r) m = fmaxf(m, sc[r]);
                        } else {
                            u32x4 p0, p1;
#pragma unroll
                            for (int i = 0; i < 4; ++i) { p0[i] = cvtpk(EX(sc[2 * i] - m), EX(sc[2 * i + 1] - m)); p1[i] = cvtpk(EX(sc[8 + 2 * i] - m), EX(sc[9 + 2 * i] - m)); }
                            o = MFMA32(v0, BF(p0), o); o = MFMA32(v1, BF(p1), o);
                        }
                        SBAR();
                    }
                }
            }
            if (pass == 0) {
                ma = max32(ma); mb = max32(mb);
                float* mx = (float*)(lds + A_MX);
                if (hi == 0) { mx[(wid * 2) * 32 + r32] = ma; mx[(wid * 2 + 1) * 32 + r32] = mb; }
                LBAR();
                ma = fmaxf(ma, mx[((wid ^ 4) * 2) * 32 + r32]); mb = fmaxf(mb, mx[((wid ^ 4) * 2 + 1) * 32 + r32]);
            }
        }
    }
    STAMP(1, 2);
    __syncthreads();
    int tz = threadIdx.x; asm volatile("" : "+v"(tz));
    const int lz = tz & 63;
    float* red = (float*)lds;
    { float* dst = red + (size_t)(wid ^ 4) * 9 * 64 + lz;
#pragma unroll
      for (int r = 0; r < 9; ++r) dst[r * 64] = kh ? oa[r] : ob[r]; }
    __syncthreads();
    float o[9];
    { const float* src = red + (size_t)wid * 9 * 64 + lz;
#pragma unroll
      for (int r = 0; r < 9; ++r) o[r] = (kh ? ob[r] : oa[r]) + src[r * 64]; }
    const float rl = __builtin_amdgcn_rcpf(swap32(o[8], false));
    u16* op = Ot + ((size_t)(((q0 >> 5) + kh) * H + head) * 512) + (lz & 31) * 8 + (lz >> 5) * 4;
    st8_wt(op, (u32x2){cvtpk(o[0] * rl, o[1] * rl), cvtpk(o[2] * rl, o[3] * rl)});
    st8_wt(op + 256, (u32x2){cvtpk(o[4] * rl, o[5] * rl), cvtpk(o[6] * rl, o[7] * rl)});
    asm volatile("s_waitcnt vmcnt(0)\n\ts_barrier" ::: "memory");
    if (tz == 0) __hip_atomic_fetch_add((gu32*)(ws + WS_CNT) + (32 + qt) * 64, 1u, RLX_AGENT);
    STAMP(1, 3);
#undef DMA_CHUNK
}

DI void wait_cnt(gu32* cnt, unsigned want) {
    for (unsigned spins = 0; spins < 400000u; ++spins) {
        if ((unsigned)__builtin_amdgcn_readfirstlane(__hip_atomic_load(cnt, RLX_AGENT)) >= want) break;
        __builtin_amdgcn_s_sleep(1);
    }
}

#define O_AUX 16
#define H_AUX 16
constexpr int G_STAGE = 24576;
constexpr int W_STAGE = 16384;
constexpr int G_RED = 4 * W_STAGE;
DI void gate_role(int b, const u16* __restrict__ x, const u16* __restrict__ wo, const u16* __restrict__ wsp, const u16* __restrict__ bsp, const u16* __restrict__ wout, char* __restrict__ ws, u16* __restrict__ out, char* lds) {
    const int xcd = b & 7, idx = b >> 3;
    const int tt = xcd * 4 + (idx & 3), ct = idx >> 2;
    const int tid = threadIdx.x, lane = tid & 63, r32 = lane & 31, hi = lane >> 5;
    const int wid = __builtin_amdgcn_readfirstlane(tid >> 6);
    const unsigned lds0 = (unsigned)(uintptr_t)lds;
    gu32* hcnt = (gu32*)(ws + WS_CNT) + tt * 64; gu32* ocnt = (gu32*)(ws + WS_CNT) + (32 + (tt >> 2)) * 64;
    STAMP(1, 0);
    if (wid >= 4) {
        const int l = wid - 4;
        if (ct < 7) {
            const char* ag = ws + WS_Z2N + (size_t)ct * 32 * 16384 + l * 4096;
            const char* bg = (const char*)wsp + (size_t)(tt * 64 + l * 16) * 4096;
            const unsigned avoff = lane * 16;
            const unsigned bv0 = (unsigned)((lane >> 3) * 4096 + (((lane & 7) ^ (lane >> 4)) * 16)), bv1 = (unsigned)((8 + (lane >> 3)) * 4096 + (((lane & 7) ^ (4 + (lane >> 4))) * 16));
            auto dma = [&](int kt, int slot) __attribute__((always_inline)) { const unsigned d = lds0 + slot * G_STAGE; const char* a_ = ag + (size_t)kt * 16384;
#pragma unroll
                for (int e = 0; e < 4; ++e) glds16s(avoff, a_ + e * 1024, d + l * 4096 + e * 1024);
                glds16s(bv0, bg + kt * 128, d + 16384 + l * 2048); glds16s(bv1, bg + kt * 128, d + 16384 + l * 2048 + 1024); };
            dma(0, 0);
            WAIT_BAR(0);
            dma(1, 1); dma(2, 2);
            int slot = 0;
            for (int kt = 1; kt < 32; ++kt) {
                if (kt < 31) WAIT_BAR(6); else WAIT_BAR(0);
                if (kt + 2 < 32) dma(kt + 2, slot);
                slot = slot == 2 ? 0 : slot + 1;
            }
            LBAR();
        }
        unsigned wv[4];
#pragma unroll
        for (int e = 0; e < 4; ++e) { const int row = l * 32 + e * 8 + (lane >> 3); wv[e] = (unsigned)(row * 1024 + ct * 128 + (((lane & 7) ^ (((row >> 1) & 1) << 2)) * 16)); }
        auto dmw = [&](const u16* w, int st) __attribute__((always_inline)) { const char* wg = (const char*)w + (size_t)st * 128 * 1024; const unsigned d = lds0 + (st & 3) * W_STAGE + l * 4096;
#pragma unroll
            for (int e = 0; e < 4; ++e) glds16s(wv[e], wg, d + e * 1024); };
        dmw(wout, 0); dmw(wout, 1); dmw(wout, 2); dmw(wout, 3);
        if (l == 0) { wait_cnt(hcnt, 7);
        }
        WAIT_BAR(12);
#pragma unroll
        for (int st = 1; st < 7; ++st) {
            if (st < 4) WAIT_BAR(8); else if (st == 4) WAIT_BAR(8); else if (st == 5) WAIT_BAR(4); else WAIT_BAR(0);
            if (st + 3 < 7) dmw(wout, st + 3);
        }
        LBAR();
        dmw(wo, 0); dmw(wo, 1); dmw(wo, 2); dmw(wo, 3);
        if (l == 0) { wait_cnt(ocnt, 32);
        }
        WAIT_BAR(0);
        return;
    }
#define GPRIO 2
    __builtin_amdgcn_s_setprio(GPRIO);
    const int g = lane >> 4, qq = (lane & 15) >> 2, pp = lane & 3;
    const int th = wid & 1, fh = wid >> 1;
    const int trow = 32 * th + r32, t = tt * 64 + trow;
    if (ct < 7) {
        f32x16 acc0 = {}, acc1 = {};
        const unsigned aoff = (unsigned)(fh * 8192 + lane * 16);
        unsigned boff[4];
#pragma unroll
        for (int ks = 0; ks < 4; ++ks) boff[ks] = (unsigned)(16384 + trow * 128 + (((2 * ks + hi) ^ ((trow >> 1) & 7)) * 16));
        bf16x8 fa0[2], fa1[2], fb[2];
        auto rd = [&](int set, lds_cptr sb, int ks) __attribute__((always_inline)) {
            fb[set] = ldsr128(sb + boff[ks]);
            fa0[set] = ldsr128(sb + aoff + ks * 1024);
            fa1[set] = ldsr128(sb + aoff + 4096 + ks * 1024); };
        auto mm = [&](int set) __attribute__((always_inline)) { };
#define GSTEP(cur, nxt, sbn, ksn) do { SBAR(); acc0 = MFMA32(fa0[cur], fb[cur], acc0); SBAR(); rd(nxt, sbn, ksn); SBAR(); acc1 = MFMA32(fa1[cur], fb[cur], acc1); SBAR(); } while (0)
        const float bias = bflo(bsp[t]);
        u32x2 z[2][4];
#pragma unroll
        for (int i = 0; i < 2; ++i)
#pragma unroll
            for (int q = 0; q < 4; ++q) z[i][q] = *(const u32x2*)((const u16*)(ws + WS_Z1) + ((size_t)((t >> 5) * 28 + ct * 4 + 2 * fh + i) * 1024) + (hi * 32 + r32) * 4 + q * 256);
        LBAR();
        rd(0, (lds_cptr)lds, 0);
        int slot = 0;
        for (int kt = 0; kt < 32; ++kt) {
            const lds_cptr sb = (lds_cptr)lds + slot * G_STAGE;
            GSTEP(0, 1, sb, 1);
            GSTEP(1, 0, sb, 2);
            GSTEP(0, 1, sb, 3);
            slot = slot == 2 ? 0 : slot + 1;
            if (kt < 31) { LBAR(); GSTEP(1, 0, (lds_cptr)lds + slot * G_STAGE, 0); }
            else { acc0 = MFMA32(fa0[1], fb[1], acc0); acc1 = MFMA32(fa1[1], fb[1], acc1); }
        }
        STAMP(1, 2);
#pragma unroll
        for (int i = 0; i < 2; ++i) {
            u16* hp = (u16*)(ws + WS_H) + ((size_t)((t >> 5) * 56 + (ct * 4 + 2 * fh + i) * 2) * 512) + r32 * 8 + hi * 4;
#pragma unroll
            for (int q = 0; q < 4; ++q) {
                float v[4];
#pragma unroll
                for (int r = 0; r < 4; ++r) v[r] = (i ? acc1[4 * q + r] : acc0[4 * q + r]) + bias;
                st8_wt(hp + (q >> 1) * 512 + (q & 1) * 256, (u32x2){cvtpk(v[0] * bflo(z[i][q][0]), v[1] * bfhi(z[i][q][0])), cvtpk(v[2] * bflo(z[i][q][1]), v[3] * bfhi(z[i][q][1]))});
            }
        }
        asm volatile("s_waitcnt vmcnt(0) lgkmcnt(0)\n\ts_barrier" ::: "memory");
        if (tid == 0) __hip_atomic_fetch_add(hcnt, 1u, RLX_AGENT);
    }
    STAMP(1, 3);
    const int kh = fh;
    const int kr = 8 * (g >> 1) + qq;
    unsigned wof[2][2];
#pragma unroll
    for (int n = 0; n < 2; ++n) { wof[n][0] = (unsigned)((64 * kh + kr) * 128 + ((n ^ ((kr >> 1) & 1)) * 64) + (g & 1) * 32 + pp * 8); wof[n][1] = wof[n][0] + 512; }
    auto gemm = [&](const __amdgpu_buffer_rsrc_t tr, auto NST, bool bars, f32x16& c0, f32x16& c1) __attribute__((always_inline)) {
        constexpr int N = decltype(NST)::value;
        u32x4 tf[2][4];
#pragma unroll
        for (int i = 0; i < 4; ++i) tf[0][i] = __builtin_amdgcn_raw_buffer_load_b128(tr, lane * 16 + (4 * kh + i) * 1024, 0, H_AUX);
#pragma unroll
        for (int st = 0; st < N; ++st) {
            if (st + 1 < N) {
#pragma unroll
                for (int i = 0; i < 4; ++i) tf[(st + 1) & 1][i] = __builtin_amdgcn_raw_buffer_load_b128(tr, lane * 16 + ((st + 1) * 8 + 4 * kh + i) * 1024, 0, H_AUX); SBAR(); }
            if (bars && st > 0) LBAR();
            const lds_cptr sb = (lds_cptr)lds + (st & 3) * W_STAGE;
#pragma unroll
            for (int i = 0; i < 4; ++i) {
                const bf16x8 w0 = cat(ldstr(sb + wof[0][0] + i * 2048), ldstr(sb + wof[0][1] + i * 2048)), w1 = cat(ldstr(sb + wof[1][0] + i * 2048), ldstr(sb + wof[1][1] + i * 2048));
                c0 = MFMA32(w0, BF(tf[st & 1][i]), c0); c1 = MFMA32(w1, BF(tf[st & 1][i]), c1);
            }
        }
    };
    auto merge = [&](f32x16& c0, f32x16& c1) __attribute__((always_inline)) {
        f32x4* dst = (f32x4*)(lds + G_RED + (wid ^ 2) * 4096) + lane;
#pragma unroll
        for (int q = 0; q < 4; ++q) dst[q * 64] = kh ? (f32x4){c0[4 * q], c0[4 * q + 1], c0[4 * q + 2], c0[4 * q + 3]} : (f32x4){c1[4 * q], c1[4 * q + 1], c1[4 * q + 2], c1[4 * q + 3]};
    };
    f32x16 gb0 = {}, gb1 = {};
    LBAR();
    gemm(__builtin_amdgcn_make_buffer_rsrc((void*)(ws + WS_H + (size_t)(tt * 2 + th) * 57344), 0, 57344, 0x00020000), std::integral_constant<int, 7>{}, true, gb0, gb1);
    merge(gb0, gb1);
    LBAR();
    { char* sc = lds + G_RED + wid * 4096;
      float gv[16];
#pragma unroll
      for (int q = 0; q < 4; ++q) { const f32x4 o4 = ((const f32x4*)sc)[q * 64 + lane];
#pragma unroll
          for (int r = 0; r < 4; ++r) { const float bb = (kh ? gb1[4 * q + r] : gb0[4 * q + r]) + o4[r]; gv[4 * q + r] = bb * __builtin_amdgcn_rcpf(1.0f + EX(-LOG2E * bb)); } }
      asm volatile("s_waitcnt lgkmcnt(0)" ::: "memory");
#pragma unroll
      for (int u = 0; u < 2; ++u) ((u32x4*)sc)[u * 64 + lane] = (u32x4){cvtpk(gv[8 * u], gv[8 * u + 1]), cvtpk(gv[8 * u + 2], gv[8 * u + 3]), cvtpk(gv[8 * u + 4], gv[8 * u + 5]), cvtpk(gv[8 * u + 6], gv[8 * u + 7])}; }
    const int n0 = ct * 64 + kh * 32 + 4 * hi;
    u32x2 xv[4];
#pragma unroll
    for (int q = 0; q < 4; ++q) xv[q] = *(const u32x2*)(x + (size_t)t * D + n0 + 8 * q);
    PIN(xv[0]); PIN(xv[1]); PIN(xv[2]); PIN(xv[3]);
    STAMP(1, 4);
    LBAR();
    STAMP(1, 5);
    f32x16 s0 = {}, s1 = {};
    {
        const __amdgpu_buffer_rsrc_t tr = __builtin_amdgcn_make_buffer_rsrc((void*)(ws + WS_O + (size_t)(tt * 2 + th) * 32768), 0, 32768, 0x00020000);
        u32x4 tf[16];
#pragma unroll
        for (int i = 0; i < 16; ++i) tf[i] = __builtin_amdgcn_raw_buffer_load_b128(tr, lane * 16 + ((i >> 2) * 8 + 4 * kh + (i & 3)) * 1024, 0, O_AUX);
        SBAR();
        bf16x8 w0[2], w1[2];
        w0[0] = cat(ldstr((lds_cptr)lds + wof[0][0]), ldstr((lds_cptr)lds + wof[0][1])); w1[0] = cat(ldstr((lds_cptr)lds + wof[1][0]), ldstr((lds_cptr)lds + wof[1][1]));
#pragma unroll
        for (int i = 0; i < 16; ++i) {
            if (i + 1 < 16) { const lds_cptr sb = (lds_cptr)lds + ((i + 1) >> 2) * W_STAGE + ((i + 1) & 3) * 2048;
                w0[(i + 1) & 1] = cat(ldstr(sb + wof[0][0]), ldstr(sb + wof[0][1])); w1[(i + 1) & 1] = cat(ldstr(sb + wof[1][0]), ldstr(sb + wof[1][1])); }
            s0 = MFMA32(w0[i & 1], BF(tf[i]), s0); s1 = MFMA32(w1[i & 1], BF(tf[i]), s1);
        }
    }
    STAMP(1, 7);
    u32x4 gq[2];
    { const u32x4* gt = (const u32x4*)(lds + G_RED + wid * 4096) + lane; gq[0] = gt[0]; gq[1] = gt[64]; }
    LBAR();
    { f32x4* dst = (f32x4*)(lds + (wid ^ 2) * 4096) + lane;
#pragma unroll
      for (int q = 0; q < 4; ++q) dst[q * 64] = kh ? (f32x4){s0[4 * q], s0[4 * q + 1], s0[4 * q + 2], s0[4 * q + 3]} : (f32x4){s1[4 * q], s1[4 * q + 1], s1[4 * q + 2], s1[4 * q + 3]}; }
    LBAR();
    { const f32x4* src = (const f32x4*)(lds + wid * 4096) + lane;
#pragma unroll
      for (int q = 0; q < 4; ++q) { const f32x4 o4 = src[q * 64]; float v[4]; const unsigned g0 = gq[q >> 1][2 * (q & 1)], g1 = gq[q >> 1][2 * (q & 1) + 1]; const float gg[4] = {bflo(g0), bfhi(g0), bflo(g1), bfhi(g1)};
#pragma unroll
          for (int r = 0; r < 4; ++r) v[r] = ((kh ? s1[4 * q + r] : s0[4 * q + r]) + o4[r]) * gg[r];
          *(u32x2*)(out + (size_t)t * D + n0 + 8 * q) = (u32x2){cvtpk(bflo(xv[q][0]) + v[0], bfhi(xv[q][0]) + v[1]), cvtpk(bflo(xv[q][1]) + v[2], bfhi(xv[q][1]) + v[3])}; } }
    STAMP(1, 6);
}

__global__ __launch_bounds__(512, 4) void k2_mix(const u16* __restrict__ x, const u16* __restrict__ wo, const u16* __restrict__ wsp, const u16* __restrict__ bsp, const u16* __restrict__ wout, char* __restrict__ ws, u16* __restrict__ out) {
    extern __shared__ __attribute__((aligned(16))) char lds[];
    if (blockIdx.x < 256) attn_role(blockIdx.x, ws, lds);
    if (blockIdx.x >= 256) gate_role(blockIdx.x - 256, x, wo, wsp, bsp, wout, ws, out, lds);
}

extern "C" void kernel_launch(void* const* d_in, const int* in_sizes, int n_in, void* d_out, int out_size, void* d_ws, size_t ws_size, hipStream_t stream) {
    const u16* x = (const u16*)d_in[0]; const u16* wqkv = (const u16*)d_in[1]; const u16* wo = (const u16*)d_in[2];
    const u16* win = (const u16*)d_in[4]; const u16* wsp = (const u16*)d_in[5]; const u16* bsp = (const u16*)d_in[6]; const u16* wout = (const u16*)d_in[7];
    char* ws = (char*)d_ws;
    (void)hipFuncSetAttribute((const void*)k1_proj, hipFuncAttributeMaxDynamicSharedMemorySize, K1_LDS);
    (void)hipFuncSetAttribute((const void*)k2_mix, hipFuncAttributeMaxDynamicSharedMemorySize, K2_LDS);
    k1_proj<<<256, 768, K1_LDS, stream>>>(x, wqkv, win, ws);
    k2_mix<<<512, 512, K2_LDS, stream>>>(x, wo, wsp, bsp, wout, ws, (u16*)d_out);
}
```

```cpp
#include <hip/hip_runtime.h>
#include <hip/hip_bf16.h>
#include <stdint.h>
#include <type_traits>

typedef unsigned short u16;
using bf16x8 = __attribute__((ext_vector_type(8))) short;
using s16x4 = __attribute__((ext_vector_type(4))) short;
using f32x16 = __attribute__((ext_vector_type(16))) float;
using f32x4 = __attribute__((ext_vector_type(4))) float;
using u32x4 = __attribute__((ext_vector_type(4))) unsigned;
using u32x2 = __attribute__((ext_vector_type(2))) unsigned;
typedef __bf16 bf16x2_t __attribute__((ext_vector_type(2)));
typedef float f32x2_t __attribute__((ext_vector_type(2)));
typedef short v4i16_t __attribute__((ext_vector_type(4)));
#define LDSAS __attribute__((address_space(3)))
typedef LDSAS const char* lds_cptr;
#define MFMA32(a, b, c) __builtin_amdgcn_mfma_f32_32x32x16_bf16(a, b, c, 0, 0, 0)
#define MFMA16(a, b, c) __builtin_amdgcn_mfma_f32_16x16x32_bf16(a, b, c, 0, 0, 0)
#define SBAR() __builtin_amdgcn_sched_barrier(0)
#define PIN(x) asm volatile("" : "+v"(x))
#define WAIT_BAR(N) asm volatile("s_waitcnt vmcnt(" #N ") lgkmcnt(0)\n\ts_barrier" ::: "memory")
#define LBAR() asm volatile("s_waitcnt lgkmcnt(0)\n\ts_barrier" ::: "memory")
#define DI __device__ __forceinline__
#define ST8(p, w) st8_wt((void*)(p), (w))
#define ST16(off, v) __builtin_amdgcn_raw_buffer_store_b128((v), wsr, (off), 0, 16)
#define STAMP(kern, i) do { } while (0)

constexpr int S = 2048, D = 512, H = 32, DH = 16, FH = 896;
constexpr float LOG2E = 1.4426950408889634f;

constexpr size_t WS_Q = 0, WS_K = 2u << 20, WS_V = 4u << 20, WS_Z1 = 6u << 20, WS_Z2R = 10u << 20, WS_Z2N = 14u << 20, WS_H = 18u << 20, WS_O = 22u << 20, WS_KMAX = 24u << 20, WS_EPOCH = 25u << 20, WS_XCH = 26u << 20, WS_XST = 27u << 20, WS_CNT = 28u << 20;

DI unsigned cvtpk(float lo, float hi) { f32x2_t v = {lo, hi}; return __builtin_bit_cast(unsigned, __builtin_convertvector(v, bf16x2_t)); }
DI float bflo(unsigned w) { return __uint_as_float(w << 16); }
DI float bfhi(unsigned w) { return __uint_as_float(w & 0xFFFF0000u); }
DI float dot2(unsigned a, unsigned b, float c) { return __builtin_amdgcn_fdot2_f32_bf16(__builtin_bit_cast(bf16x2_t, a), __builtin_bit_cast(bf16x2_t, b), c, false); }
DI void glds16s(unsigned voff, const void* sbase, unsigned lds_base) {
    lds_base = __builtin_amdgcn_readfirstlane(lds_base);
    unsigned sv; asm volatile("s_mov_b32 %0, m0\n\ts_mov_b32 m0, %3\n\ts_nop 0\n\tglobal_load_lds_dwordx4 %1, %2\n\ts_mov_b32 m0, %0" : "=&s"(sv) : "v"(voff), "s"(sbase), "s"(lds_base) : "memory"); }
DI bf16x8 ldsr128(lds_cptr p) { return *(const LDSAS bf16x8*)p; }
DI s16x4 ldstr(lds_cptr p) { return __builtin_bit_cast(s16x4, __builtin_amdgcn_ds_read_tr16_b64_v4i16((LDSAS v4i16_t*)p)); }
DI bf16x8 cat(s16x4 a, s16x4 b) { return __builtin_shufflevector(a, b, 0, 1, 2, 3, 4, 5, 6, 7); }
DI float swap32(float x, bool sum) { auto rr = __builtin_amdgcn_permlane32_swap(__float_as_uint(x), __float_as_uint(x), false, false); return sum ? __uint_as_float(rr[0]) + __uint_as_float(rr[1]) : __uint_as_float(rr[0]); }
DI float gelu_t(float x) {
    const float c1 = -2.0f * 0.7978845608028654f * LOG2E, c3 = c1 * 0.044715f;
    const float u = x * fmaf(x * x, c3, c1);
    return x * __builtin_amdgcn_rcpf(1.0f + __builtin_amdgcn_exp2f(u));
}

constexpr int K1_XB = 16384;
constexpr int K1_STAGE = K1_XB + 7 * 4096;
constexpr int K1_CS = 3 * K1_STAGE;
constexpr int K1_LDS = K1_CS + 1024;
typedef __attribute__((address_space(1))) unsigned gu32; typedef __attribute__((address_space(1))) unsigned long long gu64;
#define RLX_AGENT __ATOMIC_RELAXED, __HIP_MEMORY_SCOPE_AGENT

DI float xor16(float x) { auto r = __builtin_amdgcn_permlane16_swap(__float_as_uint(x), __float_as_uint(x), false, false); return __uint_as_float(r[0]) + __uint_as_float(r[1]); }
DI float xor32(float x) { auto r = __builtin_amdgcn_permlane32_swap(__float_as_uint(x), __float_as_uint(x), false, false); return __uint_as_float(r[0]) + __uint_as_float(r[1]); }
DI float max16(float x) { auto r = __builtin_amdgcn_permlane16_swap(__float_as_uint(x), __float_as_uint(x), false, false); return fmaxf(__uint_as_float(r[0]), __uint_as_float(r[1])); }
DI float max32(float x) { auto r = __builtin_amdgcn_permlane32_swap(__float_as_uint(x), __float_as_uint(x), false, false); return fmaxf(__uint_as_float(r[0]), __uint_as_float(r[1])); }
template <int CTRL> DI float dppf(float x) { return __uint_as_float(__builtin_amdgcn_update_dpp(0, __float_as_uint(x), CTRL, 0xF, 0xF, false)); }
DI float rowmax16(float x) { x = fmaxf(x, dppf<0x128>(x)); x = fmaxf(x, dppf<0x124>(x)); x = fmaxf(x, dppf<0x122>(x)); x = fmaxf(x, dppf<0x121>(x)); return x; }
DI float wavesum(float x) { x += dppf<0x128>(x); x += dppf<0x124>(x); x += dppf<0x122>(x); x += dppf<0x121>(x); return xor32(xor16(x)); }
DI u32x4 widen(u32x2 a, u32x2 b) { auto r0 = __builtin_amdgcn_permlane32_swap(a[0], b[0], false, false); auto r1 = __builtin_amdgcn_permlane32_swap(a[1], b[1], false, false); return (u32x4){r0[0], r1[0], r0[1], r1[1]}; }

DI void st8_wt(void* p, u32x2 w) { __hip_atomic_store((gu64*)p, ((unsigned long long)w[1] << 32) | w[0], RLX_AGENT); }
__global__ __launch_bounds__(768, 3) void k1_proj(const u16* __restrict__ x, const u16* __restrict__ wqkv, const u16* __restrict__ win, char* __restrict__ ws) {
    extern __shared__ __attribute__((aligned(16))) char lds[];
    const int tid = threadIdx.x, lane = tid & 63, r32 = lane & 31, hi = lane >> 5;
    const int wid = __builtin_amdgcn_readfirstlane(tid >> 6);
    const int b = blockIdx.x, xcd = b & 7, idx = b >> 3;
    const int panel = 8 * (xcd >> 2) + (idx & 7), j = 4 * (xcd & 3) + (idx >> 3);
    const unsigned lds0 = (unsigned)(uintptr_t)lds;
    const int jq = j & 3, u7 = 7 * (j >> 2);
    const int n1 = jq == 3 ? 1 : 2, n2 = jq == 1 ? 1 : 2;
    const int z1t0 = u7 + 2 * jq, z2t0 = u7 + (jq == 0 ? 0 : (jq == 1 ? 2 : (jq == 2 ? 3 : 5)));
    STAMP(0, 0);
    if (b == 0 && tid < 40) ((unsigned*)(ws + WS_CNT))[tid * 64] = 0u;
    if (wid >= 8) {
        const int l = wid - 8;
        const char* xb = (const char*)x + (size_t)(panel * 128 + l * 32) * 1024;
        const unsigned xv0 = (unsigned)((lane >> 3) * 1024 + (((lane & 7) ^ (lane >> 4)) * 16)), xv1 = (unsigned)((lane >> 3) * 1024 + (((lane & 7) ^ (4 + (lane >> 4))) * 16));
        const unsigned vq = (unsigned)((16 * l + (lane >> 2)) * 3072 + (lane & 3) * 16), vz = (unsigned)((16 * l + (lane >> 2)) * 3584 + (lane & 3) * 16);
        const char* wq = (const char*)wqkv + j * 64;
        int zc[4];
#pragma unroll
        for (int i = 0; i < 4; ++i) { const int ii = i < n1 + n2 ? i : n1 + n2 - 1; zc[i] = ii < n1 ? (z1t0 + ii) * 64 : FH * 2 + (z2t0 + ii - n1) * 64; }
        auto dma = [&](int st) __attribute__((always_inline)) {
            const unsigned d = lds0 + (st % 3) * K1_STAGE;
#pragma unroll
            for (int p = 0; p < 4; ++p) glds16s((p & 1) ? xv1 : xv0, xb + st * 128 + p * 8192, d + l * 4096 + p * 1024);
            const char* bq = wq + (size_t)st * 64 * 3072; const char* bz = (const char*)win + (size_t)st * 64 * 3584; const unsigned dw = d + K1_XB + l * 1024;
            glds16s(vq, bq + 2048, dw); glds16s(vq, bq, dw + 4096); glds16s(vq, bq + 1024, dw + 8192);
#pragma unroll
            for (int i = 0; i < 4; ++i) glds16s(vz, bz + zc[i], dw + (3 + i) * 4096);
        };
        dma(0);
        WAIT_BAR(0);
        dma(1); dma(2);
#pragma unroll
        for (int st = 1; st < 8; ++st) {
            if (st < 7) WAIT_BAR(11); else WAIT_BAR(0);
            if (st + 2 < 8) dma(st + 2);
        }
        {
            if ((idx & 3) == 0) {
                unsigned long long pc;
                asm volatile("s_getpc_b64 s[98:99]\n\ts_add_u32 s98, s98, _Z6k2_mixPKtS0_S0_S0_S0_PcPt@rel32@lo+4\n\ts_addc_u32 s99, s99, _Z6k2_mixPKtS0_S0_S0_S0_PcPt@rel32@hi+12\n\ts_mov_b64 %0, s[98:99]" : "=s"(pc) :: "scc", "s98", "s99");
                const u32x4* cp = (const u32x4*)(pc + (size_t)l * 4096) + lane;
                u32x4 a = {0u, 0u, 0u, 0u};
#pragma unroll
                for (int i = 0; i < 4; ++i) a ^= cp[i * 64];
                asm volatile("" :: "v"(a));
            }
        }
        return;
    }
    const int mw = wid & 3, nh = wid >> 2;
    gu32* epw = (gu32*)(ws + WS_EPOCH) + panel * 64;
    unsigned tag = __hip_atomic_load(epw, RLX_AGENT);
    u32x4 xr = *(const u32x4*)(x + (size_t)(panel * 128 + 8 * j + wid) * D + lane * 8);
    auto rowstats = [&]() __attribute__((always_inline)) {
        PIN(xr); PIN(tag); tag += 1u;
        float v[8];
#pragma unroll
        for (int i = 0; i < 4; ++i) { v[2 * i] = bflo(xr[i]); v[2 * i + 1] = bfhi(xr[i]); }
        const float m = wavesum(((v[0] + v[1]) + (v[2] + v[3])) + ((v[4] + v[5]) + (v[6] + v[7]))) * (1.0f / 512.0f);
        float q = 0.f;
#pragma unroll
        for (int i = 0; i < 8; ++i) q = fmaf(v[i] - m, v[i] - m, q);
        const float r = __builtin_amdgcn_rsqf(wavesum(q) * (1.0f / 512.0f) + 1e-5f);
        if (lane < 2) __hip_atomic_store((gu64*)(ws + WS_XST) + (size_t)(panel * 128 + 8 * j + wid) * 2 + lane, ((unsigned long long)tag << 32) | __float_as_uint(lane ? r : m), RLX_AGENT);
    };
    f32x16 acc[4];
#pragma unroll
    for (int s = 0; s < 4; ++s) acc[s] = (f32x16){};
    float cs0 = 0.f, cs1 = 0.f;
    const int g = lane >> 4, qq = (lane & 15) >> 2, pp = lane & 3;
    const int trow = 32 * mw + r32;
    unsigned xo[4];
#pragma unroll
    for (int ks = 0; ks < 4; ++ks) xo[ks] = (unsigned)(trow * 128 + (((2 * ks + hi) ^ ((trow >> 1) & 7)) * 16));
    const unsigned wlane = (unsigned)(K1_XB + (8 * (g >> 1) + qq) * 64 + (g & 1) * 32 + pp * 8);
    const __amdgpu_buffer_rsrc_t sr = __builtin_amdgcn_make_buffer_rsrc((void*)(ws + WS_XST + (size_t)panel * 128 * 16), 0, 128 * 16, 0x00020000);
    u32x4 pst;
    int tile[4];
    auto mainloop = [&](auto NHC) __attribute__((always_inline)) {
        constexpr int NHV = decltype(NHC)::value; constexpr int NS = NHV == 0 ? 4 : 3;
        unsigned wo[NS];
#pragma unroll
        for (int s = 0; s < NS; ++s) { int t = s + mw; t = t >= NS ? t - NS : t; t = t >= NS ? t - NS : t; tile[s] = 4 * NHV + t; wo[s] = wlane + tile[s] * 4096; }
        bf16x8 xa[2], wf[2][NS];
        auto rd = [&](const int set, const lds_cptr sb, const int ks) __attribute__((always_inline)) {
            xa[set] = ldsr128(sb + xo[ks]);
#pragma unroll
            for (int s = 0; s < NS; ++s) wf[set][s] = cat(ldstr(sb + wo[s] + ks * 1024), ldstr(sb + wo[s] + ks * 1024 + 256));
        };
        auto mm = [&](const int set) __attribute__((always_inline)) {
            SBAR();
#pragma unroll
            for (int s = 0; s < NS; ++s) acc[s] = MFMA32(wf[set][s], xa[set], acc[s]);
            const u32x4 w = __builtin_bit_cast(u32x4, wf[set][0]);
#pragma unroll
            for (int i = 0; i < 4; ++i) { cs0 += bflo(w[i]); cs1 += bfhi(w[i]); }
            PIN(cs0); PIN(cs1);
#pragma unroll
            for (int i = 0; i < NS; ++i) { __builtin_amdgcn_sched_group_barrier(0x008, 1, 0); __builtin_amdgcn_sched_group_barrier(0x002, NS == 4 ? 4 : 6, 0); }
            SBAR();
        };
        LBAR();
        STAMP(0, 1);
        rd(0, (lds_cptr)lds, 0);
#pragma unroll
        for (int st = 0; st < 8; ++st) {
            const lds_cptr sb = (lds_cptr)lds + (st % 3) * K1_STAGE;
            rd(1, sb, 1); mm(0);
            rd(0, sb, 2); mm(1);
            rd(1, sb, 3); mm(0);
            if (st < 7) { LBAR(); rd(0, (lds_cptr)lds + ((st + 1) % 3) * K1_STAGE, 0); }
            if (st == 1) rowstats();
            if (st == 5) pst = __builtin_amdgcn_raw_buffer_load_b128(sr, trow * 16, 0, 16);
            mm(1);
        }
    };
    if (nh == 0) mainloop(std::integral_constant<int, 0>{}); else mainloop(std::integral_constant<int, 1>{});
    STAMP(0, 2);
    float* csl = (float*)(lds + K1_CS);
    { const float c = xor32(cs0 + cs1); if (hi == 0) csl[tile[0] * 32 + r32] = c; }
    const int tok = panel * 128 + trow;
    float mu, rs;
    {   u32x4 p = pst;
        for (unsigned spins = 0; !__all((p[1] == tag) & (p[3] == tag)) && spins < 200000u; ++spins) {
            __builtin_amdgcn_s_sleep(1);
            p = __builtin_amdgcn_raw_buffer_load_b128(sr, trow * 16, 0, 16);
        }
        mu = __uint_as_float(p[0]); rs = __uint_as_float(p[2]); }
    LBAR();
    STAMP(0, 3);
    auto lnv = [&](int s, float (&v)[16], float sc) __attribute__((always_inline)) {
#pragma unroll
        for (int q = 0; q < 4; ++q) { const f32x4 c = *(const f32x4*)(csl + tile[s] * 32 + 8 * q + 4 * hi);
#pragma unroll
            for (int r = 0; r < 4; ++r) v[4 * q + r] = sc * (acc[s][4 * q + r] - mu * c[r]); } };
    const __amdgpu_buffer_rsrc_t wsr = __builtin_amdgcn_make_buffer_rsrc((void*)ws, 0, 32u << 20, 0x00020000);
    u16* Z1 = (u16*)(ws + WS_Z1);
    auto z1store = [&](int zt, const u32x2 (&w)[4]) __attribute__((always_inline)) {
        u16* p = Z1 + ((size_t)((tok >> 5) * 28 + zt) * 1024) + (hi * 32 + r32) * 4;
#pragma unroll
        for (int q = 0; q < 4; ++q) ST8(p + q * 256, w[q]); };
    auto gelu16 = [&](int s, u32x2 (&w)[4]) __attribute__((always_inline)) { float v[16]; lnv(s, v, rs);
#pragma unroll
        for (int q = 0; q < 4; ++q) w[q] = (u32x2){cvtpk(gelu_t(v[4 * q]), gelu_t(v[4 * q + 1])), cvtpk(gelu_t(v[4 * q + 2]), gelu_t(v[4 * q + 3]))}; };
    if (nh == 0) {
#pragma unroll
        for (int s = 0; s < 4; ++s) {
            const int T = tile[s];
            if (T == 3) { u32x2 w[4]; gelu16(s, w); z1store(z1t0, w); }
            else {
                float v[16]; lnv(s, v, T == 1 ? rs * (0.25f * LOG2E) : rs);
                u32x2 w[4];
#pragma unroll
                for (int q = 0; q < 4; ++q) w[q] = (u32x2){cvtpk(v[4 * q], v[4 * q + 1]), cvtpk(v[4 * q + 2], v[4 * q + 3])};
                if (T == 0) {
                    char* sc = lds + mw * 2048;
#pragma unroll
                    for (int q = 0; q < 4; ++q) *(u32x2*)(sc + r32 * 64 + (8 * q + 4 * hi) * 2) = w[q];
                    asm volatile("s_waitcnt lgkmcnt(0)" ::: "memory");
                    u16* Vt = (u16*)(ws + WS_V);
#pragma unroll
                    for (int hh = 0; hh < 2; ++hh)
#pragma unroll
                        for (int ks = 0; ks < 2; ++ks) {
                            const s16x4 t4 = ldstr((lds_cptr)lds + mw * 2048 + (16 * ks + 8 * (g >> 1) + 4 * (g & 1) + qq) * 64 + hh * 32 + pp * 8);
                            ST8(Vt + ((size_t)(2 * j + hh) * 128 + ((panel * 128 + 32 * mw) >> 4) + ks) * 256 + (g & 1) * 128 + (lane & 15) * 8 + (g >> 1) * 4, __builtin_bit_cast(u32x2, t4)); }
                } else {
                    const unsigned dsto = (unsigned)(T == 1 ? WS_Q : WS_K);
#pragma unroll
                    for (int hh = 0; hh < 2; ++hh) ST16(dsto + (((2 * j + hh) * S + tok) * 16 + 8 * hi) * 2, widen(w[2 * hh], w[2 * hh + 1]));
                    if (T == 2) {
                        float* kmax = (float*)(ws + WS_KMAX);
#pragma unroll
                        for (int hh = 0; hh < 2; ++hh) { float n = 0.f;
#pragma unroll
                            for (int q = 0; q < 2; ++q) { const u32x2 ww = w[2 * hh + q]; n = fmaf(bflo(ww[0]), bflo(ww[0]), n); n = fmaf(bfhi(ww[0]), bfhi(ww[0]), n); n = fmaf(bflo(ww[1]), bflo(ww[1]), n); n = fmaf(bfhi(ww[1]), bfhi(ww[1]), n); }
                            n = max16(rowmax16(xor32(n)));
                            if (lane == 0) kmax[(panel * 4 + mw) * H + 2 * j + hh] = n; }
                    }
                }
            }
        }
    } else {
        __builtin_amdgcn_s_setprio(2);
        float sm = 0.f;
#pragma unroll
        for (int s = 0; s < 3; ++s) { const int zi = tile[s] - 3;
            if (zi >= n1 && zi < n1 + n2) { float v[16]; lnv(s, v, rs);
#pragma unroll
                for (int r = 0; r < 16; ++r) { acc[s][r] = gelu_t(v[r]); sm += acc[s][r]; } } }
        sm = xor32(sm);
        const float ml = sm * (n2 == 2 ? (1.0f / 64.f) : (1.0f / 32.f)); float m2 = 0.f;
#pragma unroll
        for (int s = 0; s < 3; ++s) { const int zi = tile[s] - 3;
            if (zi >= n1 && zi < n1 + n2) {
#pragma unroll
                for (int r = 0; r < 16; ++r) { const float d = acc[s][r] - ml; m2 = fmaf(d, d, m2); } } }
        m2 = xor32(m2);
        __hip_atomic_store((gu64*)(ws + WS_XCH) + ((size_t)(panel * 16 + j) * 128 + trow) * 2 + hi, ((unsigned long long)tag << 32) | __float_as_uint(hi ? m2 : sm), RLX_AGENT);
        __builtin_amdgcn_s_setprio(0);
        STAMP(0, 4);
#pragma unroll
        for (int s = 0; s < 3; ++s) { const int zi = tile[s] - 3; if (zi < n1) { u32x2 w[4]; gelu16(s, w); z1store(z1t0 + zi, w); } }
        float mean, rstd;
        {
            const __amdgpu_buffer_rsrc_t xr = __builtin_amdgcn_make_buffer_rsrc((void*)(ws + WS_XCH + (size_t)panel * 16 * 128 * 16), 0, 16 * 128 * 16, 0x00020000);
            u32x4 p[8];
            for (unsigned spins = 0;; ++spins) {
                bool ok = true;
#pragma unroll
                for (int u = 0; u < 8; ++u) p[u] = __builtin_amdgcn_raw_buffer_load_b128(xr, ((8 * hi + u) * 128 + trow) * 16, 0, 16);
#pragma unroll
                for (int u = 0; u < 8; ++u) ok &= (p[u][1] == tag) & (p[u][3] == tag);
                if (__all(ok) || spins > 200000u) break;
                __builtin_amdgcn_s_sleep(1);
            }
            float tot = 0.f;
#pragma unroll
            for (int u = 0; u < 8; ++u) tot += __uint_as_float(p[u][0]);
            mean = xor32(tot) * (1.0f / FH);
            float mm2 = 0.f;
#pragma unroll
            for (int u = 0; u < 8; ++u) { const float nl = (u & 3) == 1 ? 32.f : 64.f; const float d = __uint_as_float(p[u][0]) * ((u & 3) == 1 ? (1.0f / 32.f) : (1.0f / 64.f)) - mean; mm2 += __uint_as_float(p[u][2]) + nl * d * d; }
            rstd = __builtin_amdgcn_rsqf(xor32(mm2) * (1.0f / FH) + 1e-5f);
        }
        STAMP(0, 5);
        if (wid == 4 && lane == 0) __hip_atomic_store(epw, tag, RLX_AGENT);
        const float nmr = -mean * rstd;
#pragma unroll
        for (int s = 0; s < 3; ++s) { const int zi = tile[s] - 3;
            if (zi >= n1 && zi < n1 + n2) {
                const int ft = z2t0 + zi - n1;
                u32x2 w[4];
#pragma unroll
                for (int q = 0; q < 4; ++q) w[q] = (u32x2){cvtpk(fmaf(acc[s][4 * q], rstd, nmr), fmaf(acc[s][4 * q + 1], rstd, nmr)), cvtpk(fmaf(acc[s][4 * q + 2], rstd, nmr), fmaf(acc[s][4 * q + 3], rstd, nmr))};
                char* sc = lds + 8192 + mw * 2048;
#pragma unroll
                for (int q = 0; q < 4; ++q) *(u32x2*)(sc + r32 * 64 + (8 * q + 4 * hi) * 2) = w[q];
                asm volatile("s_waitcnt lgkmcnt(0)" ::: "memory");
                const int tk0 = panel * 128 + 32 * mw;
#pragma unroll
                for (int ks = 0; ks < 2; ++ks) {
                    const lds_cptr rp = (lds_cptr)lds + 8192 + mw * 2048 + (16 * ks + 8 * (g >> 1) + qq) * 64 + (g & 1) * 32 + pp * 8;
                    const bf16x8 fr = cat(ldstr(rp), ldstr(rp + 256));
                    ST16((unsigned)WS_Z2N + (unsigned)(((((ft >> 2) * 32 + (tk0 >> 6)) * 4 + (ft & 3)) * 4 + ((tk0 >> 4) & 3) + ks) * 1024 + lane * 16), __builtin_bit_cast(u32x4, fr));
                }
                asm volatile("s_waitcnt lgkmcnt(0)" ::: "memory");
            } }
    }
    STAMP(0, 6);
}

constexpr int A_KR = 0, A_VR = 32768, A_CONST = 65536, A_FLAG = A_CONST + 4096, A_MX = A_FLAG + 64, K2_LDS = 81920;
constexpr float SAFE_T = 48.f;
#define EX(x) __builtin_amdgcn_exp2f(x)
#define BF(p) __builtin_bit_cast(bf16x8, p)

DI void attn_role(int b, char* __restrict__ ws, char* lds) {
    const u16* Qs = (const u16*)(ws + WS_Q); const u16* Kb = (const u16*)(ws + WS_K); const u16* Vt = (const u16*)(ws + WS_V); const float* kmax = (const float*)(ws + WS_KMAX); u16* Ot = (u16*)(ws + WS_O);
    const int tid = threadIdx.x, lane = tid & 63, r32 = lane & 31, hi = lane >> 5;
    const int wid = __builtin_amdgcn_readfirstlane(tid >> 6);
    const int xcd = b & 7, idx = b >> 3, head = xcd * 4 + (idx & 3), qt = idx >> 2;
    const int qp = wid & 3, kh = wid >> 2;
    const unsigned lds0 = (unsigned)(uintptr_t)lds;
    const char* kg = (const char*)(Kb + (size_t)head * S * DH);
    const char* vg = (const char*)(Vt + (size_t)head * S * DH);
    const unsigned kvoff = wid * 1024 + ((lane ^ ((lane >> 5) & 1)) * 16);
    const unsigned vvoff = wid * 1024 + lane * 16;
#define DMA_CHUNK(c) do { glds16s(kvoff, kg + (c) * 8192, lds0 + A_KR + ((c) & 3) * 8192 + wid * 1024); glds16s(vvoff, vg + (c) * 8192, lds0 + A_VR + ((c) & 3) * 8192 + wid * 1024); } while (0)
    STAMP(1, 0);
    const int q0 = qt * 256 + qp * 64;
    const bf16x8 qa = *(const bf16x8*)(Qs + ((size_t)head * S + q0 + r32) * DH + hi * 8);
    const bf16x8 qb = *(const bf16x8*)(Qs + ((size_t)head * S + q0 + 32 + r32) * DH + hi * 8);
    float km = kmax[lane * H + head];
    DMA_CHUNK(0);
    if (tid < 256) { const unsigned w = (tid & 31) ? 0u : 0x3F803F80u; u32x4 v = {w, w, w, w}; *(u32x4*)(lds + A_CONST + tid * 16) = v; }
    km = max32(max16(rowmax16(km)));
    float qna = 0.f, qnb = 0.f;
#pragma unroll
    for (int j = 0; j < 8; ++j) { const float va = bflo((u16)qa[j]), vb = bflo((u16)qb[j]); qna = fmaf(va, va, qna); qnb = fmaf(vb, vb, qnb); }
    qna = xor32(qna); qnb = xor32(qnb);
    const float mh = __builtin_amdgcn_sqrtf(fmaxf(qna, qnb) * km) * 1.001f;
    { const bool unsafe = __any(!(mh <= SAFE_T)); if (lane == 0) ((unsigned*)(lds + A_FLAG))[wid] = unsafe ? 1u : 0u; }
    const lds_cptr kp0 = (lds_cptr)lds + A_KR + kh * 4096 + r32 * 32 + ((hi ^ (r32 >> 4)) * 16);
    const bool vreal = r32 < 16;
    const unsigned vlane = vreal ? (unsigned)(A_VR + kh * 4096 + hi * 256 + r32 * 16) : (unsigned)(A_CONST + (r32 == 16 ? 0 : ((r32 >= 20 && r32 < 28) ? 64 : 16)));
    const unsigned vmask = vreal ? 0xFFFFFFFFu : 0u;
    f32x16 oa = {}, ob = {};
    WAIT_BAR(0);
    bool slow;
    { const u32x4 f0 = *(const u32x4*)(lds + A_FLAG), f1 = *(const u32x4*)(lds + A_FLAG + 16); slow = __builtin_amdgcn_readfirstlane(f0[0] | f0[1] | f0[2] | f0[3] | f1[0] | f1[1] | f1[2] | f1[3]) != 0; }
    if (!slow) {
        DMA_CHUNK(1); DMA_CHUNK(2); DMA_CHUNK(3);
        f32x16 cinit;
#pragma unroll
        for (int r = 0; r < 16; ++r) cinit[r] = -mh;
        PIN(cinit);
        bf16x8 kf, v0, v1; f32x16 sA, sB; u32x4 p0, p1;
        kf = ldsr128(kp0);
        { lds_cptr vp = (lds_cptr)lds + vlane; v0 = ldsr128(vp); v1 = ldsr128(vp + 512); }
        sA = MFMA32(kf, qa, cinit);
        p1 = (u32x4){0u, 0u, 0u, 0u};
#define HSTEP(SC, SN, QN, OP, OC, PRE1, PRE2, PRE3) do { \
        SN = MFMA32(kf, QN, cinit); PIN(SN); SBAR(); \
        PRE1; \
        SC[0] = EX(SC[0]); SC[1] = EX(SC[1]); SC[2] = EX(SC[2]); SC[3] = EX(SC[3]); SC[4] = EX(SC[4]); SC[5] = EX(SC[5]); PIN(SC); SBAR(); \
        OP = MFMA32(v1, BF(p1), OP); PIN(OP); SBAR(); \
        PRE2; \
        SC[6] = EX(SC[6]); SC[7] = EX(SC[7]); \
        p0[0] = cvtpk(SC[0], SC[1]); p0[1] = cvtpk(SC[2], SC[3]); p0[2] = cvtpk(SC[4], SC[5]); p0[3] = cvtpk(SC[6], SC[7]); PIN(p0); SBAR(); \
        SC[8] = EX(SC[8]); SC[9] = EX(SC[9]); SC[10] = EX(SC[10]); PIN(SC); SBAR(); \
        OC = MFMA32(v0, BF(p0), OC); PIN(OC); SBAR(); \
        PRE3; \
        SC[11] = EX(SC[11]); SC[12] = EX(SC[12]); SC[13] = EX(SC[13]); SC[14] = EX(SC[14]); SC[15] = EX(SC[15]); \
        p1[0] = cvtpk(SC[8], SC[9]); p1[1] = cvtpk(SC[10], SC[11]); p1[2] = cvtpk(SC[12], SC[13]); p1[3] = cvtpk(SC[14], SC[15]); PIN(p1); SBAR(); \
    } while (0)
#define BLOCK(kn, vc, vco, vn, vno) do { \
        HSTEP(sA, sB, qb, ob, oa, kf = ldsr128(kn), v1 = ldsr128((vc) + (vco) + 512), (void)0); \
        HSTEP(sB, sA, qa, oa, ob, (void)0, (void)0, v0 = ldsr128((vn) + (vno))); \
    } while (0)
        STAMP(1, 1);
        for (int c = 0; c < 8; ++c) {
            const unsigned ro = (unsigned)((c & 3) * 8192), rn = (unsigned)(((c + 1) & 3) * 8192);
            const lds_cptr vp = (lds_cptr)lds + (vlane + (ro & vmask)), vpn = (lds_cptr)lds + (vlane + (rn & vmask));
            const lds_cptr kp = kp0 + ro, kpn = kp0 + rn;
            BLOCK(kp + 1024, vp, 0, vp, 1024);
            BLOCK(kp + 2048, vp, 1024, vp, 2048);
            BLOCK(kp + 3072, vp, 2048, vp, 3072);
            if (c == 0) WAIT_BAR(4); else if (c < 6) WAIT_BAR(2); else if (c == 6) WAIT_BAR(0);
            if (c >= 1 && c + 3 < 8) DMA_CHUNK(c + 3);
            BLOCK(kpn, vp, 3072, vpn, 0);
        }
        ob = MFMA32(v1, BF(p1), ob);
#undef HSTEP
#undef BLOCK
    } else {
        float ma = -3.0e38f, mb = -3.0e38f;
        for (int pass = 0; pass < 2; ++pass) {
            for (int c = 0; c < 8; ++c) {
                LBAR();
                DMA_CHUNK(c);
                WAIT_BAR(0);
                const unsigned ro = (unsigned)((c & 3) * 8192);
                const lds_cptr vp = (lds_cptr)lds + (vlane + (ro & vmask)), kp = kp0 + ro;
                for (int j = 0; j < 4; ++j) {
                    const bf16x8 kf = ldsr128(kp + j * 1024);
                    const bf16x8 v0 = ldsr128(vp + j * 1024), v1 = ldsr128(vp + j * 1024 + 512);
#pragma unroll
                    for (int u = 0; u < 2; ++u) {
                        const f32x16 sc = MFMA32(kf, u ? qb : qa, (f32x16){});
                        float& m = u ? mb : ma; f32x16& o = u ? ob : oa;
                        if (pass == 0) {
#pragma unroll
                            for (int r = 0; r < 16; ++r) m = fmaxf(m, sc[r]);
                        } else {
                            u32x4 p0, p1;
#pragma unroll
                            for (int i = 0; i < 4; ++i) { p0[i] = cvtpk(EX(sc[2 * i] - m), EX(sc[2 * i + 1] - m)); p1[i] = cvtpk(EX(sc[8 + 2 * i] - m), EX(sc[9 + 2 * i] - m)); }
                            o = MFMA32(v0, BF(p0), o); o = MFMA32(v1, BF(p1), o);
                        }
                        SBAR();
                    }
                }
            }
            if (pass == 0) {
                ma = max32(ma); mb = max32(mb);
                float* mx = (float*)(lds + A_MX);
                if (hi == 0) { mx[(wid * 2) * 32 + r32] = ma; mx[(wid * 2 + 1) * 32 + r32] = mb; }
                LBAR();
                ma = fmaxf(ma, mx[((wid ^ 4) * 2) * 32 + r32]); mb = fmaxf(mb, mx[((wid ^ 4) * 2 + 1) * 32 + r32]);
            }
        }
    }
    STAMP(1, 2);
    __syncthreads();
    int tz = threadIdx.x; asm volatile("" : "+v"(tz));
    const int lz = tz & 63;
    float* red = (float*)lds;
    { float* dst = red + (size_t)(wid ^ 4) * 9 * 64 + lz;
#pragma unroll
      for (int r = 0; r < 9; ++r) dst[r * 64] = kh ? oa[r] : ob[r]; }
    __syncthreads();
    float o[9];
    { const float* src = red + (size_t)wid * 9 * 64 + lz;
#pragma unroll
      for (int r = 0; r < 9; ++r) o[r] = (kh ? ob[r] : oa[r]) + src[r * 64]; }
    const float rl = __builtin_amdgcn_rcpf(swap32(o[8], false));
    u16* op = Ot + ((size_t)(((q0 >> 5) + kh) * H + head) * 512) + (lz & 31) * 8 + (lz >> 5) * 4;
    st8_wt(op, (u32x2){cvtpk(o[0] * rl, o[1] * rl), cvtpk(o[2] * rl, o[3] * rl)});
    st8_wt(op + 256, (u32x2){cvtpk(o[4] * rl, o[5] * rl), cvtpk(o[6] * rl, o[7] * rl)});
    asm volatile("s_waitcnt vmcnt(0)\n\ts_barrier" ::: "memory");
    if (tz == 0) __hip_atomic_fetch_add((gu32*)(ws + WS_CNT) + (32 + qt) * 64, 1u, RLX_AGENT);
    STAMP(1, 3);
#undef DMA_CHUNK
}

DI void wait_cnt(gu32* cnt, unsigned want) {
    for (unsigned spins = 0; spins < 400000u; ++spins) {
        if ((unsigned)__builtin_amdgcn_readfirstlane(__hip_atomic_load(cnt, RLX_AGENT)) >= want) break;
        __builtin_amdgcn_s_sleep(1);
    }
}

#define O_AUX 16
#define H_AUX 16
constexpr int G_STAGE = 24576;
constexpr int W_STAGE = 16384;
constexpr int G_RED = 4 * W_STAGE;
DI void gate_role(int b, const u16* __restrict__ x, const u16* __restrict__ wo, const u16* __restrict__ wsp, const u16* __restrict__ bsp, const u16* __restrict__ wout, char* __restrict__ ws, u16* __restrict__ out, char* lds) {
    const int xcd = b & 7, idx = b >> 3;
    const int tt = xcd * 4 + (idx & 3), ct = idx >> 2;
    const int tid = threadIdx.x, lane = tid & 63, r32 = lane & 31, hi = lane >> 5;
    const int wid = __builtin_amdgcn_readfirstlane(tid >> 6);
    const unsigned lds0 = (unsigned)(uintptr_t)lds;
    gu32* hcnt = (gu32*)(ws + WS_CNT) + tt * 64; gu32* ocnt = (gu32*)(ws + WS_CNT) + (32 + (tt >> 2)) * 64;
    STAMP(1, 0);
    if (wid >= 4) {
        const int l = wid - 4;
        if (ct < 7) {
            const char* ag = ws + WS_Z2N + (size_t)ct * 32 * 16384 + l * 4096;
            const char* bg = (const char*)wsp + (size_t)(tt * 64 + l * 16) * 4096;
            const unsigned avoff = lane * 16;
            const unsigned bv0 = (unsigned)((lane >> 3) * 4096 + (((lane & 7) ^ (lane >> 4)) * 16)), bv1 = (unsigned)((8 + (lane >> 3)) * 4096 + (((lane & 7) ^ (4 + (lane >> 4))) * 16));
            auto dma = [&](int kt, int slot) __attribute__((always_inline)) { const unsigned d = lds0 + slot * G_STAGE; const char* a_ = ag + (size_t)kt * 16384;
#pragma unroll
                for (int e = 0; e < 4; ++e) glds16s(avoff, a_ + e * 1024, d + l * 4096 + e * 1024);
                glds16s(bv0, bg + kt * 128, d + 16384 + l * 2048); glds16s(bv1, bg + kt * 128, d + 16384 + l * 2048 + 1024); };
            dma(0, 0);
            WAIT_BAR(0);
            dma(1, 1); dma(2, 2);
            int slot = 0;
            for (int kt = 1; kt < 32; ++kt) {
                if (kt < 31) WAIT_BAR(6); else WAIT_BAR(0);
                if (kt + 2 < 32) dma(kt + 2, slot);
                slot = slot == 2 ? 0 : slot + 1;
            }
            LBAR();
        }
        unsigned wv[4];
#pragma unroll
        for (int e = 0; e < 4; ++e) { const int row = l * 32 + e * 8 + (lane >> 3); wv[e] = (unsigned)(row * 1024 + ct * 128 + (((lane & 7) ^ (((row >> 1) & 1) << 2)) * 16)); }
        auto dmw = [&](const u16* w, int st) __attribute__((always_inline)) { const char* wg = (const char*)w + (size_t)st * 128 * 1024; const unsigned d = lds0 + (st & 3) * W_STAGE + l * 4096;
#pragma unroll
            for (int e = 0; e < 4; ++e) glds16s(wv[e], wg, d + e * 1024); };
        dmw(wout, 0); dmw(wout, 1); dmw(wout, 2); dmw(wout, 3);
        if (l == 0) { wait_cnt(hcnt, 7);
        }
        WAIT_BAR(12);
#pragma unroll
        for (int st = 1; st < 7; ++st) {
            if (st < 4) WAIT_BAR(8); else if (st == 4) WAIT_BAR(8); else if (st == 5) WAIT_BAR(4); else WAIT_BAR(0);
            if (st + 3 < 7) dmw(wout, st + 3);
        }
        LBAR();
        dmw(wo, 0); dmw(wo, 1); dmw(wo, 2); dmw(wo, 3);
        if (l == 0) { wait_cnt(ocnt, 32);
        }
        WAIT_BAR(0);
        return;
    }
#define GPRIO 2
    __builtin_amdgcn_s_setprio(GPRIO);
    const int g = lane >> 4, qq = (lane & 15) >> 2, pp = lane & 3;
    const int th = wid & 1, fh = wid >> 1;
    const int trow = 32 * th + r32, t = tt * 64 + trow;
    if (ct < 7) {
        f32x16 acc0 = {}, acc1 = {};
        const unsigned aoff = (unsigned)(fh * 8192 + lane * 16);
        unsigned boff[4];
#pragma unroll
        for (int ks = 0; ks < 4; ++ks) boff[ks] = (unsigned)(16384 + trow * 128 + (((2 * ks + hi) ^ ((trow >> 1) & 7)) * 16));
        bf16x8 fa0[2], fa1[2], fb[2];
        auto rd = [&](int set, lds_cptr sb, int ks) __attribute__((always_inline)) {
            fb[set] = ldsr128(sb + boff[ks]);
            fa0[set] = ldsr128(sb + aoff + ks * 1024);
            fa1[set] = ldsr128(sb + aoff + 4096 + ks * 1024); };
        auto mm = [&](int set) __attribute__((always_inline)) { };
#define GSTEP(cur, nxt, sbn, ksn) do { SBAR(); acc0 = MFMA32(fa0[cur], fb[cur], acc0); SBAR(); rd(nxt, sbn, ksn); SBAR(); acc1 = MFMA32(fa1[cur], fb[cur], acc1); SBAR(); } while (0)
        const float bias = bflo(bsp[t]);
        u32x2 z[2][4];
#pragma unroll
        for (int i = 0; i < 2; ++i)
#pragma unroll
            for (int q = 0; q < 4; ++q) z[i][q] = *(const u32x2*)((const u16*)(ws + WS_Z1) + ((size_t)((t >> 5) * 28 + ct * 4 + 2 * fh + i) * 1024) + (hi * 32 + r32) * 4 + q * 256);
        LBAR();
        rd(0, (lds_cptr)lds, 0);
        int slot = 0;
        for (int kt = 0; kt < 32; ++kt) {
            const lds_cptr sb = (lds_cptr)lds + slot * G_STAGE;
            GSTEP(0, 1, sb, 1);
            GSTEP(1, 0, sb, 2);
            GSTEP(0, 1, sb, 3);
            slot = slot == 2 ? 0 : slot + 1;
            if (kt < 31) { LBAR(); GSTEP(1, 0, (lds_cptr)lds + slot * G_STAGE, 0); }
            else { acc0 = MFMA32(fa0[1], fb[1], acc0); acc1 = MFMA32(fa1[1], fb[1], acc1); }
        }
        STAMP(1, 2);
#pragma unroll
        for (int i = 0; i < 2; ++i) {
            u16* hp = (u16*)(ws + WS_H) + ((size_t)((t >> 5) * 56 + (ct * 4 + 2 * fh + i) * 2) * 512) + r32 * 8 + hi * 4;
#pragma unroll
            for (int q = 0; q < 4; ++q) {
                float v[4];
#pragma unroll
                for (int r = 0; r < 4; ++r) v[r] = (i ? acc1[4 * q + r] : acc0[4 * q + r]) + bias;
                st8_wt(hp + (q >> 1) * 512 + (q & 1) * 256, (u32x2){cvtpk(v[0] * bflo(z[i][q][0]), v[1] * bfhi(z[i][q][0])), cvtpk(v[2] * bflo(z[i][q][1]), v[3] * bfhi(z[i][q][1]))});
            }
        }
        asm volatile("s_waitcnt vmcnt(0) lgkmcnt(0)\n\ts_barrier" ::: "memory");
        if (tid == 0) __hip_atomic_fetch_add(hcnt, 1u, RLX_AGENT);
    }
    STAMP(1, 3);
    const int kh = fh;
    const int kr = 8 * (g >> 1) + qq;
    unsigned wof[2][2];
#pragma unroll
    for (int n = 0; n < 2; ++n) { wof[n][0] = (unsigned)((64 * kh + kr) * 128 + ((n ^ ((kr >> 1) & 1)) * 64) + (g & 1) * 32 + pp * 8); wof[n][1] = wof[n][0] + 512; }
    auto gemm = [&](const __amdgpu_buffer_rsrc_t tr, auto NST, bool bars, f32x16& c0, f32x16& c1) __attribute__((always_inline)) {
        constexpr int N = decltype(NST)::value;
        u32x4 tf[2][4];
#pragma unroll
        for (int i = 0; i < 4; ++i) tf[0][i] = __builtin_amdgcn_raw_buffer_load_b128(tr, lane * 16 + (4 * kh + i) * 1024, 0, H_AUX);
#pragma unroll
        for (int st = 0; st < N; ++st) {
            if (st + 1 < N) {
#pragma unroll
                for (int i = 0; i < 4; ++i) tf[(st + 1) & 1][i] = __builtin_amdgcn_raw_buffer_load_b128(tr, lane * 16 + ((st + 1) * 8 + 4 * kh + i) * 1024, 0, H_AUX); SBAR(); }
            if (bars && st > 0) LBAR();
            const lds_cptr sb = (lds_cptr)lds + (st & 3) * W_STAGE;
#pragma unroll
            for (int i = 0; i < 4; ++i) {
                const bf16x8 w0 = cat(ldstr(sb + wof[0][0] + i * 2048), ldstr(sb + wof[0][1] + i * 2048)), w1 = cat(ldstr(sb + wof[1][0] + i * 2048), ldstr(sb + wof[1][1] + i * 2048));
                c0 = MFMA32(w0, BF(tf[st & 1][i]), c0); c1 = MFMA32(w1, BF(tf[st & 1][i]), c1);
            }
        }
    };
    auto merge = [&](f32x16& c0, f32x16& c1) __attribute__((always_inline)) {
        f32x4* dst = (f32x4*)(lds + G_RED + (wid ^ 2) * 4096) + lane;
#pragma unroll
        for (int q = 0; q < 4; ++q) dst[q * 64] = kh ? (f32x4){c0[4 * q], c0[4 * q + 1], c0[4 * q + 2], c0[4 * q + 3]} : (f32x4){c1[4 * q], c1[4 * q + 1], c1[4 * q + 2], c1[4 * q + 3]};
    };
    f32x16 gb0 = {}, gb1 = {};
    LBAR();
    gemm(__builtin_amdgcn_make_buffer_rsrc((void*)(ws + WS_H + (size_t)(tt * 2 + th) * 57344), 0, 57344, 0x00020000), std::integral_constant<int, 7>{}, true, gb0, gb1);
    merge(gb0, gb1);
    LBAR();
    { char* sc = lds + G_RED + wid * 4096;
      float gv[16];
#pragma unroll
      for (int q = 0; q < 4; ++q) { const f32x4 o4 = ((const f32x4*)sc)[q * 64 + lane];
#pragma unroll
          for (int r = 0; r < 4; ++r) { const float bb = (kh ? gb1[4 * q + r] : gb0[4 * q + r]) + o4[r]; gv[4 * q + r] = bb * __builtin_amdgcn_rcpf(1.0f + EX(-LOG2E * bb)); } }
      asm volatile("s_waitcnt lgkmcnt(0)" ::: "memory");
#pragma unroll
      for (int u = 0; u < 2; ++u) ((u32x4*)sc)[u * 64 + lane] = (u32x4){cvtpk(gv[8 * u], gv[8 * u + 1]), cvtpk(gv[8 * u + 2], gv[8 * u + 3]), cvtpk(gv[8 * u + 4], gv[8 * u + 5]), cvtpk(gv[8 * u + 6], gv[8 * u + 7])}; }
    const int n0 = ct * 64 + kh * 32 + 4 * hi;
    u32x2 xv[4];
#pragma unroll
    for (int q = 0; q < 4; ++q) xv[q] = *(const u32x2*)(x + (size_t)t * D + n0 + 8 * q);
    PIN(xv[0]); PIN(xv[1]); PIN(xv[2]); PIN(xv[3]);
    STAMP(1, 4);
    LBAR();
    STAMP(1, 5);
    f32x16 s0 = {}, s1 = {};
    {
        const __amdgpu_buffer_rsrc_t tr = __builtin_amdgcn_make_buffer_rsrc((void*)(ws + WS_O + (size_t)(tt * 2 + th) * 32768), 0, 32768, 0x00020000);
        u32x4 tf[16];
#pragma unroll
        for (int i = 0; i < 16; ++i) tf[i] = __builtin_amdgcn_raw_buffer_load_b128(tr, lane * 16 + ((i >> 2) * 8 + 4 * kh + (i & 3)) * 1024, 0, O_AUX);
        SBAR();
        bf16x8 w0[2], w1[2];
        w0[0] = cat(ldstr((lds_cptr)lds + wof[0][0]), ldstr((lds_cptr)lds + wof[0][1])); w1[0] = cat(ldstr((lds_cptr)lds + wof[1][0]), ldstr((lds_cptr)lds + wof[1][1]));
#pragma unroll
        for (int i = 0; i < 16; ++i) {
            if (i + 1 < 16) { const lds_cptr sb = (lds_cptr)lds + ((i + 1) >> 2) * W_STAGE + ((i + 1) & 3) * 2048;
                w0[(i + 1) & 1] = cat(ldstr(sb + wof[0][0]), ldstr(sb + wof[0][1])); w1[(i + 1) & 1] = cat(ldstr(sb + wof[1][0]), ldstr(sb + wof[1][1])); }
            s0 = MFMA32(w0[i & 1], BF(tf[i]), s0); s1 = MFMA32(w1[i & 1], BF(tf[i]), s1);
        }
    }
    STAMP(1, 7);
    u32x4 gq[2];
    { const u32x4* gt = (const u32x4*)(lds + G_RED + wid * 4096) + lane; gq[0] = gt[0]; gq[1] = gt[64]; }
    LBAR();
    { f32x4* dst = (f32x4*)(lds + (wid ^ 2) * 4096) + lane;
#pragma unroll
      for (int q = 0; q < 4; ++q) dst[q * 64] = kh ? (f32x4){s0[4 * q], s0[4 * q + 1], s0[4 * q + 2], s0[4 * q + 3]} : (f32x4){s1[4 * q], s1[4 * q + 1], s1[4 * q + 2], s1[4 * q + 3]}; }
    LBAR();
    { const f32x4* src = (const f32x4*)(lds + wid * 4096) + lane;
#pragma unroll
      for (int q = 0; q < 4; ++q) { const f32x4 o4 = src[q * 64]; float v[4]; const unsigned g0 = gq[q >> 1][2 * (q & 1)], g1 = gq[q >> 1][2 * (q & 1) + 1]; const float gg[4] = {bflo(g0), bfhi(g0), bflo(g1), bfhi(g1)};
#pragma unroll
          for (int r = 0; r < 4; ++r) v[r] = ((kh ? s1[4 * q + r] : s0[4 * q + r]) + o4[r]) * gg[r];
          *(u32x2*)(out + (size_t)t * D + n0 + 8 * q) = (u32x2){cvtpk(bflo(xv[q][0]) + v[0], bfhi(xv[q][0]) + v[1]), cvtpk(bflo(xv[q][1]) + v[2], bfhi(xv[q][1]) + v[3])}; } }
    STAMP(1, 6);
}

__global__ __launch_bounds__(512, 4) void k2_mix(const u16* __restrict__ x, const u16* __restrict__ wo, const u16* __restrict__ wsp, const u16* __restrict__ bsp, const u16* __restrict__ wout, char* __restrict__ ws, u16* __restrict__ out) {
    extern __shared__ __attribute__((aligned(16))) char lds[];
    if (blockIdx.x < 256) attn_role(blockIdx.x, ws, lds);
    if (blockIdx.x >= 256) gate_role(blockIdx.x - 256, x, wo, wsp, bsp, wout, ws, out, lds);
}

extern "C" void kernel_launch(void* const* d_in, const int* in_sizes, int n_in, void* d_out, int out_size, void* d_ws, size_t ws_size, hipStream_t stream) {
    const u16* x = (const u16*)d_in[0]; const u16* wqkv = (const u16*)d_in[1]; const u16* wo = (const u16*)d_in[2];
    const u16* win = (const u16*)d_in[4]; const u16* wsp = (const u16*)d_in[5]; const u16* bsp = (const u16*)d_in[6]; const u16* wout = (const u16*)d_in[7];
    char* ws = (char*)d_ws;
    (void)hipFuncSetAttribute((const void*)k1_proj, hipFuncAttributeMaxDynamicSharedMemorySize, K1_LDS);
    (void)hipFuncSetAttribute((const void*)k2_mix, hipFuncAttributeMaxDynamicSharedMemorySize, K2_LDS);
    k1_proj<<<256, 768, K1_LDS, stream>>>(x, wqkv, win, ws);
    k2_mix<<<512, 512, K2_LDS, stream>>>(x, wo, wsp, bsp, wout, ws, (u16*)d_out);
}
```

```cpp
#include <hip/hip_runtime.h>
#include <hip/hip_bf16.h>
#include <stdint.h>
#include <type_traits>

typedef unsigned short u16;
using bf16x8 = __attribute__((ext_vector_type(8))) short;
using s16x4 = __attribute__((ext_vector_type(4))) short;
using f32x16 = __attribute__((ext_vector_type(16))) float;
using f32x4 = __attribute__((ext_vector_type(4))) float;
using u32x4 = __attribute__((ext_vector_type(4))) unsigned;
using u32x2 = __attribute__((ext_vector_type(2))) unsigned;
typedef __bf16 bf16x2_t __attribute__((ext_vector_type(2)));
typedef float f32x2_t __attribute__((ext_vector_type(2)));
typedef short v4i16_t __attribute__((ext_vector_type(4)));
#define LDSAS __attribute__((address_space(3)))
typedef LDSAS const char* lds_cptr;
#define MFMA32(a, b, c) __builtin_amdgcn_mfma_f32_32x32x16_bf16(a, b, c, 0, 0, 0)
#define MFMA16(a, b, c) __builtin_amdgcn_mfma_f32_16x16x32_bf16(a, b, c, 0, 0, 0)
#define SBAR() __builtin_amdgcn_sched_barrier(0)
#define PIN(x) asm volatile("" : "+v"(x))
#define WAIT_BAR(N) asm volatile("s_waitcnt vmcnt(" #N ") lgkmcnt(0)\n\ts_barrier" ::: "memory")
#define LBAR() asm volatile("s_waitcnt lgkmcnt(0)\n\ts_barrier" ::: "memory")
#define DI __device__ __forceinline__
#define ST8(p, w) st8_wt((void*)(p), (w))
#define ST16(off, v) __builtin_amdgcn_raw_buffer_store_b128((v), wsr, (off), 0, 16)
#define STAMP(kern, i) do { } while (0)

constexpr int S = 2048, D = 512, H = 32, DH = 16, FH = 896;
constexpr float LOG2E = 1.4426950408889634f;

constexpr size_t WS_Q = 0, WS_K = 2u << 20, WS_V = 4u << 20, WS_Z1 = 6u << 20, WS_Z2R = 10u << 20, WS_Z2N = 14u << 20, WS_H = 18u << 20, WS_O = 22u << 20, WS_KMAX = 24u << 20, WS_EPOCH = 25u << 20, WS_XCH = 26u << 20, WS_XST = 27u << 20, WS_CNT = 28u << 20;

DI unsigned cvtpk(float lo, float hi) { f32x2_t v = {lo, hi}; return __builtin_bit_cast(unsigned, __builtin_convertvector(v, bf16x2_t)); }
DI float bflo(unsigned w) { return __uint_as_float(w << 16); }
DI float bfhi(unsigned w) { return __uint_as_float(w & 0xFFFF0000u); }
DI float dot2(unsigned a, unsigned b, float c) { return __builtin_amdgcn_fdot2_f32_bf16(__builtin_bit_cast(bf16x2_t, a), __builtin_bit_cast(bf16x2_t, b), c, false); }
DI void glds16s(unsigned voff, const void* sbase, unsigned lds_base) {
    lds_base = __builtin_amdgcn_readfirstlane(lds_base);
    unsigned sv; asm volatile("s_mov_b32 %0, m0\n\ts_mov_b32 m0, %3\n\ts_nop 0\n\tglobal_load_lds_dwordx4 %1, %2\n\ts_mov_b32 m0, %0" : "=&s"(sv) : "v"(voff), "s"(sbase), "s"(lds_base) : "memory"); }
DI bf16x8 ldsr128(lds_cptr p) { return *(const LDSAS bf16x8*)p; }
DI s16x4 ldstr(lds_cptr p) { return __builtin_bit_cast(s16x4, __builtin_amdgcn_ds_read_tr16_b64_v4i16((LDSAS v4i16_t*)p)); }
DI bf16x8 cat(s16x4 a, s16x4 b) { return __builtin_shufflevector(a, b, 0, 1, 2, 3, 4, 5, 6, 7); }
DI float swap32(float x, bool sum) { auto rr = __builtin_amdgcn_permlane32_swap(__float_as_uint(x), __float_as_uint(x), false, false); return sum ? __uint_as_float(rr[0]) + __uint_as_float(rr[1]) : __uint_as_float(rr[0]); }
DI float gelu_t(float x) {
    const float c1 = -2.0f * 0.7978845608028654f * LOG2E, c3 = c1 * 0.044715f;
    const float u = x * fmaf(x * x, c3, c1);
    return x * __builtin_amdgcn_rcpf(1.0f + __builtin_amdgcn_exp2f(u));
}

constexpr int K1_XB = 16384;
constexpr int K1_STAGE = K1_XB + 7 * 4096;
constexpr int K1_CS = 3 * K1_STAGE;
constexpr int K1_LDS = K1_CS + 1024;
typedef __attribute__((address_space(1))) unsigned gu32; typedef __attribute__((address_space(1))) unsigned long long gu64;
#define RLX_AGENT __ATOMIC_RELAXED, __HIP_MEMORY_SCOPE_AGENT

DI float xor16(float x) { auto r = __builtin_amdgcn_permlane16_swap(__float_as_uint(x), __float_as_uint(x), false, false); return __uint_as_float(r[0]) + __uint_as_float(r[1]); }
DI float xor32(float x) { auto r = __builtin_amdgcn_permlane32_swap(__float_as_uint(x), __float_as_uint(x), false, false); return __uint_as_float(r[0]) + __uint_as_float(r[1]); }
DI float max16(float x) { auto r = __builtin_amdgcn_permlane16_swap(__float_as_uint(x), __float_as_uint(x), false, false); return fmaxf(__uint_as_float(r[0]), __uint_as_float(r[1])); }
DI float max32(float x) { auto r = __builtin_amdgcn_permlane32_swap(__float_as_uint(x), __float_as_uint(x), false, false); return fmaxf(__uint_as_float(r[0]), __uint_as_float(r[1])); }
template <int CTRL> DI float dppf(float x) { return __uint_as_float(__builtin_amdgcn_update_dpp(0, __float_as_uint(x), CTRL, 0xF, 0xF, false)); }
DI float rowmax16(float x) { x = fmaxf(x, dppf<0x128>(x)); x = fmaxf(x, dppf<0x124>(x)); x = fmaxf(x, dppf<0x122>(x)); x = fmaxf(x, dppf<0x121>(x)); return x; }
DI float wavesum(float x) { x += dppf<0x128>(x); x += dppf<0x124>(x); x += dppf<0x122>(x); x += dppf<0x121>(x); return xor32(xor16(x)); }
DI u32x4 widen(u32x2 a, u32x2 b) { auto r0 = __builtin_amdgcn_permlane32_swap(a[0], b[0], false, false); auto r1 = __builtin_amdgcn_permlane32_swap(a[1], b[1], false, false); return (u32x4){r0[0], r1[0], r0[1], r1[1]}; }

DI void st8_wt(void* p, u32x2 w) { __hip_atomic_store((gu64*)p, ((unsigned long long)w[1] << 32) | w[0], RLX_AGENT); }
__global__ __launch_bounds__(768, 3) void k1_proj(const u16* __restrict__ x, const u16* __restrict__ wqkv, const u16* __restrict__ win, char* __restrict__ ws) {
    extern __shared__ __attribute__((aligned(16))) char lds[];
    const int tid = threadIdx.x, lane = tid & 63, r32 = lane & 31, hi = lane >> 5;
    const int wid = __builtin_amdgcn_readfirstlane(tid >> 6);
    const int b = blockIdx.x, xcd = b & 7, idx = b >> 3;
    const int panel = 8 * (xcd >> 2) + (idx & 7), j = 4 * (xcd & 3) + (idx >> 3);
    const unsigned lds0 = (unsigned)(uintptr_t)lds;
    const int jq = j & 3, u7 = 7 * (j >> 2);
    const int n1 = jq == 3 ? 1 : 2, n2 = jq == 1 ? 1 : 2;
    const int z1t0 = u7 + 2 * jq, z2t0 = u7 + (jq == 0 ? 0 : (jq == 1 ? 2 : (jq == 2 ? 3 : 5)));
    STAMP(0, 0);
    if (b == 0 && tid < 40) ((unsigned*)(ws + WS_CNT))[tid * 64] = 0u;
    if (wid >= 8) {
        const int l = wid - 8;
        const char* xb = (const char*)x + (size_t)(panel * 128 + l * 32) * 1024;
        const unsigned xv0 = (unsigned)((lane >> 3) * 1024 + (((lane & 7) ^ (lane >> 4)) * 16)), xv1 = (unsigned)((lane >> 3) * 1024 + (((lane & 7) ^ (4 + (lane >> 4))) * 16));
        const unsigned vq = (unsigned)((16 * l + (lane >> 2)) * 3072 + (lane & 3) * 16), vz = (unsigned)((16 * l + (lane >> 2)) * 3584 + (lane & 3) * 16);
        const char* wq = (const char*)wqkv + j * 64;
        int zc[4];
#pragma unroll
        for (int i = 0; i < 4; ++i) { const int ii = i < n1 + n2 ? i : n1 + n2 - 1; zc[i] = ii < n1 ? (z1t0 + ii) * 64 : FH * 2 + (z2t0 + ii - n1) * 64; }
        auto dma = [&](int st) __attribute__((always_inline)) {
            const unsigned d = lds0 + (st % 3) * K1_STAGE;
#pragma unroll
            for (int p = 0; p < 4; ++p) glds16s((p & 1) ? xv1 : xv0, xb + st * 128 + p * 8192, d + l * 4096 + p * 1024);
            const char* bq = wq + (size_t)st * 64 * 3072; const char* bz = (const char*)win + (size_t)st * 64 * 3584; const unsigned dw = d + K1_XB + l * 1024;
            glds16s(vq, bq + 2048, dw); glds16s(vq, bq, dw + 4096); glds16s(vq, bq + 1024, dw + 8192);
#pragma unroll
            for (int i = 0; i < 4; ++i) glds16s(vz, bz + zc[i], dw + (3 + i) * 4096);
        };
        dma(0);
        WAIT_BAR(0);
        dma(1); dma(2);
#pragma unroll
        for (int st = 1; st < 8; ++st) {
            if (st < 7) WAIT_BAR(11); else WAIT_BAR(0);
            if (st + 2 < 8) dma(st + 2);
        }
        {
            if ((idx & 3) == 0) {
                unsigned long long pc;
                asm volatile("s_getpc_b64 s[98:99]\n\ts_add_u32 s98, s98, _Z6k2_mixPKtS0_S0_S0_S0_PcPt@rel32@lo+4\n\ts_addc_u32 s99, s99, _Z6k2_mixPKtS0_S0_S0_S0_PcPt@rel32@hi+12\n\ts_mov_b64 %0, s[98:99]" : "=s"(pc) :: "scc", "s98", "s99");
                const u32x4* cp = (const u32x4*)(pc + (size_t)l * 4096) + lane;
                u32x4 a = {0u, 0u, 0u, 0u};
#pragma unroll
                for (int i = 0; i < 4; ++i) a ^= cp[i * 64];
                asm volatile("" :: "v"(a));
            }
        }
        return;
    }
    const int mw = wid & 3, nh = wid >> 2;
    gu32* epw = (gu32*)(ws + WS_EPOCH) + panel * 64;
    unsigned tag = __hip_atomic_load(epw, RLX_AGENT);
    u32x4 xr = *(const u32x4*)(x + (size_t)(panel * 128 + 8 * j + wid) * D + lane * 8);
    auto rowstats = [&]() __attribute__((always_inline)) {
        PIN(xr); PIN(tag); tag += 1u;
        float v[8];
#pragma unroll
        for (int i = 0; i < 4; ++i) { v[2 * i] = bflo(xr[i]); v[2 * i + 1] = bfhi(xr[i]); }
        const float m = wavesum(((v[0] + v[1]) + (v[2] + v[3])) + ((v[4] + v[5]) + (v[6] + v[7]))) * (1.0f / 512.0f);
        float q = 0.f;
#pragma unroll
        for (int i = 0; i < 8; ++i) q = fmaf(v[i] - m, v[i] - m, q);
        const float r = __builtin_amdgcn_rsqf(wavesum(q) * (1.0f / 512.0f) + 1e-5f);
        if (lane < 2) __hip_atomic_store((gu64*)(ws + WS_XST) + (size_t)(panel * 128 + 8 * j + wid) * 2 + lane, ((unsigned long long)tag << 32) | __float_as_uint(lane ? r : m), RLX_AGENT);
    };
    f32x16 acc[4];
#pragma unroll
    for (int s = 0; s < 4; ++s) acc[s] = (f32x16){};
    float cs0 = 0.f, cs1 = 0.f;
    const int g = lane >> 4, qq = (lane & 15) >> 2, pp = lane & 3;
    const int trow = 32 * mw + r32;
    unsigned xo[4];
#pragma unroll
    for (int ks = 0; ks < 4; ++ks) xo[ks] = (unsigned)(trow * 128 + (((2 * ks + hi) ^ ((trow >> 1) & 7)) * 16));
    const unsigned wlane = (unsigned)(K1_XB + (8 * (g >> 1) + qq) * 64 + (g & 1) * 32 + pp * 8);
    const __amdgpu_buffer_rsrc_t sr = __builtin_amdgcn_make_buffer_rsrc((void*)(ws + WS_XST + (size_t)panel * 128 * 16), 0, 128 * 16, 0x00020000);
    u32x4 pst;
    int tile[4];
    auto mainloop = [&](auto NHC) __attribute__((always_inline)) {
        constexpr int NHV = decltype(NHC)::value; constexpr int NS = NHV == 0 ? 4 : 3;
        unsigned wo[NS];
#pragma unroll
        for (int s = 0; s < NS; ++s) { int t = s + mw; t = t >= NS ? t - NS : t; t = t >= NS ? t - NS : t; tile[s] = 4 * NHV + t; wo[s] = wlane + tile[s] * 4096; }
        bf16x8 xa[2], wf[2][NS];
        auto rd = [&](const int set, const lds_cptr sb, const int ks) __attribute__((always_inline)) {
            xa[set] = ldsr128(sb + xo[ks]);
#pragma unroll
            for (int s = 0; s < NS; ++s) wf[set][s] = cat(ldstr(sb + wo[s] + ks * 1024), ldstr(sb + wo[s] + ks * 1024 + 256));
        };
        auto mm = [&](const int set) __attribute__((always_inline)) {
            SBAR();
#pragma unroll
            for (int s = 0; s < NS; ++s) acc[s] = MFMA32(wf[set][s], xa[set], acc[s]);
            const u32x4 w = __builtin_bit_cast(u32x4, wf[set][0]);
#pragma unroll
            for (int i = 0; i < 4; ++i) { cs0 += bflo(w[i]); cs1 += bfhi(w[i]); }
            PIN(cs0); PIN(cs1);
#pragma unroll
            for (int i = 0; i < NS; ++i) { __builtin_amdgcn_sched_group_barrier(0x008, 1, 0); __builtin_amdgcn_sched_group_barrier(0x002, NS == 4 ? 4 : 6, 0); }
            SBAR();
        };
        LBAR();
        STAMP(0, 1);
        rd(0, (lds_cptr)lds, 0);
#pragma unroll
        for (int st = 0; st < 8; ++st) {
            const lds_cptr sb = (lds_cptr)lds + (st % 3) * K1_STAGE;
            rd(1, sb, 1); mm(0);
            rd(0, sb, 2); mm(1);
            rd(1, sb, 3); mm(0);
            if (st < 7) { LBAR(); rd(0, (lds_cptr)lds + ((st + 1) % 3) * K1_STAGE, 0); }
            if (st == 1) rowstats();
            if (st == 5) pst = __builtin_amdgcn_raw_buffer_load_b128(sr, trow * 16, 0, 16);
            mm(1);
        }
    };
    if (nh == 0) mainloop(std::integral_constant<int, 0>{}); else mainloop(std::integral_constant<int, 1>{});
    STAMP(0, 2);
    float* csl = (float*)(lds + K1_CS);
    { const float c = xor32(cs0 + cs1); if (hi == 0) csl[tile[0] * 32 + r32] = c; }
    const int tok = panel * 128 + trow;
    float mu, rs;
    {   u32x4 p = pst;
        for (unsigned spins = 0; !__all((p[1] == tag) & (p[3] == tag)) && spins < 200000u; ++spins) {
            __builtin_amdgcn_s_sleep(1);
            p = __builtin_amdgcn_raw_buffer_load_b128(sr, trow * 16, 0, 16);
        }
        mu = __uint_as_float(p[0]); rs = __uint_as_float(p[2]); }
    LBAR();
    STAMP(0, 3);
    auto lnv = [&](int s, float (&v)[16], float sc) __attribute__((always_inline)) {
#pragma unroll
        for (int q = 0; q < 4; ++q) { const f32x4 c = *(const f32x4*)(csl + tile[s] * 32 + 8 * q + 4 * hi);
#pragma unroll
            for (int r = 0; r < 4; ++r) v[4 * q + r] = sc * (acc[s][4 * q + r] - mu * c[r]); } };
    const __amdgpu_buffer_rsrc_t wsr = __builtin_amdgcn_make_buffer_rsrc((void*)ws, 0, 32u << 20, 0x00020000);
    u16* Z1 = (u16*)(ws + WS_Z1);
    auto z1store = [&](int zt, const u32x2 (&w)[4]) __attribute__((always_inline)) {
        u16* p = Z1 + ((size_t)((tok >> 5) * 28 + zt) * 1024) + (hi * 32 + r32) * 4;
#pragma unroll
        for (int q = 0; q < 4; ++q) ST8(p + q * 256, w[q]); };
    auto gelu16 = [&](int s, u32x2 (&w)[4]) __attribute__((always_inline)) { float v[16]; lnv(s, v, rs);
#pragma unroll
        for (int q = 0; q < 4; ++q) w[q] = (u32x2){cvtpk(gelu_t(v[4 * q]), gelu_t(v[4 * q + 1])), cvtpk(gelu_t(v[4 * q + 2]), gelu_t(v[4 * q + 3]))}; };
    if (nh == 0) {
#pragma unroll
        for (int s = 0; s < 4; ++s) {
            const int T = tile[s];
            if (T == 3) { u32x2 w[4]; gelu16(s, w); z1store(z1t0, w); }
            else {
                float v[16]; lnv(s, v, T == 1 ? rs * (0.25f * LOG2E) : rs);
                u32x2 w[4];
#pragma unroll
                for (int q = 0; q < 4; ++q) w[q] = (u32x2){cvtpk(v[4 * q], v[4 * q + 1]), cvtpk(v[4 * q + 2], v[4 * q + 3])};
                if (T == 0) {
                    char* sc = lds + mw * 2048;
#pragma unroll
                    for (int q = 0; q < 4; ++q) *(u32x2*)(sc + r32 * 64 + (8 * q + 4 * hi) * 2) = w[q];
                    asm volatile("s_waitcnt lgkmcnt(0)" ::: "memory");
                    u16* Vt = (u16*)(ws + WS_V);
#pragma unroll
                    for (int hh = 0; hh < 2; ++hh)
#pragma unroll
                        for (int ks = 0; ks < 2; ++ks) {
                            const s16x4 t4 = ldstr((lds_cptr)lds + mw * 2048 + (16 * ks + 8 * (g >> 1) + 4 * (g & 1) + qq) * 64 + hh * 32 + pp * 8);
                            ST8(Vt + ((size_t)(2 * j + hh) * 128 + ((panel * 128 + 32 * mw) >> 4) + ks) * 256 + (g & 1) * 128 + (lane & 15) * 8 + (g >> 1) * 4, __builtin_bit_cast(u32x2, t4)); }
                } else {
                    const unsigned dsto = (unsigned)(T == 1 ? WS_Q : WS_K);
#pragma unroll
                    for (int hh = 0; hh < 2; ++hh) ST16(dsto + (((2 * j + hh) * S + tok) * 16 + 8 * hi) * 2, widen(w[2 * hh], w[2 * hh + 1]));
                    if (T == 2) {
                        float* kmax = (float*)(ws + WS_KMAX);
#pragma unroll
                        for (int hh = 0; hh < 2; ++hh) { float n = 0.f;
#pragma unroll
                            for (int q = 0; q < 2; ++q) { const u32x2 ww = w[2 * hh + q]; n = fmaf(bflo(ww[0]), bflo(ww[0]), n); n = fmaf(bfhi(ww[0]), bfhi(ww[0]), n); n = fmaf(bflo(ww[1]), bflo(ww[1]), n); n = fmaf(bfhi(ww[1]), bfhi(ww[1]), n); }
                            n = max16(rowmax16(xor32(n)));
                            if (lane == 0) kmax[(panel * 4 + mw) * H + 2 * j + hh] = n; }
                    }
                }
            }
        }
    } else {
        __builtin_amdgcn_s_setprio(2);
        float sm = 0.f;
#pragma unroll
        for (int s = 0; s < 3; ++s) { const int zi = tile[s] - 3;
            if (zi >= n1 && zi < n1 + n2) { float v[16]; lnv(s, v, rs);
#pragma unroll
                for (int r = 0; r < 16; ++r) { acc[s][r] = gelu_t(v[r]); sm += acc[s][r]; } } }
        sm = xor32(sm);
        const float ml = sm * (n2 == 2 ? (1.0f / 64.f) : (1.0f / 32.f)); float m2 = 0.f;
#pragma unroll
        for (int s = 0; s < 3; ++s) { const int zi = tile[s] - 3;
            if (zi >= n1 && zi < n1 + n2) {
#pragma unroll
                for (int r = 0; r < 16; ++r) { const float d = acc[s][r] - ml; m2 = fmaf(d, d, m2); } } }
        m2 = xor32(m2);
        __hip_atomic_store((gu64*)(ws + WS_XCH) + ((size_t)(panel * 16 + j) * 128 + trow) * 2 + hi, ((unsigned long long)tag << 32) | __float_as_uint(hi ? m2 : sm), RLX_AGENT);
        __builtin_amdgcn_s_setprio(0);
        STAMP(0, 4);
#pragma unroll
        for (int s = 0; s < 3; ++s) { const int zi = tile[s] - 3; if (zi < n1) { u32x2 w[4]; gelu16(s, w); z1store(z1t0 + zi, w); } }
        float mean, rstd;
        {
            const __amdgpu_buffer_rsrc_t xr = __builtin_amdgcn_make_buffer_rsrc((void*)(ws + WS_XCH + (size_t)panel * 16 * 128 * 16), 0, 16 * 128 * 16, 0x00020000);
            u32x4 p[8];
            for (unsigned spins = 0;; ++spins) {
                bool ok = true;
#pragma unroll
                for (int u = 0; u < 8; ++u) p[u] = __builtin_amdgcn_raw_buffer_load_b128(xr, ((8 * hi + u) * 128 + trow) * 16, 0, 16);
#pragma unroll
                for (int u = 0; u < 8; ++u) ok &= (p[u][1] == tag) & (p[u][3] == tag);
                if (__all(ok) || spins > 200000u) break;
                __builtin_amdgcn_s_sleep(1);
            }
            float tot = 0.f;
#pragma unroll
            for (int u = 0; u < 8; ++u) tot += __uint_as_float(p[u][0]);
            mean = xor32(tot) * (1.0f / FH);
            float mm2 = 0.f;
#pragma unroll
            for (int u = 0; u < 8; ++u) { const float nl = (u & 3) == 1 ? 32.f : 64.f; const float d = __uint_as_float(p[u][0]) * ((u & 3) == 1 ? (1.0f / 32.f) : (1.0f / 64.f)) - mean; mm2 += __uint_as_float(p[u][2]) + nl * d * d; }
            rstd = __builtin_amdgcn_rsqf(xor32(mm2) * (1.0f / FH) + 1e-5f);
        }
        STAMP(0, 5);
        if (wid == 4 && lane == 0) __hip_atomic_store(epw, tag, RLX_AGENT);
        const float nmr = -mean * rstd;
#pragma unroll
        for (int s = 0; s < 3; ++s) { const int zi = tile[s] - 3;
            if (zi >= n1 && zi < n1 + n2) {
                const int ft = z2t0 + zi - n1;
                u32x2 w[4];
#pragma unroll
                for (int q = 0; q < 4; ++q) w[q] = (u32x2){cvtpk(fmaf(acc[s][4 * q], rstd, nmr), fmaf(acc[s][4 * q + 1], rstd, nmr)), cvtpk(fmaf(acc[s][4 * q + 2], rstd, nmr), fmaf(acc[s][4 * q + 3], rstd, nmr))};
                char* sc = lds + 8192 + mw * 2048;
#pragma unroll
                for (int q = 0; q < 4; ++q) *(u32x2*)(sc + r32 * 64 + (8 * q + 4 * hi) * 2) = w[q];
                asm volatile("s_waitcnt lgkmcnt(0)" ::: "memory");
                const int tk0 = panel * 128 + 32 * mw;
#pragma unroll
                for (int ks = 0; ks < 2; ++ks) {
                    const lds_cptr rp = (lds_cptr)lds + 8192 + mw * 2048 + (16 * ks + 8 * (g >> 1) + qq) * 64 + (g & 1) * 32 + pp * 8;
                    const bf16x8 fr = cat(ldstr(rp), ldstr(rp + 256));
                    ST16((unsigned)WS_Z2N + (unsigned)(((((ft >> 2) * 32 + (tk0 >> 6)) * 4 + (ft & 3)) * 4 + ((tk0 >> 4) & 3) + ks) * 1024 + lane * 16), __builtin_bit_cast(u32x4, fr));
                }
                asm volatile("s_waitcnt lgkmcnt(0)" ::: "memory");
            } }
    }
    STAMP(0, 6);
}

constexpr int A_KR = 0, A_VR = 32768, A_CONST = 65536, A_FLAG = A_CONST + 4096, A_MX = A_FLAG + 64, K2_LDS = 81920;
constexpr float SAFE_T = 48.f;
#define EX(x) __builtin_amdgcn_exp2f(x)
#define BF(p) __builtin_bit_cast(bf16x8, p)

DI void attn_role(int b, char* __restrict__ ws, char* lds) {
    const u16* Qs = (const u16*)(ws + WS_Q); const u16* Kb = (const u16*)(ws + WS_K); const u16* Vt = (const u16*)(ws + WS_V); const float* kmax = (const float*)(ws + WS_KMAX); u16* Ot = (u16*)(ws + WS_O);
    const int tid = threadIdx.x, lane = tid & 63, r32 = lane & 31, hi = lane >> 5;
    const int wid = __builtin_amdgcn_readfirstlane(tid >> 6);
    const int xcd = b & 7, idx = b >> 3, head = xcd * 4 + (idx & 3), qt = idx >> 2;
    const int qp = wid & 3, kh = wid >> 2;
    const unsigned lds0 = (unsigned)(uintptr_t)lds;
    const char* kg = (const char*)(Kb + (size_t)head * S * DH);
    const char* vg = (const char*)(Vt + (size_t)head * S * DH);
    const unsigned kvoff = wid * 1024 + ((lane ^ ((lane >> 5) & 1)) * 16);
    const unsigned vvoff = wid * 1024 + lane * 16;
#define DMA_CHUNK(c) do { glds16s(kvoff, kg + (c) * 8192, lds0 + A_KR + ((c) & 3) * 8192 + wid * 1024); glds16s(vvoff, vg + (c) * 8192, lds0 + A_VR + ((c) & 3) * 8192 + wid * 1024); } while (0)
    STAMP(1, 0);
    const int q0 = qt * 256 + qp * 64;
    const bf16x8 qa = *(const bf16x8*)(Qs + ((size_t)head * S + q0 + r32) * DH + hi * 8);
    const bf16x8 qb = *(const bf16x8*)(Qs + ((size_t)head * S + q0 + 32 + r32) * DH + hi * 8);
    float km = kmax[lane * H + head];
    DMA_CHUNK(0);
    if (tid < 256) { const unsigned w = (tid & 31) ? 0u : 0x3F803F80u; u32x4 v = {w, w, w, w}; *(u32x4*)(lds + A_CONST + tid * 16) = v; }
    km = max32(max16(rowmax16(km)));
    float qna = 0.f, qnb = 0.f;
#pragma unroll
    for (int j = 0; j < 8; ++j) { const float va = bflo((u16)qa[j]), vb = bflo((u16)qb[j]); qna = fmaf(va, va, qna); qnb = fmaf(vb, vb, qnb); }
    qna = xor32(qna); qnb = xor32(qnb);
    const float mh = __builtin_amdgcn_sqrtf(fmaxf(qna, qnb) * km) * 1.001f;
    { const bool unsafe = __any(!(mh <= SAFE_T)); if (lane == 0) ((unsigned*)(lds + A_FLAG))[wid] = unsafe ? 1u : 0u; }
    const lds_cptr kp0 = (lds_cptr)lds + A_KR + kh * 4096 + r32 * 32 + ((hi ^ (r32 >> 4)) * 16);
    const bool vreal = r32 < 16;
    const unsigned vlane = vreal ? (unsigned)(A_VR + kh * 4096 + hi * 256 + r32 * 16) : (unsigned)(A_CONST + (r32 == 16 ? 0 : ((r32 >= 20 && r32 < 28) ? 64 : 16)));
    const unsigned vmask = vreal ? 0xFFFFFFFFu : 0u;
    f32x16 oa = {}, ob = {};
    WAIT_BAR(0);
    bool slow;
    { const u32x4 f0 = *(const u32x4*)(lds + A_FLAG), f1 = *(const u32x4*)(lds + A_FLAG + 16); slow = __builtin_amdgcn_readfirstlane(f0[0] | f0[1] | f0[2] | f0[3] | f1[0] | f1[1] | f1[2] | f1[3]) != 0; }
    if (!slow) {
        DMA_CHUNK(1); DMA_CHUNK(2); DMA_CHUNK(3);
        f32x16 cinit;
#pragma unroll
        for (int r = 0; r < 16; ++r) cinit[r] = -mh;
        PIN(cinit);
        bf16x8 kf, v0, v1; f32x16 sA, sB; u32x4 p0, p1;
        kf = ldsr128(kp0);
        { lds_cptr vp = (lds_cptr)lds + vlane; v0 = ldsr128(vp); v1 = ldsr128(vp + 512); }
        sA = MFMA32(kf, qa, cinit);
        p1 = (u32x4){0u, 0u, 0u, 0u};
#define HSTEP(SC, SN, QN, OP, OC, PRE1, PRE2, PRE3) do { \
        SN = MFMA32(kf, QN, cinit); PIN(SN); SBAR(); \
        PRE1; \
        SC[0] = EX(SC[0]); SC[1] = EX(SC[1]); SC[2] = EX(SC[2]); SC[3] = EX(SC[3]); SC[4] = EX(SC[4]); SC[5] = EX(SC[5]); PIN(SC); SBAR(); \
        OP = MFMA32(v1, BF(p1), OP); PIN(OP); SBAR(); \
        PRE2; \
        SC[6] = EX(SC[6]); SC[7] = EX(SC[7]); \
        p0[0] = cvtpk(SC[0], SC[1]); p0[1] = cvtpk(SC[2], SC[3]); p0[2] = cvtpk(SC[4], SC[5]); p0[3] = cvtpk(SC[6], SC[7]); PIN(p0); SBAR(); \
        SC[8] = EX(SC[8]); SC[9] = EX(SC[9]); SC[10] = EX(SC[10]); PIN(SC); SBAR(); \
        OC = MFMA32(v0, BF(p0), OC); PIN(OC); SBAR(); \
        PRE3; \
        SC[11] = EX(SC[11]); SC[12] = EX(SC[12]); SC[13] = EX(SC[13]); SC[14] = EX(SC[14]); SC[15] = EX(SC[15]); \
        p1[0] = cvtpk(SC[8], SC[9]); p1[1] = cvtpk(SC[10], SC[11]); p1[2] = cvtpk(SC[12], SC[13]); p1[3] = cvtpk(SC[14], SC[15]); PIN(p1); SBAR(); \
    } while (0)
#define BLOCK(kn, vc, vco, vn, vno) do { \
        HSTEP(sA, sB, qb, ob, oa, kf = ldsr128(kn), v1 = ldsr128((vc) + (vco) + 512), (void)0); \
        HSTEP(sB, sA, qa, oa, ob, (void)0, (void)0, v0 = ldsr128((vn) + (vno))); \
    } while (0)
        STAMP(1, 1);
        for (int c = 0; c < 8; ++c) {
            const unsigned ro = (unsigned)((c & 3) * 8192), rn = (unsigned)(((c + 1) & 3) * 8192);
            const lds_cptr vp = (lds_cptr)lds + (vlane + (ro & vmask)), vpn = (lds_cptr)lds + (vlane + (rn & vmask));
            const lds_cptr kp = kp0 + ro, kpn = kp0 + rn;
            BLOCK(kp + 1024, vp, 0, vp, 1024);
            BLOCK(kp + 2048, vp, 1024, vp, 2048);
            BLOCK(kp + 3072, vp, 2048, vp, 3072);
            if (c == 0) WAIT_BAR(4); else if (c < 6) WAIT_BAR(2); else if (c == 6) WAIT_BAR(0);
            if (c >= 1 && c + 3 < 8) DMA_CHUNK(c + 3);
            BLOCK(kpn, vp, 3072, vpn, 0);
        }
        ob = MFMA32(v1, BF(p1), ob);
#undef HSTEP
#undef BLOCK
    } else {
        float ma = -3.0e38f, mb = -3.0e38f;
        for (int pass = 0; pass < 2; ++pass) {
            for (int c = 0; c < 8; ++c) {
                LBAR();
                DMA_CHUNK(c);
                WAIT_BAR(0);
                const unsigned ro = (unsigned)((c & 3) * 8192);
                const lds_cptr vp = (lds_cptr)lds + (vlane + (ro & vmask)), kp = kp0 + ro;
                for (int j = 0; j < 4; ++j) {
                    const bf16x8 kf = ldsr128(kp + j * 1024);
                    const bf16x8 v0 = ldsr128(vp + j * 1024), v1 = ldsr128(vp + j * 1024 + 512);
#pragma unroll
                    for (int u = 0; u < 2; ++u) {
                        const f32x16 sc = MFMA32(kf, u ? qb : qa, (f32x16){});
                        float& m = u ? mb : ma; f32x16& o = u ? ob : oa;
                        if (pass == 0) {
#pragma unroll
                            for (int r = 0; r < 16; ++r) m = fmaxf(m, sc[r]);
                        } else {
                            u32x4 p0, p1;
#pragma unroll
                            for (int i = 0; i < 4; ++i) { p0[i] = cvtpk(EX(sc[2 * i] - m), EX(sc[2 * i + 1] - m)); p1[i] = cvtpk(EX(sc[8 + 2 * i] - m), EX(sc[9 + 2 * i] - m)); }
                            o = MFMA32(v0, BF(p0), o); o = MFMA32(v1, BF(p1), o);
                        }
                        SBAR();
                    }
                }
            }
            if (pass == 0) {
                ma = max32(ma); mb = max32(mb);
                float* mx = (float*)(lds + A_MX);
                if (hi == 0) { mx[(wid * 2) * 32 + r32] = ma; mx[(wid * 2 + 1) * 32 + r32] = mb; }
                LBAR();
                ma = fmaxf(ma, mx[((wid ^ 4) * 2) * 32 + r32]); mb = fmaxf(mb, mx[((wid ^ 4) * 2 + 1) * 32 + r32]);
            }
        }
    }
    STAMP(1, 2);
    __syncthreads();
    int tz = threadIdx.x; asm volatile("" : "+v"(tz));
    const int lz = tz & 63;
    float* red = (float*)lds;
    { float* dst = red + (size_t)(wid ^ 4) * 9 * 64 + lz;
#pragma unroll
      for (int r = 0; r < 9; ++r) dst[r * 64] = kh ? oa[r] : ob[r]; }
    __syncthreads();
    float o[9];
    { const float* src = red + (size_t)wid * 9 * 64 + lz;
#pragma unroll
      for (int r = 0; r < 9; ++r) o[r] = (kh ? ob[r] : oa[r]) + src[r * 64]; }
    const float rl = __builtin_amdgcn_rcpf(swap32(o[8], false));
    u16* op = Ot + ((size_t)(((q0 >> 5) + kh) * H + head) * 512) + (lz & 31) * 8 + (lz >> 5) * 4;
    st8_wt(op, (u32x2){cvtpk(o[0] * rl, o[1] * rl), cvtpk(o[2] * rl, o[3] * rl)});
    st8_wt(op + 256, (u32x2){cvtpk(o[4] * rl, o[5] * rl), cvtpk(o[6] * rl, o[7] * rl)});
    asm volatile("s_waitcnt vmcnt(0)\n\ts_barrier" ::: "memory");
    if (tz == 0) __hip_atomic_fetch_add((gu32*)(ws + WS_CNT) + (32 + qt) * 64, 1u, RLX_AGENT);
    STAMP(1, 3);
#undef DMA_CHUNK
}

DI void wait_cnt(gu32* cnt, unsigned want) {
    for (unsigned spins = 0; spins < 400000u; ++spins) {
        if ((unsigned)__builtin_amdgcn_readfirstlane(__hip_atomic_load(cnt, RLX_AGENT)) >= want) break;
        __builtin_amdgcn_s_sleep(1);
    }
}

#define O_AUX 16
#define H_AUX 16
constexpr int G_STAGE = 24576;
constexpr int W_STAGE = 16384;
constexpr int G_RED = 4 * W_STAGE;
DI void gate_role(int b, const u16* __restrict__ x, const u16* __restrict__ wo, const u16* __restrict__ wsp, const u16* __restrict__ bsp, const u16* __restrict__ wout, char* __restrict__ ws, u16* __restrict__ out, char* lds) {
    const int xcd = b & 7, idx = b >> 3;
    const int tt = xcd * 4 + (idx & 3), ct = idx >> 2;
    const int tid = threadIdx.x, lane = tid & 63, r32 = lane & 31, hi = lane >> 5;
    const int wid = __builtin_amdgcn_readfirstlane(tid >> 6);
    const unsigned lds0 = (unsigned)(uintptr_t)lds;
    gu32* hcnt = (gu32*)(ws + WS_CNT) + tt * 64; gu32* ocnt = (gu32*)(ws + WS_CNT) + (32 + (tt >> 2)) * 64;
    STAMP(1, 0);
    if (wid >= 4) {
        const int l = wid - 4;
        if (ct < 7) {
            const char* ag = ws + WS_Z2N + (size_t)ct * 32 * 16384 + l * 4096;
            const char* bg = (const char*)wsp + (size_t)(tt * 64 + l * 16) * 4096;
            const unsigned avoff = lane * 16;
            const unsigned bv0 = (unsigned)((lane >> 3) * 4096 + (((lane & 7) ^ (lane >> 4)) * 16)), bv1 = (unsigned)((8 + (lane >> 3)) * 4096 + (((lane & 7) ^ (4 + (lane >> 4))) * 16));
            auto dma = [&](int kt, int slot) __attribute__((always_inline)) { const unsigned d = lds0 + slot * G_STAGE; const char* a_ = ag + (size_t)kt * 16384;
#pragma unroll
                for (int e = 0; e < 4; ++e) glds16s(avoff, a_ + e * 1024, d + l * 4096 + e * 1024);
                glds16s(bv0, bg + kt * 128, d + 16384 + l * 2048); glds16s(bv1, bg + kt * 128, d + 16384 + l * 2048 + 1024); };
            dma(0, 0);
            WAIT_BAR(0);
            dma(1, 1); dma(2, 2);
            int slot = 0;
            for (int kt = 1; kt < 32; ++kt) {
                if (kt < 31) WAIT_BAR(6); else WAIT_BAR(0);
                if (kt + 2 < 32) dma(kt + 2, slot);
                slot = slot == 2 ? 0 : slot + 1;
            }
            LBAR();
        }
        unsigned wv[4];
#pragma unroll
        for (int e = 0; e < 4; ++e) { const int row = l * 32 + e * 8 + (lane >> 3); wv[e] = (unsigned)(row * 1024 + ct * 128 + (((lane & 7) ^ (((row >> 1) & 1) << 2)) * 16)); }
        auto dmw = [&](const u16* w, int st) __attribute__((always_inline)) { const char* wg = (const char*)w + (size_t)st * 128 * 1024; const unsigned d = lds0 + (st & 3) * W_STAGE + l * 4096;
#pragma unroll
            for (int e = 0; e < 4; ++e) glds16s(wv[e], wg, d + e * 1024); };
        dmw(wout, 0); dmw(wout, 1); dmw(wout, 2); dmw(wout, 3);
        if (l == 0) { wait_cnt(hcnt, 7);
        }
        WAIT_BAR(12);
#pragma unroll
        for (int st = 1; st < 7; ++st) {
            if (st < 4) WAIT_BAR(8); else if (st == 4) WAIT_BAR(8); else if (st == 5) WAIT_BAR(4); else WAIT_BAR(0);
            if (st + 3 < 7) dmw(wout, st + 3);
        }
        LBAR();
        dmw(wo, 0); dmw(wo, 1); dmw(wo, 2); dmw(wo, 3);
        if (l == 0) { wait_cnt(ocnt, 32);
        }
        WAIT_BAR(0);
        return;
    }
#define GPRIO 2
    __builtin_amdgcn_s_setprio(GPRIO);
    const int g = lane >> 4, qq = (lane & 15) >> 2, pp = lane & 3;
    const int th = wid & 1, fh = wid >> 1;
    const int trow = 32 * th + r32, t = tt * 64 + trow;
    if (ct < 7) {
        f32x16 acc0 = {}, acc1 = {};
        const unsigned aoff = (unsigned)(fh * 8192 + lane * 16);
        unsigned boff[4];
#pragma unroll
        for (int ks = 0; ks < 4; ++ks) boff[ks] = (unsigned)(16384 + trow * 128 + (((2 * ks + hi) ^ ((trow >> 1) & 7)) * 16));
        bf16x8 fa0[2], fa1[2], fb[2];
        auto rd = [&](int set, lds_cptr sb, int ks) __attribute__((always_inline)) {
            fb[set] = ldsr128(sb + boff[ks]);
            fa0[set] = ldsr128(sb + aoff + ks * 1024);
            fa1[set] = ldsr128(sb + aoff + 4096 + ks * 1024); };
        auto mm = [&](int set) __attribute__((always_inline)) { };
#define GSTEP(cur, nxt, sbn, ksn) do { SBAR(); acc0 = MFMA32(fa0[cur], fb[cur], acc0); SBAR(); rd(nxt, sbn, ksn); SBAR(); acc1 = MFMA32(fa1[cur], fb[cur], acc1); SBAR(); } while (0)
        const float bias = bflo(bsp[t]);
        u32x2 z[2][4];
#pragma unroll
        for (int i = 0; i < 2; ++i)
#pragma unroll
            for (int q = 0; q < 4; ++q) z[i][q] = *(const u32x2*)((const u16*)(ws + WS_Z1) + ((size_t)((t >> 5) * 28 + ct * 4 + 2 * fh + i) * 1024) + (hi * 32 + r32) * 4 + q * 256);
        LBAR();
        rd(0, (lds_cptr)lds, 0);
        int slot = 0;
        for (int kt = 0; kt < 32; ++kt) {
            const lds_cptr sb = (lds_cptr)lds + slot * G_STAGE;
            GSTEP(0, 1, sb, 1);
            GSTEP(1, 0, sb, 2);
            GSTEP(0, 1, sb, 3);
            slot = slot == 2 ? 0 : slot + 1;
            if (kt < 31) { LBAR(); GSTEP(1, 0, (lds_cptr)lds + slot * G_STAGE, 0); }
            else { acc0 = MFMA32(fa0[1], fb[1], acc0); acc1 = MFMA32(fa1[1], fb[1], acc1); }
        }
        STAMP(1, 2);
#pragma unroll
        for (int i = 0; i < 2; ++i) {
            u16* hp = (u16*)(ws + WS_H) + ((size_t)((t >> 5) * 56 + (ct * 4 + 2 * fh + i) * 2) * 512) + r32 * 8 + hi * 4;
#pragma unroll
            for (int q = 0; q < 4; ++q) {
                float v[4];
#pragma unroll
                for (int r = 0; r < 4; ++r) v[r] = (i ? acc1[4 * q + r] : acc0[4 * q + r]) + bias;
                st8_wt(hp + (q >> 1) * 512 + (q & 1) * 256, (u32x2){cvtpk(v[0] * bflo(z[i][q][0]), v[1] * bfhi(z[i][q][0])), cvtpk(v[2] * bflo(z[i][q][1]), v[3] * bfhi(z[i][q][1]))});
            }
        }
        asm volatile("s_waitcnt vmcnt(0) lgkmcnt(0)\n\ts_barrier" ::: "memory");
        if (tid == 0) __hip_atomic_fetch_add(hcnt, 1u, RLX_AGENT);
    }
    STAMP(1, 3);
    const int kh = fh;
    const int kr = 8 * (g >> 1) + qq;
    unsigned wof[2][2];
#pragma unroll
    for (int n = 0; n < 2; ++n) { wof[n][0] = (unsigned)((64 * kh + kr) * 128 + ((n ^ ((kr >> 1) & 1)) * 64) + (g & 1) * 32 + pp * 8); wof[n][1] = wof[n][0] + 512; }
    auto gemm = [&](const __amdgpu_buffer_rsrc_t tr, auto NST, bool bars, f32x16& c0, f32x16& c1) __attribute__((always_inline)) {
        constexpr int N = decltype(NST)::value;
        u32x4 tf[4][4];
#pragma unroll
        for (int st = 0; st < 4; ++st)
#pragma unroll
            for (int i = 0; i < 4; ++i) tf[st][i] = __builtin_amdgcn_raw_buffer_load_b128(tr, lane * 16 + (st * 8 + 4 * kh + i) * 1024, 0, H_AUX);
        SBAR();
#pragma unroll
        for (int st = 0; st < N; ++st) {
            if (bars && st > 0) LBAR();
            const lds_cptr sb = (lds_cptr)lds + (st & 3) * W_STAGE;
#pragma unroll
            for (int i = 0; i < 4; ++i) {
                const bf16x8 w0 = cat(ldstr(sb + wof[0][0] + i * 2048), ldstr(sb + wof[0][1] + i * 2048)), w1 = cat(ldstr(sb + wof[1][0] + i * 2048), ldstr(sb + wof[1][1] + i * 2048));
                c0 = MFMA32(w0, BF(tf[st & 3][i]), c0); c1 = MFMA32(w1, BF(tf[st & 3][i]), c1);
            }
            SBAR();
            if (st + 4 < N) {
#pragma unroll
                for (int i = 0; i < 4; ++i) tf[st & 3][i] = __builtin_amdgcn_raw_buffer_load_b128(tr, lane * 16 + ((st + 4) * 8 + 4 * kh + i) * 1024, 0, H_AUX);
                SBAR(); }
        }
    };
    auto merge = [&](f32x16& c0, f32x16& c1) __attribute__((always_inline)) {
        f32x4* dst = (f32x4*)(lds + G_RED + (wid ^ 2) * 4096) + lane;
#pragma unroll
        for (int q = 0; q < 4; ++q) dst[q * 64] = kh ? (f32x4){c0[4 * q], c0[4 * q + 1], c0[4 * q + 2], c0[4 * q + 3]} : (f32x4){c1[4 * q], c1[4 * q + 1], c1[4 * q + 2], c1[4 * q + 3]};
    };
    f32x16 gb0 = {}, gb1 = {};
    LBAR();
    gemm(__builtin_amdgcn_make_buffer_rsrc((void*)(ws + WS_H + (size_t)(tt * 2 + th) * 57344), 0, 57344, 0x00020000), std::integral_constant<int, 7>{}, true, gb0, gb1);
    merge(gb0, gb1);
    LBAR();
    { char* sc = lds + G_RED + wid * 4096;
      float gv[16];
#pragma unroll
      for (int q = 0; q < 4; ++q) { const f32x4 o4 = ((const f32x4*)sc)[q * 64 + lane];
#pragma unroll
          for (int r = 0; r < 4; ++r) { const float bb = (kh ? gb1[4 * q + r] : gb0[4 * q + r]) + o4[r]; gv[4 * q + r] = bb * __builtin_amdgcn_rcpf(1.0f + EX(-LOG2E * bb)); } }
      asm volatile("s_waitcnt lgkmcnt(0)" ::: "memory");
#pragma unroll
      for (int u = 0; u < 2; ++u) ((u32x4*)sc)[u * 64 + lane] = (u32x4){cvtpk(gv[8 * u], gv[8 * u + 1]), cvtpk(gv[8 * u + 2], gv[8 * u + 3]), cvtpk(gv[8 * u + 4], gv[8 * u + 5]), cvtpk(gv[8 * u + 6], gv[8 * u + 7])}; }
    const int n0 = ct * 64 + kh * 32 + 4 * hi;
    u32x2 xv[4];
#pragma unroll
    for (int q = 0; q < 4; ++q) xv[q] = *(const u32x2*)(x + (size_t)t * D + n0 + 8 * q);
    PIN(xv[0]); PIN(xv[1]); PIN(xv[2]); PIN(xv[3]);
    STAMP(1, 4);
    LBAR();
    STAMP(1, 5);
    f32x16 s0 = {}, s1 = {};
    {
        const __amdgpu_buffer_rsrc_t tr = __builtin_amdgcn_make_buffer_rsrc((void*)(ws + WS_O + (size_t)(tt * 2 + th) * 32768), 0, 32768, 0x00020000);
        u32x4 tf[16];
#pragma unroll
        for (int i = 0; i < 16; ++i) tf[i] = __builtin_amdgcn_raw_buffer_load_b128(tr, lane * 16 + ((i >> 2) * 8 + 4 * kh + (i & 3)) * 1024, 0, O_AUX);
        SBAR();
        bf16x8 w0[2], w1[2];
        w0[0] = cat(ldstr((lds_cptr)lds + wof[0][0]), ldstr((lds_cptr)lds + wof[0][1])); w1[0] = cat(ldstr((lds_cptr)lds + wof[1][0]), ldstr((lds_cptr)lds + wof[1][1]));
#pragma unroll
        for (int i = 0; i < 16; ++i) {
            if (i + 1 < 16) { const lds_cptr sb = (lds_cptr)lds + ((i + 1) >> 2) * W_STAGE + ((i + 1) & 3) * 2048;
                w0[(i + 1) & 1] = cat(ldstr(sb + wof[0][0]), ldstr(sb + wof[0][1])); w1[(i + 1) & 1] = cat(ldstr(sb + wof[1][0]), ldstr(sb + wof[1][1])); }
            s0 = MFMA32(w0[i & 1], BF(tf[i]), s0); s1 = MFMA32(w1[i & 1], BF(tf[i]), s1);
        }
    }
    STAMP(1, 7);
    u32x4 gq[2];
    { const u32x4* gt = (const u32x4*)(lds + G_RED + wid * 4096) + lane; gq[0] = gt[0]; gq[1] = gt[64]; }
    LBAR();
    { f32x4* dst = (f32x4*)(lds + (wid ^ 2) * 4096) + lane;
#pragma unroll
      for (int q = 0; q < 4; ++q) dst[q * 64] = kh ? (f32x4){s0[4 * q], s0[4 * q + 1], s0[4 * q + 2], s0[4 * q + 3]} : (f32x4){s1[4 * q], s1[4 * q + 1], s1[4 * q + 2], s1[4 * q + 3]}; }
    LBAR();
    { const f32x4* src = (const f32x4*)(lds + wid * 4096) + lane;
#pragma unroll
      for (int q = 0; q < 4; ++q) { const f32x4 o4 = src[q * 64]; float v[4]; const unsigned g0 = gq[q >> 1][2 * (q & 1)], g1 = gq[q >> 1][2 * (q & 1) + 1]; const float gg[4] = {bflo(g0), bfhi(g0), bflo(g1), bfhi(g1)};
#pragma unroll
          for (int r = 0; r < 4; ++r) v[r] = ((kh ? s1[4 * q + r] : s0[4 * q + r]) + o4[r]) * gg[r];
          *(u32x2*)(out + (size_t)t * D + n0 + 8 * q) = (u32x2){cvtpk(bflo(xv[q][0]) + v[0], bfhi(xv[q][0]) + v[1]), cvtpk(bflo(xv[q][1]) + v[2], bfhi(xv[q][1]) + v[3])}; } }
    STAMP(1, 6);
}

__global__ __launch_bounds__(512, 4) void k2_mix(const u16* __restrict__ x, const u16* __restrict__ wo, const u16* __restrict__ wsp, const u16* __restrict__ bsp, const u16* __restrict__ wout, char* __restrict__ ws, u16* __restrict__ out) {
    extern __shared__ __attribute__((aligned(16))) char lds[];
    if (blockIdx.x < 256) attn_role(blockIdx.x, ws, lds);
    if (blockIdx.x >= 256) gate_role(blockIdx.x - 256, x, wo, wsp, bsp, wout, ws, out, lds);
}

extern "C" void kernel_launch(void* const* d_in, const int* in_sizes, int n_in, void* d_out, int out_size, void* d_ws, size_t ws_size, hipStream_t stream) {
    const u16* x = (const u16*)d_in[0]; const u16* wqkv = (const u16*)d_in[1]; const u16* wo = (const u16*)d_in[2];
    const u16* win = (const u16*)d_in[4]; const u16* wsp = (const u16*)d_in[5]; const u16* bsp = (const u16*)d_in[6]; const u16* wout = (const u16*)d_in[7];
    char* ws = (char*)d_ws;
    (void)hipFuncSetAttribute((const void*)k1_proj, hipFuncAttributeMaxDynamicSharedMemorySize, K1_LDS);
    (void)hipFuncSetAttribute((const void*)k2_mix, hipFuncAttributeMaxDynamicSharedMemorySize, K2_LDS);
    k1_proj<<<256, 768, K1_LDS, stream>>>(x, wqkv, win, ws);
    k2_mix<<<512, 512, K2_LDS, stream>>>(x, wo, wsp, bsp, wout, ws, (u16*)d_out);
}
```

```cpp
#include <hip/hip_runtime.h>
#include <hip/hip_bf16.h>
#include <stdint.h>
#include <type_traits>

typedef unsigned short u16;
using bf16x8 = __attribute__((ext_vector_type(8))) short;
using s16x4 = __attribute__((ext_vector_type(4))) short;
using f32x16 = __attribute__((ext_vector_type(16))) float;
using f32x4 = __attribute__((ext_vector_type(4))) float;
using u32x4 = __attribute__((ext_vector_type(4))) unsigned;
using u32x2 = __attribute__((ext_vector_type(2))) unsigned;
typedef __bf16 bf16x2_t __attribute__((ext_vector_type(2)));
typedef float f32x2_t __attribute__((ext_vector_type(2)));
typedef short v4i16_t __attribute__((ext_vector_type(4)));
#define LDSAS __attribute__((address_space(3)))
typedef LDSAS const char* lds_cptr;
#define MFMA32(a, b, c) __builtin_amdgcn_mfma_f32_32x32x16_bf16(a, b, c, 0, 0, 0)
#define MFMA16(a, b, c) __builtin_amdgcn_mfma_f32_16x16x32_bf16(a, b, c, 0, 0, 0)
#define SBAR() __builtin_amdgcn_sched_barrier(0)
#define PIN(x) asm volatile("" : "+v"(x))
#define WAIT_BAR(N) asm volatile("s_waitcnt vmcnt(" #N ") lgkmcnt(0)\n\ts_barrier" ::: "memory")
#define LBAR() asm volatile("s_waitcnt lgkmcnt(0)\n\ts_barrier" ::: "memory")
#define DI __device__ __forceinline__
#define ST8(p, w) st8_wt((void*)(p), (w))
#define ST16(off, v) __builtin_amdgcn_raw_buffer_store_b128((v), wsr, (off), 0, 16)
#define STAMP(kern, i) do { } while (0)

constexpr int S = 2048, D = 512, H = 32, DH = 16, FH = 896;
constexpr float LOG2E = 1.4426950408889634f;

constexpr size_t WS_Q = 0, WS_K = 2u << 20, WS_V = 4u << 20, WS_Z1 = 6u << 20, WS_Z2N = WS_Z1 + 3670016, WS_H = WS_Z2N + 3670016, WS_O = WS_H + 3670016,
                 WS_XCH = WS_O + (2u << 20), WS_XST = WS_XCH + (512u << 10), WS_KMAX = WS_XST + (32u << 10), WS_EPOCH = WS_KMAX + (8u << 10), WS_CNT = WS_EPOCH + (4u << 10), WS_END = WS_CNT + (32u << 10);

DI unsigned cvtpk(float lo, float hi) { f32x2_t v = {lo, hi}; return __builtin_bit_cast(unsigned, __builtin_convertvector(v, bf16x2_t)); }
DI float bflo(unsigned w) { return __uint_as_float(w << 16); }
DI float bfhi(unsigned w) { return __uint_as_float(w & 0xFFFF0000u); }
DI float dot2(unsigned a, unsigned b, float c) { return __builtin_amdgcn_fdot2_f32_bf16(__builtin_bit_cast(bf16x2_t, a), __builtin_bit_cast(bf16x2_t, b), c, false); }
DI void glds16s(unsigned voff, const void* sbase, unsigned lds_base) {
    lds_base = __builtin_amdgcn_readfirstlane(lds_base);
    unsigned sv; asm volatile("s_mov_b32 %0, m0\n\ts_mov_b32 m0, %3\n\ts_nop 0\n\tglobal_load_lds_dwordx4 %1, %2\n\ts_mov_b32 m0, %0" : "=&s"(sv) : "v"(voff), "s"(sbase), "s"(lds_base) : "memory"); }
DI bf16x8 ldsr128(lds_cptr p) { return *(const LDSAS bf16x8*)p; }
DI s16x4 ldstr(lds_cptr p) { return __builtin_bit_cast(s16x4, __builtin_amdgcn_ds_read_tr16_b64_v4i16((LDSAS v4i16_t*)p)); }
DI bf16x8 cat(s16x4 a, s16x4 b) { return __builtin_shufflevector(a, b, 0, 1, 2, 3, 4, 5, 6, 7); }
DI float swap32(float x, bool sum) { auto rr = __builtin_amdgcn_permlane32_swap(__float_as_uint(x), __float_as_uint(x), false, false); return sum ? __uint_as_float(rr[0]) + __uint_as_float(rr[1]) : __uint_as_float(rr[0]); }
DI float gelu_t(float x) {
    const float c1 = -2.0f * 0.7978845608028654f * LOG2E, c3 = c1 * 0.044715f;
    const float u = x * fmaf(x * x, c3, c1);
    return x * __builtin_amdgcn_rcpf(1.0f + __builtin_amdgcn_exp2f(u));
}

constexpr int K1_XB = 16384;
constexpr int K1_STAGE = K1_XB + 7 * 4096;
constexpr int K1_CS = 3 * K1_STAGE;
constexpr int K1_PF = K1_CS + 1024;
constexpr int K1_LDS = K1_PF + 4096;
typedef __attribute__((address_space(1))) unsigned gu32; typedef __attribute__((address_space(1))) unsigned long long gu64;
#define RLX_AGENT __ATOMIC_RELAXED, __HIP_MEMORY_SCOPE_AGENT

DI float xor16(float x) { auto r = __builtin_amdgcn_permlane16_swap(__float_as_uint(x), __float_as_uint(x), false, false); return __uint_as_float(r[0]) + __uint_as_float(r[1]); }
DI float xor32(float x) { auto r = __builtin_amdgcn_permlane32_swap(__float_as_uint(x), __float_as_uint(x), false, false); return __uint_as_float(r[0]) + __uint_as_float(r[1]); }
DI float max16(float x) { auto r = __builtin_amdgcn_permlane16_swap(__float_as_uint(x), __float_as_uint(x), false, false); return fmaxf(__uint_as_float(r[0]), __uint_as_float(r[1])); }
DI float max32(float x) { auto r = __builtin_amdgcn_permlane32_swap(__float_as_uint(x), __float_as_uint(x), false, false); return fmaxf(__uint_as_float(r[0]), __uint_as_float(r[1])); }
template <int CTRL> DI float dppf(float x) { return __uint_as_float(__builtin_amdgcn_update_dpp(0, __float_as_uint(x), CTRL, 0xF, 0xF, false)); }
DI float rowmax16(float x) { x = fmaxf(x, dppf<0x128>(x)); x = fmaxf(x, dppf<0x124>(x)); x = fmaxf(x, dppf<0x122>(x)); x = fmaxf(x, dppf<0x121>(x)); return x; }
DI float wavesum(float x) { x += dppf<0x128>(x); x += dppf<0x124>(x); x += dppf<0x122>(x); x += dppf<0x121>(x); return xor32(xor16(x)); }
DI u32x4 widen(u32x2 a, u32x2 b) { auto r0 = __builtin_amdgcn_permlane32_swap(a[0], b[0], false, false); auto r1 = __builtin_amdgcn_permlane32_swap(a[1], b[1], false, false); return (u32x4){r0[0], r1[0], r0[1], r1[1]}; }

DI void st8_wt(void* p, u32x2 w) { __hip_atomic_store((gu64*)p, ((unsigned long long)w[1] << 32) | w[0], RLX_AGENT); }
__global__ __launch_bounds__(768, 3) void k1_proj(const u16* __restrict__ x, const u16* __restrict__ wqkv, const u16* __restrict__ win, const u16* __restrict__ wsp, const u16* __restrict__ wout, const u16* __restrict__ wo, char* __restrict__ ws) {
    extern __shared__ __attribute__((aligned(16))) char lds[];
    const int tid = threadIdx.x, lane = tid & 63, r32 = lane & 31, hi = lane >> 5;
    const int wid = __builtin_amdgcn_readfirstlane(tid >> 6);
    const int b = blockIdx.x, xcd = b & 7, idx = b >> 3;
    const int panel = 8 * (xcd >> 2) + (idx & 7), j = 4 * (xcd & 3) + (idx >> 3);
    const unsigned lds0 = (unsigned)(uintptr_t)lds;
    const int jq = j & 3, u7 = 7 * (j >> 2);
    const int n1 = jq == 3 ? 1 : 2, n2 = jq == 1 ? 1 : 2;
    const int z1t0 = u7 + 2 * jq, z2t0 = u7 + (jq == 0 ? 0 : (jq == 1 ? 2 : (jq == 2 ? 3 : 5)));
    STAMP(0, 0);
    if (b == 0 && tid < 40) ((unsigned*)(ws + WS_CNT))[tid * 64] = 0u;
    if (wid >= 8) {
        const int l = wid - 8;
        const char* xb = (const char*)x + (size_t)(panel * 128 + l * 32) * 1024;
        const unsigned xv0 = (unsigned)((lane >> 3) * 1024 + (((lane & 7) ^ (lane >> 4)) * 16)), xv1 = (unsigned)((lane >> 3) * 1024 + (((lane & 7) ^ (4 + (lane >> 4))) * 16));
        const unsigned vq = (unsigned)((16 * l + (lane >> 2)) * 3072 + (lane & 3) * 16), vz = (unsigned)((16 * l + (lane >> 2)) * 3584 + (lane & 3) * 16);
        const char* wq = (const char*)wqkv + j * 64;
        int zc[4];
#pragma unroll
        for (int i = 0; i < 4; ++i) { const int ii = i < n1 + n2 ? i : n1 + n2 - 1; zc[i] = ii < n1 ? (z1t0 + ii) * 64 : FH * 2 + (z2t0 + ii - n1) * 64; }
        const char* pfb = (const char*)wsp + (size_t)(256 * xcd + 8 * idx + 2 * l) * 4096;
        auto dma = [&](int st) __attribute__((always_inline)) {
            const unsigned d = lds0 + (st % 3) * K1_STAGE;
#pragma unroll
            for (int p = 0; p < 4; ++p) glds16s((p & 1) ? xv1 : xv0, xb + st * 128 + p * 8192, d + l * 4096 + p * 1024);
            const char* bq = wq + (size_t)st * 64 * 3072; const char* bz = (const char*)win + (size_t)st * 64 * 3584; const unsigned dw = d + K1_XB + l * 1024;
            glds16s(vq, bq + 2048, dw); glds16s(vq, bq, dw + 4096); glds16s(vq, bq + 1024, dw + 8192);
#pragma unroll
            for (int i = 0; i < 4; ++i) glds16s(vz, bz + zc[i], dw + (3 + i) * 4096);
            glds16s(lane * 16, pfb + st * 1024, lds0 + K1_PF + l * 1024);
        };
        dma(0);
        WAIT_BAR(0);
        dma(1); dma(2);
#pragma unroll
        for (int st = 1; st < 8; ++st) {
            if (st < 7) WAIT_BAR(12); else WAIT_BAR(0);
            if (st + 2 < 8) dma(st + 2);
        }
        LBAR();
        {
            if ((idx & 3) == 0) {
                unsigned long long pc;
                asm volatile("s_getpc_b64 s[98:99]\n\ts_add_u32 s98, s98, _Z6k2_mixPKtS0_S0_S0_S0_PcPt@rel32@lo+4\n\ts_addc_u32 s99, s99, _Z6k2_mixPKtS0_S0_S0_S0_PcPt@rel32@hi+12\n\ts_mov_b64 %0, s[98:99]" : "=s"(pc) :: "scc", "s98", "s99");
                const u32x4* cp = (const u32x4*)(pc + (size_t)l * 4096) + lane;
                u32x4 a = {0u, 0u, 0u, 0u};
#pragma unroll
                for (int i = 0; i < 4; ++i) a ^= cp[i * 64];
                asm volatile("" :: "v"(a));
            }
        }
        return;
    }
    const int mw = wid & 3, nh = wid >> 2;
    gu32* epw = (gu32*)(ws + WS_EPOCH) + panel * 64;
    unsigned tag = __hip_atomic_load(epw, RLX_AGENT);
    u32x4 xr = *(const u32x4*)(x + (size_t)(panel * 128 + 8 * j + wid) * D + lane * 8);
    auto rowstats = [&]() __attribute__((always_inline)) {
        PIN(xr); PIN(tag); tag += 1u;
        float v[8];
#pragma unroll
        for (int i = 0; i < 4; ++i) { v[2 * i] = bflo(xr[i]); v[2 * i + 1] = bfhi(xr[i]); }
        const float m = wavesum(((v[0] + v[1]) + (v[2] + v[3])) + ((v[4] + v[5]) + (v[6] + v[7]))) * (1.0f / 512.0f);
        float q = 0.f;
#pragma unroll
        for (int i = 0; i < 8; ++i) q = fmaf(v[i] - m, v[i] - m, q);
        const float r = __builtin_amdgcn_rsqf(wavesum(q) * (1.0f / 512.0f) + 1e-5f);
        if (lane < 2) __hip_atomic_store((gu64*)(ws + WS_XST) + (size_t)(panel * 128 + 8 * j + wid) * 2 + lane, ((unsigned long long)tag << 32) | __float_as_uint(lane ? r : m), RLX_AGENT);
    };
    f32x16 acc[4];
#pragma unroll
    for (int s = 0; s < 4; ++s) acc[s] = (f32x16){};
    float cs0 = 0.f, cs1 = 0.f;
    const int g = lane >> 4, qq = (lane & 15) >> 2, pp = lane & 3;
    const int trow = 32 * mw + r32;
    unsigned xo[4];
#pragma unroll
    for (int ks = 0; ks < 4; ++ks) xo[ks] = (unsigned)(trow * 128 + (((2 * ks + hi) ^ ((trow >> 1) & 7)) * 16));
    const unsigned wlane = (unsigned)(K1_XB + (8 * (g >> 1) + qq) * 64 + (g & 1) * 32 + pp * 8);
    const __amdgpu_buffer_rsrc_t sr = __builtin_amdgcn_make_buffer_rsrc((void*)(ws + WS_XST + (size_t)panel * 128 * 16), 0, 128 * 16, 0x00020000);
    u32x4 pst;
    int tile[4];
    auto mainloop = [&](auto NHC) __attribute__((always_inline)) {
        constexpr int NHV = decltype(NHC)::value; constexpr int NS = NHV == 0 ? 4 : 3;
        unsigned wo[NS];
#pragma unroll
        for (int s = 0; s < NS; ++s) { int t = s + mw; t = t >= NS ? t - NS : t; t = t >= NS ? t - NS : t; tile[s] = 4 * NHV + t; wo[s] = wlane + tile[s] * 4096; }
        bf16x8 xa[2], wf[2][NS];
        auto rd = [&](const int set, const lds_cptr sb, const int ks) __attribute__((always_inline)) {
            xa[set] = ldsr128(sb + xo[ks]);
#pragma unroll
            for (int s = 0; s < NS; ++s) wf[set][s] = cat(ldstr(sb + wo[s] + ks * 1024), ldstr(sb + wo[s] + ks * 1024 + 256));
        };
        auto mm = [&](const int set) __attribute__((always_inline)) {
            SBAR();
#pragma unroll
            for (int s = 0; s < NS; ++s) acc[s] = MFMA32(wf[set][s], xa[set], acc[s]);
            const u32x4 w = __builtin_bit_cast(u32x4, wf[set][0]);
#pragma unroll
            for (int i = 0; i < 4; ++i) { cs0 += bflo(w[i]); cs1 += bfhi(w[i]); }
            PIN(cs0); PIN(cs1);
#pragma unroll
            for (int i = 0; i < NS; ++i) { __builtin_amdgcn_sched_group_barrier(0x008, 1, 0); __builtin_amdgcn_sched_group_barrier(0x002, NS == 4 ? 4 : 6, 0); }
            SBAR();
        };
        LBAR();
        STAMP(0, 1);
        rd(0, (lds_cptr)lds, 0);
#pragma unroll
        for (int st = 0; st < 8; ++st) {
            const lds_cptr sb = (lds_cptr)lds + (st % 3) * K1_STAGE;
            rd(1, sb, 1); mm(0);
            rd(0, sb, 2); mm(1);
            rd(1, sb, 3); mm(0);
            if (st < 7) { LBAR(); rd(0, (lds_cptr)lds + ((st + 1) % 3) * K1_STAGE, 0); }
            if (st == 1) rowstats();
            if (st == 5) pst = __builtin_amdgcn_raw_buffer_load_b128(sr, trow * 16, 0, 16);
            mm(1);
        }
    };
    if (nh == 0) mainloop(std::integral_constant<int, 0>{}); else mainloop(std::integral_constant<int, 1>{});
    STAMP(0, 2);
    float* csl = (float*)(lds + K1_CS);
    { const float c = xor32(cs0 + cs1); if (hi == 0) csl[tile[0] * 32 + r32] = c; }
    const int tok = panel * 128 + trow;
    float mu, rs;
    {   u32x4 p = pst;
        for (unsigned spins = 0; !__all((p[1] == tag) & (p[3] == tag)) && spins < 200000u; ++spins) {
            __builtin_amdgcn_s_sleep(1);
            p = __builtin_amdgcn_raw_buffer_load_b128(sr, trow * 16, 0, 16);
        }
        mu = __uint_as_float(p[0]); rs = __uint_as_float(p[2]); }
    LBAR();
    STAMP(0, 3);
    auto lnv = [&](int s, float (&v)[16], float sc) __attribute__((always_inline)) {
#pragma unroll
        for (int q = 0; q < 4; ++q) { const f32x4 c = *(const f32x4*)(csl + tile[s] * 32 + 8 * q + 4 * hi);
#pragma unroll
            for (int r = 0; r < 4; ++r) v[4 * q + r] = sc * (acc[s][4 * q + r] - mu * c[r]); } };
    const __amdgpu_buffer_rsrc_t wsr = __builtin_amdgcn_make_buffer_rsrc((void*)ws, 0, 32u << 20, 0x00020000);
    u16* Z1 = (u16*)(ws + WS_Z1);
    auto z1store = [&](int zt, const u32x2 (&w)[4]) __attribute__((always_inline)) {
        u16* p = Z1 + ((size_t)((tok >> 5) * 28 + zt) * 1024) + (hi * 32 + r32) * 4;
#pragma unroll
        for (int q = 0; q < 4; ++q) ST8(p + q * 256, w[q]); };
    auto gelu16 = [&](int s, u32x2 (&w)[4]) __attribute__((always_inline)) { float v[16]; lnv(s, v, rs);
#pragma unroll
        for (int q = 0; q < 4; ++q) w[q] = (u32x2){cvtpk(gelu_t(v[4 * q]), gelu_t(v[4 * q + 1])), cvtpk(gelu_t(v[4 * q + 2]), gelu_t(v[4 * q + 3]))}; };
    if (nh == 0) {
#pragma unroll
        for (int s = 0; s < 4; ++s) {
            const int T = tile[s];
            if (T == 3) { u32x2 w[4]; gelu16(s, w); z1store(z1t0, w); }
            else {
                float v[16]; lnv(s, v, T == 1 ? rs * (0.25f * LOG2E) : rs);
                u32x2 w[4];
#pragma unroll
                for (int q = 0; q < 4; ++q) w[q] = (u32x2){cvtpk(v[4 * q], v[4 * q + 1]), cvtpk(v[4 * q + 2], v[4 * q + 3])};
                if (T == 0) {
                    char* sc = lds + mw * 2048;
#pragma unroll
                    for (int q = 0; q < 4; ++q) *(u32x2*)(sc + r32 * 64 + (8 * q + 4 * hi) * 2) = w[q];
                    asm volatile("s_waitcnt lgkmcnt(0)" ::: "memory");
                    u16* Vt = (u16*)(ws + WS_V);
#pragma unroll
                    for (int hh = 0; hh < 2; ++hh)
#pragma unroll
                        for (int ks = 0; ks < 2; ++ks) {
                            const s16x4 t4 = ldstr((lds_cptr)lds + mw * 2048 + (16 * ks + 8 * (g >> 1) + 4 * (g & 1) + qq) * 64 + hh * 32 + pp * 8);
                            ST8(Vt + ((size_t)(2 * j + hh) * 128 + ((panel * 128 + 32 * mw) >> 4) + ks) * 256 + (g & 1) * 128 + (lane & 15) * 8 + (g >> 1) * 4, __builtin_bit_cast(u32x2, t4)); }
                } else {
                    const unsigned dsto = (unsigned)(T == 1 ? WS_Q : WS_K);
#pragma unroll
                    for (int hh = 0; hh < 2; ++hh) ST16(dsto + (((2 * j + hh) * S + tok) * 16 + 8 * hi) * 2, widen(w[2 * hh], w[2 * hh + 1]));
                    if (T == 2) {
                        float* kmax = (float*)(ws + WS_KMAX);
#pragma unroll
                        for (int hh = 0; hh < 2; ++hh) { float n = 0.f;
#pragma unroll
                            for (int q = 0; q < 2; ++q) { const u32x2 ww = w[2 * hh + q]; n = fmaf(bflo(ww[0]), bflo(ww[0]), n); n = fmaf(bfhi(ww[0]), bfhi(ww[0]), n); n = fmaf(bflo(ww[1]), bflo(ww[1]), n); n = fmaf(bfhi(ww[1]), bfhi(ww[1]), n); }
                            n = max16(rowmax16(xor32(n)));
                            if (lane == 0) kmax[(panel * 4 + mw) * H + 2 * j + hh] = n; }
                    }
                }
            }
        }
    } else {
        __builtin_amdgcn_s_setprio(2);
        float sm = 0.f;
#pragma unroll
        for (int s = 0; s < 3; ++s) { const int zi = tile[s] - 3;
            if (zi >= n1 && zi < n1 + n2) { float v[16]; lnv(s, v, rs);
#pragma unroll
                for (int r = 0; r < 16; ++r) { acc[s][r] = gelu_t(v[r]); sm += acc[s][r]; } } }
        sm = xor32(sm);
        const float ml = sm * (n2 == 2 ? (1.0f / 64.f) : (1.0f / 32.f)); float m2 = 0.f;
#pragma unroll
        for (int s = 0; s < 3; ++s) { const int zi = tile[s] - 3;
            if (zi >= n1 && zi < n1 + n2) {
#pragma unroll
                for (int r = 0; r < 16; ++r) { const float d = acc[s][r] - ml; m2 = fmaf(d, d, m2); } } }
        m2 = xor32(m2);
        __hip_atomic_store((gu64*)(ws + WS_XCH) + ((size_t)(panel * 16 + j) * 128 + trow) * 2 + hi, ((unsigned long long)tag << 32) | __float_as_uint(hi ? m2 : sm), RLX_AGENT);
        __builtin_amdgcn_s_setprio(0);
        STAMP(0, 4);
#pragma unroll
        for (int s = 0; s < 3; ++s) { const int zi = tile[s] - 3; if (zi < n1) { u32x2 w[4]; gelu16(s, w); z1store(z1t0 + zi, w); } }
        float mean, rstd;
        {
            const __amdgpu_buffer_rsrc_t xr = __builtin_amdgcn_make_buffer_rsrc((void*)(ws + WS_XCH + (size_t)panel * 16 * 128 * 16), 0, 16 * 128 * 16, 0x00020000);
            u32x4 p[8];
            for (unsigned spins = 0;; ++spins) {
                bool ok = true;
#pragma unroll
                for (int u = 0; u < 8; ++u) p[u] = __builtin_amdgcn_raw_buffer_load_b128(xr, ((8 * hi + u) * 128 + trow) * 16, 0, 16);
#pragma unroll
                for (int u = 0; u < 8; ++u) ok &= (p[u][1] == tag) & (p[u][3] == tag);
                if (__all(ok) || spins > 200000u) break;
                __builtin_amdgcn_s_sleep(1);
            }
            float tot = 0.f;
#pragma unroll
            for (int u = 0; u < 8; ++u) tot += __uint_as_float(p[u][0]);
            mean = xor32(tot) * (1.0f / FH);
            float mm2 = 0.f;
#pragma unroll
            for (int u = 0; u < 8; ++u) { const float nl = (u & 3) == 1 ? 32.f : 64.f; const float d = __uint_as_float(p[u][0]) * ((u & 3) == 1 ? (1.0f / 32.f) : (1.0f / 64.f)) - mean; mm2 += __uint_as_float(p[u][2]) + nl * d * d; }
            rstd = __builtin_amdgcn_rsqf(xor32(mm2) * (1.0f / FH) + 1e-5f);
        }
        STAMP(0, 5);
        if (wid == 4 && lane == 0) __hip_atomic_store(epw, tag, RLX_AGENT);
        const float nmr = -mean * rstd;
#pragma unroll
        for (int s = 0; s < 3; ++s) { const int zi = tile[s] - 3;
            if (zi >= n1 && zi < n1 + n2) {
                const int ft = z2t0 + zi - n1;
                u32x2 w[4];
#pragma unroll
                for (int q = 0; q < 4; ++q) w[q] = (u32x2){cvtpk(fmaf(acc[s][4 * q], rstd, nmr), fmaf(acc[s][4 * q + 1], rstd, nmr)), cvtpk(fmaf(acc[s][4 * q + 2], rstd, nmr), fmaf(acc[s][4 * q + 3], rstd, nmr))};
                char* sc = lds + 8192 + mw * 2048;
#pragma unroll
                for (int q = 0; q < 4; ++q) *(u32x2*)(sc + r32 * 64 + (8 * q + 4 * hi) * 2) = w[q];
                asm volatile("s_waitcnt lgkmcnt(0)" ::: "memory");
                const int tk0 = panel * 128 + 32 * mw;
#pragma unroll
                for (int ks = 0; ks < 2; ++ks) {
                    const lds_cptr rp = (lds_cptr)lds + 8192 + mw * 2048 + (16 * ks + 8 * (g >> 1) + qq) * 64 + (g & 1) * 32 + pp * 8;
                    const bf16x8 fr = cat(ldstr(rp), ldstr(rp + 256));
                    ST16((unsigned)WS_Z2N + (unsigned)(((((ft >> 2) * 32 + (tk0 >> 6)) * 4 + (ft & 3)) * 4 + ((tk0 >> 4) & 3) + ks) * 1024 + lane * 16), __builtin_bit_cast(u32x4, fr));
                }
                asm volatile("s_waitcnt lgkmcnt(0)" ::: "memory");
            } }
    }
    STAMP(0, 6);
}

constexpr int A_KR = 0, A_VR = 32768, A_CONST = 65536, A_FLAG = A_CONST + 4096, A_MX = A_FLAG + 64, K2_LDS = 81920;
constexpr float SAFE_T = 48.f;
#define EX(x) __builtin_amdgcn_exp2f(x)
#define BF(p) __builtin_bit_cast(bf16x8, p)

DI void attn_role(int b, char* __restrict__ ws, char* lds) {
    const u16* Qs = (const u16*)(ws + WS_Q); const u16* Kb = (const u16*)(ws + WS_K); const u16* Vt = (const u16*)(ws + WS_V); const float* kmax = (const float*)(ws + WS_KMAX); u16* Ot = (u16*)(ws + WS_O);
    const int tid = threadIdx.x, lane = tid & 63, r32 = lane & 31, hi = lane >> 5;
    const int wid = __builtin_amdgcn_readfirstlane(tid >> 6);
    const int xcd = b & 7, idx = b >> 3, head = xcd * 4 + (idx & 3), qt = idx >> 2;
    const int qp = wid & 3, kh = wid >> 2;
    const unsigned lds0 = (unsigned)(uintptr_t)lds;
    const char* kg = (const char*)(Kb + (size_t)head * S * DH);
    const char* vg = (const char*)(Vt + (size_t)head * S * DH);
    const unsigned kvoff = wid * 1024 + ((lane ^ ((lane >> 5) & 1)) * 16);
    const unsigned vvoff = wid * 1024 + lane * 16;
#define DMA_CHUNK(c) do { glds16s(kvoff, kg + (c) * 8192, lds0 + A_KR + ((c) & 3) * 8192 + wid * 1024); glds16s(vvoff, vg + (c) * 8192, lds0 + A_VR + ((c) & 3) * 8192 + wid * 1024); } while (0)
    STAMP(1, 0);
    const int q0 = qt * 256 + qp * 64;
    const bf16x8 qa = *(const bf16x8*)(Qs + ((size_t)head * S + q0 + r32) * DH + hi * 8);
    const bf16x8 qb = *(const bf16x8*)(Qs + ((size_t)head * S + q0 + 32 + r32) * DH + hi * 8);
    float km = kmax[lane * H + head];
    DMA_CHUNK(0);
    if (tid < 256) { const unsigned w = (tid & 31) ? 0u : 0x3F803F80u; u32x4 v = {w, w, w, w}; *(u32x4*)(lds + A_CONST + tid * 16) = v; }
    km = max32(max16(rowmax16(km)));
    float qna = 0.f, qnb = 0.f;
#pragma unroll
    for (int j = 0; j < 8; ++j) { const float va = bflo((u16)qa[j]), vb = bflo((u16)qb[j]); qna = fmaf(va, va, qna); qnb = fmaf(vb, vb, qnb); }
    qna = xor32(qna); qnb = xor32(qnb);
    const float mh = __builtin_amdgcn_sqrtf(fmaxf(qna, qnb) * km) * 1.001f;
    { const bool unsafe = __any(!(mh <= SAFE_T)); if (lane == 0) ((unsigned*)(lds + A_FLAG))[wid] = unsafe ? 1u : 0u; }
    const lds_cptr kp0 = (lds_cptr)lds + A_KR + kh * 4096 + r32 * 32 + ((hi ^ (r32 >> 4)) * 16);
    const bool vreal = r32 < 16;
    const unsigned vlane = vreal ? (unsigned)(A_VR + kh * 4096 + hi * 256 + r32 * 16) : (unsigned)(A_CONST + (r32 == 16 ? 0 : ((r32 >= 20 && r32 < 28) ? 64 : 16)));
    const unsigned vmask = vreal ? 0xFFFFFFFFu : 0u;
    f32x16 oa = {}, ob = {};
    WAIT_BAR(0);
    bool slow;
    { const u32x4 f0 = *(const u32x4*)(lds + A_FLAG), f1 = *(const u32x4*)(lds + A_FLAG + 16); slow = __builtin_amdgcn_readfirstlane(f0[0] | f0[1] | f0[2] | f0[3] | f1[0] | f1[1] | f1[2] | f1[3]) != 0; }
    if (!slow) {
        DMA_CHUNK(1); DMA_CHUNK(2); DMA_CHUNK(3);
        f32x16 cinit;
#pragma unroll
        for (int r = 0; r < 16; ++r) cinit[r] = -mh;
        PIN(cinit);
        bf16x8 kf, v0, v1; f32x16 sA, sB; u32x4 p0, p1;
        kf = ldsr128(kp0);
        { lds_cptr vp = (lds_cptr)lds + vlane; v0 = ldsr128(vp); v1 = ldsr128(vp + 512); }
        sA = MFMA32(kf, qa, cinit);
        p1 = (u32x4){0u, 0u, 0u, 0u};
#define HSTEP(SC, SN, QN, OP, OC, PRE1, PRE2, PRE3) do { \
        SN = MFMA32(kf, QN, cinit); PIN(SN); SBAR(); \
        PRE1; \
        SC[0] = EX(SC[0]); SC[1] = EX(SC[1]); SC[2] = EX(SC[2]); SC[3] = EX(SC[3]); SC[4] = EX(SC[4]); SC[5] = EX(SC[5]); PIN(SC); SBAR(); \
        OP = MFMA32(v1, BF(p1), OP); PIN(OP); SBAR(); \
        PRE2; \
        SC[6] = EX(SC[6]); SC[7] = EX(SC[7]); \
        p0[0] = cvtpk(SC[0], SC[1]); p0[1] = cvtpk(SC[2], SC[3]); p0[2] = cvtpk(SC[4], SC[5]); p0[3] = cvtpk(SC[6], SC[7]); PIN(p0); SBAR(); \
        SC[8] = EX(SC[8]); SC[9] = EX(SC[9]); SC[10] = EX(SC[10]); PIN(SC); SBAR(); \
        OC = MFMA32(v0, BF(p0), OC); PIN(OC); SBAR(); \
        PRE3; \
        SC[11] = EX(SC[11]); SC[12] = EX(SC[12]); SC[13] = EX(SC[13]); SC[14] = EX(SC[14]); SC[15] = EX(SC[15]); \
        p1[0] = cvtpk(SC[8], SC[9]); p1[1] = cvtpk(SC[10], SC[11]); p1[2] = cvtpk(SC[12], SC[13]); p1[3] = cvtpk(SC[14], SC[15]); PIN(p1); SBAR(); \
    } while (0)
#define BLOCK(kn, vc, vco, vn, vno) do { \
        HSTEP(sA, sB, qb, ob, oa, kf = ldsr128(kn), v1 = ldsr128((vc) + (vco) + 512), (void)0); \
        HSTEP(sB, sA, qa, oa, ob, (void)0, (void)0, v0 = ldsr128((vn) + (vno))); \
    } while (0)
        STAMP(1, 1);
        for (int c = 0; c < 8; ++c) {
            const unsigned ro = (unsigned)((c & 3) * 8192), rn = (unsigned)(((c + 1) & 3) * 8192);
            const lds_cptr vp = (lds_cptr)lds + (vlane + (ro & vmask)), vpn = (lds_cptr)lds + (vlane + (rn & vmask));
            const lds_cptr kp = kp0 + ro, kpn = kp0 + rn;
            BLOCK(kp + 1024, vp, 0, vp, 1024);
            BLOCK(kp + 2048, vp, 1024, vp, 2048);
            BLOCK(kp + 3072, vp, 2048, vp, 3072);
            if (c == 0) WAIT_BAR(4); else if (c < 6) WAIT_BAR(2); else if (c == 6) WAIT_BAR(0);
            if (c >= 1 && c + 3 < 8) DMA_CHUNK(c + 3);
            BLOCK(kpn, vp, 3072, vpn, 0);
        }
        ob = MFMA32(v1, BF(p1), ob);
#undef HSTEP
#undef BLOCK
    } else {
        float ma = -3.0e38f, mb = -3.0e38f;
        for (int pass = 0; pass < 2; ++pass) {
            for (int c = 0; c < 8; ++c) {
                LBAR();
                DMA_CHUNK(c);
                WAIT_BAR(0);
                const unsigned ro = (unsigned)((c & 3) * 8192);
                const lds_cptr vp = (lds_cptr)lds + (vlane + (ro & vmask)), kp = kp0 + ro;
                for (int j = 0; j < 4; ++j) {
                    const bf16x8 kf = ldsr128(kp + j * 1024);
                    const bf16x8 v0 = ldsr128(vp + j * 1024), v1 = ldsr128(vp + j * 1024 + 512);
#pragma unroll
                    for (int u = 0; u < 2; ++u) {
                        const f32x16 sc = MFMA32(kf, u ? qb : qa, (f32x16){});
                        float& m = u ? mb : ma; f32x16& o = u ? ob : oa;
                        if (pass == 0) {
#pragma unroll
                            for (int r = 0; r < 16; ++r) m = fmaxf(m, sc[r]);
                        } else {
                            u32x4 p0, p1;
#pragma unroll
                            for (int i = 0; i < 4; ++i) { p0[i] = cvtpk(EX(sc[2 * i] - m), EX(sc[2 * i + 1] - m)); p1[i] = cvtpk(EX(sc[8 + 2 * i] - m), EX(sc[9 + 2 * i] - m)); }
                            o = MFMA32(v0, BF(p0), o); o = MFMA32(v1, BF(p1), o);
                        }
                        SBAR();
                    }
                }
            }
            if (pass == 0) {
                ma = max32(ma); mb = max32(mb);
                float* mx = (float*)(lds + A_MX);
                if (hi == 0) { mx[(wid * 2) * 32 + r32] = ma; mx[(wid * 2 + 1) * 32 + r32] = mb; }
                LBAR();
                ma = fmaxf(ma, mx[((wid ^ 4) * 2) * 32 + r32]); mb = fmaxf(mb, mx[((wid ^ 4) * 2 + 1) * 32 + r32]);
            }
        }
    }
    STAMP(1, 2);
    __syncthreads();
    int tz = threadIdx.x; asm volatile("" : "+v"(tz));
    const int lz = tz & 63;
    float* red = (float*)lds;
    { float* dst = red + (size_t)(wid ^ 4) * 9 * 64 + lz;
#pragma unroll
      for (int r = 0; r < 9; ++r) dst[r * 64] = kh ? oa[r] : ob[r]; }
    __syncthreads();
    float o[9];
    { const float* src = red + (size_t)wid * 9 * 64 + lz;
#pragma unroll
      for (int r = 0; r < 9; ++r) o[r] = (kh ? ob[r] : oa[r]) + src[r * 64]; }
    const float rl = __builtin_amdgcn_rcpf(swap32(o[8], false));
    u16* op = Ot + ((size_t)(((q0 >> 5) + kh) * H + head) * 512) + (lz & 31) * 8 + (lz >> 5) * 4;
    st8_wt(op, (u32x2){cvtpk(o[0] * rl, o[1] * rl), cvtpk(o[2] * rl, o[3] * rl)});
    st8_wt(op + 256, (u32x2){cvtpk(o[4] * rl, o[5] * rl), cvtpk(o[6] * rl, o[7] * rl)});
    asm volatile("s_waitcnt vmcnt(0)\n\ts_barrier" ::: "memory");
    if (tz == 0) __hip_atomic_fetch_add((gu32*)(ws + WS_CNT) + (32 + qt) * 64, 1u, RLX_AGENT);
    STAMP(1, 3);
#undef DMA_CHUNK
}

DI void wait_cnt(gu32* cnt, unsigned want) {
    for (unsigned spins = 0; spins < 400000u; ++spins) {
        if ((unsigned)__builtin_amdgcn_readfirstlane(__hip_atomic_load(cnt, RLX_AGENT)) >= want) break;
        __builtin_amdgcn_s_sleep(1);
    }
}

#define O_AUX 16
#define H_AUX 16
constexpr int G_STAGE = 24576;
constexpr int W_STAGE = 16384;
constexpr int G_RED = 4 * W_STAGE;
DI void gate_role(int b, const u16* __restrict__ x, const u16* __restrict__ wo, const u16* __restrict__ wsp, const u16* __restrict__ bsp, const u16* __restrict__ wout, char* __restrict__ ws, u16* __restrict__ out, char* lds) {
    const int xcd = b & 7, idx = b >> 3;
    const int tt = xcd * 4 + (idx & 3), ct = idx >> 2;
    const int tid = threadIdx.x, lane = tid & 63, r32 = lane & 31, hi = lane >> 5;
    const int wid = __builtin_amdgcn_readfirstlane(tid >> 6);
    const unsigned lds0 = (unsigned)(uintptr_t)lds;
    gu32* hcnt = (gu32*)(ws + WS_CNT) + tt * 64; gu32* ocnt = (gu32*)(ws + WS_CNT) + (32 + (tt >> 2)) * 64;
    STAMP(1, 0);
    if (wid >= 4) {
        const int l = wid - 4;
        if (ct < 7) {
            const char* ag = ws + WS_Z2N + (size_t)ct * 32 * 16384 + l * 4096;
            const char* bg = (const char*)wsp + (size_t)(tt * 64 + l * 16) * 4096;
            const unsigned avoff = lane * 16;
            const unsigned bv0 = (unsigned)((lane >> 3) * 4096 + (((lane & 7) ^ (lane >> 4)) * 16)), bv1 = (unsigned)((8 + (lane >> 3)) * 4096 + (((lane & 7) ^ (4 + (lane >> 4))) * 16));
            auto dma = [&](int kt, int slot) __attribute__((always_inline)) { const unsigned d = lds0 + slot * G_STAGE; const char* a_ = ag + (size_t)kt * 16384;
#pragma unroll
                for (int e = 0; e < 4; ++e) glds16s(avoff, a_ + e * 1024, d + l * 4096 + e * 1024);
                glds16s(bv0, bg + kt * 128, d + 16384 + l * 2048); glds16s(bv1, bg + kt * 128, d + 16384 + l * 2048 + 1024); };
            dma(0, 0);
            WAIT_BAR(0);
            dma(1, 1); dma(2, 2);
            int slot = 0;
            for (int kt = 1; kt < 32; ++kt) {
                if (kt < 31) WAIT_BAR(6); else WAIT_BAR(0);
                if (kt + 2 < 32) dma(kt + 2, slot);
                slot = slot == 2 ? 0 : slot + 1;
            }
            LBAR();
        }
        unsigned wv[4];
#pragma unroll
        for (int e = 0; e < 4; ++e) { const int row = l * 32 + e * 8 + (lane >> 3); wv[e] = (unsigned)(row * 1024 + ct * 128 + (((lane & 7) ^ (((row >> 1) & 1) << 2)) * 16)); }
        auto dmw = [&](const u16* w, int st) __attribute__((always_inline)) { const char* wg = (const char*)w + (size_t)st * 128 * 1024; const unsigned d = lds0 + (st & 3) * W_STAGE + l * 4096;
#pragma unroll
            for (int e = 0; e < 4; ++e) glds16s(wv[e], wg, d + e * 1024); };
        dmw(wout, 0); dmw(wout, 1); dmw(wout, 2); dmw(wout, 3);
        if (l == 0) { wait_cnt(hcnt, 7);
        }
        WAIT_BAR(12);
#pragma unroll
        for (int st = 1; st < 7; ++st) {
            if (st < 4) WAIT_BAR(8); else if (st == 4) WAIT_BAR(8); else if (st == 5) WAIT_BAR(4); else WAIT_BAR(0);
            if (st + 3 < 7) dmw(wout, st + 3);
        }
        LBAR();
        dmw(wo, 0); dmw(wo, 1); dmw(wo, 2); dmw(wo, 3);
        if (l == 0) { wait_cnt(ocnt, 32);
        }
        WAIT_BAR(0);
        return;
    }
#define GPRIO 2
    __builtin_amdgcn_s_setprio(GPRIO);
    const int g = lane >> 4, qq = (lane & 15) >> 2, pp = lane & 3;
    const int th = wid & 1, fh = wid >> 1;
    const int trow = 32 * th + r32, t = tt * 64 + trow;
    if (ct < 7) {
        f32x16 acc0 = {}, acc1 = {};
        const unsigned aoff = (unsigned)(fh * 8192 + lane * 16);
        unsigned boff[4];
#pragma unroll
        for (int ks = 0; ks < 4; ++ks) boff[ks] = (unsigned)(16384 + trow * 128 + (((2 * ks + hi) ^ ((trow >> 1) & 7)) * 16));
        bf16x8 fa0[2], fa1[2], fb[2];
        auto rd = [&](int set, lds_cptr sb, int ks) __attribute__((always_inline)) {
            fb[set] = ldsr128(sb + boff[ks]);
            fa0[set] = ldsr128(sb + aoff + ks * 1024);
            fa1[set] = ldsr128(sb + aoff + 4096 + ks * 1024); };
        auto mm = [&](int set) __attribute__((always_inline)) { };
#define GSTEP(cur, nxt, sbn, ksn) do { SBAR(); acc0 = MFMA32(fa0[cur], fb[cur], acc0); SBAR(); rd(nxt, sbn, ksn); SBAR(); acc1 = MFMA32(fa1[cur], fb[cur], acc1); SBAR(); } while (0)
        const float bias = bflo(bsp[t]);
        u32x2 z[2][4];
#pragma unroll
        for (int i = 0; i < 2; ++i)
#pragma unroll
            for (int q = 0; q < 4; ++q) z[i][q] = *(const u32x2*)((const u16*)(ws + WS_Z1) + ((size_t)((t >> 5) * 28 + ct * 4 + 2 * fh + i) * 1024) + (hi * 32 + r32) * 4 + q * 256);
        LBAR();
        rd(0, (lds_cptr)lds, 0);
        int slot = 0;
        for (int kt = 0; kt < 32; ++kt) {
            const lds_cptr sb = (lds_cptr)lds + slot * G_STAGE;
            GSTEP(0, 1, sb, 1);
            GSTEP(1, 0, sb, 2);
            GSTEP(0, 1, sb, 3);
            slot = slot == 2 ? 0 : slot + 1;
            if (kt < 31) { LBAR(); GSTEP(1, 0, (lds_cptr)lds + slot * G_STAGE, 0); }
            else { acc0 = MFMA32(fa0[1], fb[1], acc0); acc1 = MFMA32(fa1[1], fb[1], acc1); }
        }
        STAMP(1, 2);
#pragma unroll
        for (int i = 0; i < 2; ++i) {
            u16* hp = (u16*)(ws + WS_H) + ((size_t)((t >> 5) * 56 + (ct * 4 + 2 * fh + i) * 2) * 512) + r32 * 8 + hi * 4;
#pragma unroll
            for (int q = 0; q < 4; ++q) {
                float v[4];
#pragma unroll
                for (int r = 0; r < 4; ++r) v[r] = (i ? acc1[4 * q + r] : acc0[4 * q + r]) + bias;
                st8_wt(hp + (q >> 1) * 512 + (q & 1) * 256, (u32x2){cvtpk(v[0] * bflo(z[i][q][0]), v[1] * bfhi(z[i][q][0])), cvtpk(v[2] * bflo(z[i][q][1]), v[3] * bfhi(z[i][q][1]))});
            }
        }
        asm volatile("s_waitcnt vmcnt(0) lgkmcnt(0)\n\ts_barrier" ::: "memory");
        if (tid == 0) __hip_atomic_fetch_add(hcnt, 1u, RLX_AGENT);
    }
    STAMP(1, 3);
    const int kh = fh;
    const int kr = 8 * (g >> 1) + qq;
    unsigned wof[2][2];
#pragma unroll
    for (int n = 0; n < 2; ++n) { wof[n][0] = (unsigned)((64 * kh + kr) * 128 + ((n ^ ((kr >> 1) & 1)) * 64) + (g & 1) * 32 + pp * 8); wof[n][1] = wof[n][0] + 512; }
    auto gemm = [&](const __amdgpu_buffer_rsrc_t tr, auto NST, bool bars, f32x16& c0, f32x16& c1) __attribute__((always_inline)) {
        constexpr int N = decltype(NST)::value;
        u32x4 tf[4][4];
#pragma unroll
        for (int st = 0; st < 4; ++st)
#pragma unroll
            for (int i = 0; i < 4; ++i) tf[st][i] = __builtin_amdgcn_raw_buffer_load_b128(tr, lane * 16 + (st * 8 + 4 * kh + i) * 1024, 0, H_AUX);
        SBAR();
#pragma unroll
        for (int st = 0; st < N; ++st) {
            if (bars && st > 0) LBAR();
            const lds_cptr sb = (lds_cptr)lds + (st & 3) * W_STAGE;
#pragma unroll
            for (int i = 0; i < 4; ++i) {
                const bf16x8 w0 = cat(ldstr(sb + wof[0][0] + i * 2048), ldstr(sb + wof[0][1] + i * 2048)), w1 = cat(ldstr(sb + wof[1][0] + i * 2048), ldstr(sb + wof[1][1] + i * 2048));
                c0 = MFMA32(w0, BF(tf[st & 3][i]), c0); c1 = MFMA32(w1, BF(tf[st & 3][i]), c1);
            }
            SBAR();
            if (st + 4 < N) {
#pragma unroll
                for (int i = 0; i < 4; ++i) tf[st & 3][i] = __builtin_amdgcn_raw_buffer_load_b128(tr, lane * 16 + ((st + 4) * 8 + 4 * kh + i) * 1024, 0, H_AUX);
                SBAR(); }
        }
    };
    auto merge = [&](f32x16& c0, f32x16& c1) __attribute__((always_inline)) {
        f32x4* dst = (f32x4*)(lds + G_RED + (wid ^ 2) * 4096) + lane;
#pragma unroll
        for (int q = 0; q < 4; ++q) dst[q * 64] = kh ? (f32x4){c0[4 * q], c0[4 * q + 1], c0[4 * q + 2], c0[4 * q + 3]} : (f32x4){c1[4 * q], c1[4 * q + 1], c1[4 * q + 2], c1[4 * q + 3]};
    };
    f32x16 gb0 = {}, gb1 = {};
    LBAR();
    gemm(__builtin_amdgcn_make_buffer_rsrc((void*)(ws + WS_H + (size_t)(tt * 2 + th) * 57344), 0, 57344, 0x00020000), std::integral_constant<int, 7>{}, true, gb0, gb1);
    merge(gb0, gb1);
    LBAR();
    { char* sc = lds + G_RED + wid * 4096;
      float gv[16];
#pragma unroll
      for (int q = 0; q < 4; ++q) { const f32x4 o4 = ((const f32x4*)sc)[q * 64 + lane];
#pragma unroll
          for (int r = 0; r < 4; ++r) { const float bb = (kh ? gb1[4 * q + r] : gb0[4 * q + r]) + o4[r]; gv[4 * q + r] = bb * __builtin_amdgcn_rcpf(1.0f + EX(-LOG2E * bb)); } }
      asm volatile("s_waitcnt lgkmcnt(0)" ::: "memory");
#pragma unroll
      for (int u = 0; u < 2; ++u) ((u32x4*)sc)[u * 64 + lane] = (u32x4){cvtpk(gv[8 * u], gv[8 * u + 1]), cvtpk(gv[8 * u + 2], gv[8 * u + 3]), cvtpk(gv[8 * u + 4], gv[8 * u + 5]), cvtpk(gv[8 * u + 6], gv[8 * u + 7])}; }
    const int n0 = ct * 64 + kh * 32 + 4 * hi;
    u32x2 xv[4];
#pragma unroll
    for (int q = 0; q < 4; ++q) xv[q] = *(const u32x2*)(x + (size_t)t * D + n0 + 8 * q);
    PIN(xv[0]); PIN(xv[1]); PIN(xv[2]); PIN(xv[3]);
    STAMP(1, 4);
    LBAR();
    STAMP(1, 5);
    f32x16 s0 = {}, s1 = {};
    {
        const __amdgpu_buffer_rsrc_t tr = __builtin_amdgcn_make_buffer_rsrc((void*)(ws + WS_O + (size_t)(tt * 2 + th) * 32768), 0, 32768, 0x00020000);
        u32x4 tf[16];
#pragma unroll
        for (int i = 0; i < 16; ++i) tf[i] = __builtin_amdgcn_raw_buffer_load_b128(tr, lane * 16 + ((i >> 2) * 8 + 4 * kh + (i & 3)) * 1024, 0, O_AUX);
        SBAR();
        bf16x8 w0[2], w1[2];
        w0[0] = cat(ldstr((lds_cptr)lds + wof[0][0]), ldstr((lds_cptr)lds + wof[0][1])); w1[0] = cat(ldstr((lds_cptr)lds + wof[1][0]), ldstr((lds_cptr)lds + wof[1][1]));
#pragma unroll
        for (int i = 0; i < 16; ++i) {
            if (i + 1 < 16) { const lds_cptr sb = (lds_cptr)lds + ((i + 1) >> 2) * W_STAGE + ((i + 1) & 3) * 2048;
                w0[(i + 1) & 1] = cat(ldstr(sb + wof[0][0]), ldstr(sb + wof[0][1])); w1[(i + 1) & 1] = cat(ldstr(sb + wof[1][0]), ldstr(sb + wof[1][1])); }
            s0 = MFMA32(w0[i & 1], BF(tf[i]), s0); s1 = MFMA32(w1[i & 1], BF(tf[i]), s1);
        }
    }
    STAMP(1, 7);
    u32x4 gq[2];
    { const u32x4* gt = (const u32x4*)(lds + G_RED + wid * 4096) + lane; gq[0] = gt[0]; gq[1] = gt[64]; }
    LBAR();
    { f32x4* dst = (f32x4*)(lds + (wid ^ 2) * 4096) + lane;
#pragma unroll
      for (int q = 0; q < 4; ++q) dst[q * 64] = kh ? (f32x4){s0[4 * q], s0[4 * q + 1], s0[4 * q + 2], s0[4 * q + 3]} : (f32x4){s1[4 * q], s1[4 * q + 1], s1[4 * q + 2], s1[4 * q + 3]}; }
    LBAR();
    { const f32x4* src = (const f32x4*)(lds + wid * 4096) + lane;
#pragma unroll
      for (int q = 0; q < 4; ++q) { const f32x4 o4 = src[q * 64]; float v[4]; const unsigned g0 = gq[q >> 1][2 * (q & 1)], g1 = gq[q >> 1][2 * (q & 1) + 1]; const float gg[4] = {bflo(g0), bfhi(g0), bflo(g1), bfhi(g1)};
#pragma unroll
          for (int r = 0; r < 4; ++r) v[r] = ((kh ? s1[4 * q + r] : s0[4 * q + r]) + o4[r]) * gg[r];
          *(u32x2*)(out + (size_t)t * D + n0 + 8 * q) = (u32x2){cvtpk(bflo(xv[q][0]) + v[0], bfhi(xv[q][0]) + v[1]), cvtpk(bflo(xv[q][1]) + v[2], bfhi(xv[q][1]) + v[3])}; } }
    STAMP(1, 6);
}

__global__ __launch_bounds__(512, 4) void k2_mix(const u16* __restrict__ x, const u16* __restrict__ wo, const u16* __restrict__ wsp, const u16* __restrict__ bsp, const u16* __restrict__ wout, char* __restrict__ ws, u16* __restrict__ out) {
    extern __shared__ __attribute__((aligned(16))) char lds[];
    if (blockIdx.x < 256) attn_role(blockIdx.x, ws, lds);
    if (blockIdx.x >= 256) gate_role(blockIdx.x - 256, x, wo, wsp, bsp, wout, ws, out, lds);
}

extern "C" void kernel_launch(void* const* d_in, const int* in_sizes, int n_in, void* d_out, int out_size, void* d_ws, size_t ws_size, hipStream_t stream) {
    const u16* x = (const u16*)d_in[0]; const u16* wqkv = (const u16*)d_in[1]; const u16* wo = (const u16*)d_in[2];
    const u16* win = (const u16*)d_in[4]; const u16* wsp = (const u16*)d_in[5]; const u16* bsp = (const u16*)d_in[6]; const u16* wout = (const u16*)d_in[7];
    char* ws = (char*)d_ws;
    (void)hipFuncSetAttribute((const void*)k1_proj, hipFuncAttributeMaxDynamicSharedMemorySize, K1_LDS);
    (void)hipFuncSetAttribute((const void*)k2_mix, hipFuncAttributeMaxDynamicSharedMemorySize, K2_LDS);
    k1_proj<<<256, 768, K1_LDS, stream>>>(x, wqkv, win, wsp, wout, wo, ws);
    k2_mix<<<512, 512, K2_LDS, stream>>>(x, wo, wsp, bsp, wout, ws, (u16*)d_out);
}
```

```cpp
#include <hip/hip_runtime.h>
#include <hip/hip_bf16.h>
#include <stdint.h>
#include <type_traits>

typedef unsigned short u16;
using bf16x8 = __attribute__((ext_vector_type(8))) short;
using s16x4 = __attribute__((ext_vector_type(4))) short;
using f32x16 = __attribute__((ext_vector_type(16))) float;
using f32x4 = __attribute__((ext_vector_type(4))) float;
using u32x4 = __attribute__((ext_vector_type(4))) unsigned;
using u32x2 = __attribute__((ext_vector_type(2))) unsigned;
typedef __bf16 bf16x2_t __attribute__((ext_vector_type(2)));
typedef float f32x2_t __attribute__((ext_vector_type(2)));
typedef short v4i16_t __attribute__((ext_vector_type(4)));
#define LDSAS __attribute__((address_space(3)))
typedef LDSAS const char* lds_cptr;
#define MFMA32(a, b, c) __builtin_amdgcn_mfma_f32_32x32x16_bf16(a, b, c, 0, 0, 0)
#define MFMA16(a, b, c) __builtin_amdgcn_mfma_f32_16x16x32_bf16(a, b, c, 0, 0, 0)
#define SBAR() __builtin_amdgcn_sched_barrier(0)
#define PIN(x) asm volatile("" : "+v"(x))
#define WAIT_BAR(N) asm volatile("s_waitcnt vmcnt(" #N ") lgkmcnt(0)\n\ts_barrier" ::: "memory")
#define LBAR() asm volatile("s_waitcnt lgkmcnt(0)\n\ts_barrier" ::: "memory")
#define DI __device__ __forceinline__
#define ST8(p, w) st8_wt((void*)(p), (w))
#define ST16(off, v) __builtin_amdgcn_raw_buffer_store_b128((v), wsr, (off), 0, 16)
#define STAMP(kern, i) do { } while (0)

constexpr int S = 2048, D = 512, H = 32, DH = 16, FH = 896;
constexpr float LOG2E = 1.4426950408889634f;

constexpr size_t WS_Q = 0, WS_K = 2u << 20, WS_V = 4u << 20, WS_Z1 = 6u << 20, WS_Z2R = 10u << 20, WS_Z2N = 14u << 20, WS_H = 18u << 20, WS_O = 22u << 20, WS_KMAX = 24u << 20, WS_EPOCH = 25u << 20, WS_XCH = 26u << 20, WS_XST = 27u << 20, WS_CNT = 28u << 20;

DI unsigned cvtpk(float lo, float hi) { f32x2_t v = {lo, hi}; return __builtin_bit_cast(unsigned, __builtin_convertvector(v, bf16x2_t)); }
DI float bflo(unsigned w) { return __uint_as_float(w << 16); }
DI float bfhi(unsigned w) { return __uint_as_float(w & 0xFFFF0000u); }
DI float dot2(unsigned a, unsigned b, float c) { return __builtin_amdgcn_fdot2_f32_bf16(__builtin_bit_cast(bf16x2_t, a), __builtin_bit_cast(bf16x2_t, b), c, false); }
DI void glds16s(unsigned voff, const void* sbase, unsigned lds_base) {
    lds_base = __builtin_amdgcn_readfirstlane(lds_base);
    unsigned sv; asm volatile("s_mov_b32 %0, m0\n\ts_mov_b32 m0, %3\n\ts_nop 0\n\tglobal_load_lds_dwordx4 %1, %2\n\ts_mov_b32 m0, %0" : "=&s"(sv) : "v"(voff), "s"(sbase), "s"(lds_base) : "memory"); }
DI bf16x8 ldsr128(lds_cptr p) { return *(const LDSAS bf16x8*)p; }
DI s16x4 ldstr(lds_cptr p) { return __builtin_bit_cast(s16x4, __builtin_amdgcn_ds_read_tr16_b64_v4i16((LDSAS v4i16_t*)p)); }
DI bf16x8 cat(s16x4 a, s16x4 b) { return __builtin_shufflevector(a, b, 0, 1, 2, 3, 4, 5, 6, 7); }
DI float swap32(float x, bool sum) { auto rr = __builtin_amdgcn_permlane32_swap(__float_as_uint(x), __float_as_uint(x), false, false); return sum ? __uint_as_float(rr[0]) + __uint_as_float(rr[1]) : __uint_as_float(rr[0]); }
DI float gelu_t(float x) {
    const float c1 = -2.0f * 0.7978845608028654f * LOG2E, c3 = c1 * 0.044715f;
    const float u = x * fmaf(x * x, c3, c1);
    return x * __builtin_amdgcn_rcpf(1.0f + __builtin_amdgcn_exp2f(u));
}

constexpr int K1_XB = 16384;
constexpr int K1_STAGE = K1_XB + 7 * 4096;
constexpr int K1_CS = 3 * K1_STAGE;
constexpr int K1_LDS = K1_CS + 1024;
typedef __attribute__((address_space(1))) unsigned gu32; typedef __attribute__((address_space(1))) unsigned long long gu64;
#define RLX_AGENT __ATOMIC_RELAXED, __HIP_MEMORY_SCOPE_AGENT

DI float xor16(float x) { auto r = __builtin_amdgcn_permlane16_swap(__float_as_uint(x), __float_as_uint(x), false, false); return __uint_as_float(r[0]) + __uint_as_float(r[1]); }
DI float xor32(float x) { auto r = __builtin_amdgcn_permlane32_swap(__float_as_uint(x), __float_as_uint(x), false, false); return __uint_as_float(r[0]) + __uint_as_float(r[1]); }
DI float max16(float x) { auto r = __builtin_amdgcn_permlane16_swap(__float_as_uint(x), __float_as_uint(x), false, false); return fmaxf(__uint_as_float(r[0]), __uint_as_float(r[1])); }
DI float max32(float x) { auto r = __builtin_amdgcn_permlane32_swap(__float_as_uint(x), __float_as_uint(x), false, false); return fmaxf(__uint_as_float(r[0]), __uint_as_float(r[1])); }
template <int CTRL> DI float dppf(float x) { return __uint_as_float(__builtin_amdgcn_update_dpp(0, __float_as_uint(x), CTRL, 0xF, 0xF, false)); }
DI float rowmax16(float x) { x = fmaxf(x, dppf<0x128>(x)); x = fmaxf(x, dppf<0x124>(x)); x = fmaxf(x, dppf<0x122>(x)); x = fmaxf(x, dppf<0x121>(x)); return x; }
DI float wavesum(float x) { x += dppf<0x128>(x); x += dppf<0x124>(x); x += dppf<0x122>(x); x += dppf<0x121>(x); return xor32(xor16(x)); }
DI u32x4 widen(u32x2 a, u32x2 b) { auto r0 = __builtin_amdgcn_permlane32_swap(a[0], b[0], false, false); auto r1 = __builtin_amdgcn_permlane32_swap(a[1], b[1], false, false); return (u32x4){r0[0], r1[0], r0[1], r1[1]}; }

DI void st8_wt(void* p, u32x2 w) { __hip_atomic_store((gu64*)p, ((unsigned long long)w[1] << 32) | w[0], RLX_AGENT); }
__global__ __launch_bounds__(768, 3) void k1_proj(const u16* __restrict__ x, const u16* __restrict__ wqkv, const u16* __restrict__ win, char* __restrict__ ws) {
    extern __shared__ __attribute__((aligned(16))) char lds[];
    const int tid = threadIdx.x, lane = tid & 63, r32 = lane & 31, hi = lane >> 5;
    const int wid = __builtin_amdgcn_readfirstlane(tid >> 6);
    const int b = blockIdx.x, xcd = b & 7, idx = b >> 3;
    const int panel = 8 * (xcd >> 2) + (idx & 7), j = 4 * (xcd & 3) + (idx >> 3);
    const unsigned lds0 = (unsigned)(uintptr_t)lds;
    const int jq = j & 3, u7 = 7 * (j >> 2);
    const int n1 = jq == 3 ? 1 : 2, n2 = jq == 1 ? 1 : 2;
    const int z1t0 = u7 + 2 * jq, z2t0 = u7 + (jq == 0 ? 0 : (jq == 1 ? 2 : (jq == 2 ? 3 : 5)));
    STAMP(0, 0);
    if (b == 0 && tid < 40) ((unsigned*)(ws + WS_CNT))[tid * 64] = 0u;
    if (wid >= 8) {
        const int l = wid - 8;
        const char* xb = (const char*)x + (size_t)(panel * 128 + l * 32) * 1024;
        const unsigned xv0 = (unsigned)((lane >> 3) * 1024 + (((lane & 7) ^ (lane >> 4)) * 16)), xv1 = (unsigned)((lane >> 3) * 1024 + (((lane & 7) ^ (4 + (lane >> 4))) * 16));
        const unsigned vq = (unsigned)((16 * l + (lane >> 2)) * 3072 + (lane & 3) * 16), vz = (unsigned)((16 * l + (lane >> 2)) * 3584 + (lane & 3) * 16);
        const char* wq = (const char*)wqkv + j * 64;
        int zc[4];
#pragma unroll
        for (int i = 0; i < 4; ++i) { const int ii = i < n1 + n2 ? i : n1 + n2 - 1; zc[i] = ii < n1 ? (z1t0 + ii) * 64 : FH * 2 + (z2t0 + ii - n1) * 64; }
        auto dma = [&](int st) __attribute__((always_inline)) {
            const unsigned d = lds0 + (st % 3) * K1_STAGE;
#pragma unroll
            for (int p = 0; p < 4; ++p) glds16s((p & 1) ? xv1 : xv0, xb + st * 128 + p * 8192, d + l * 4096 + p * 1024);
            const char* bq = wq + (size_t)st * 64 * 3072; const char* bz = (const char*)win + (size_t)st * 64 * 3584; const unsigned dw = d + K1_XB + l * 1024;
            glds16s(vq, bq + 2048, dw); glds16s(vq, bq, dw + 4096); glds16s(vq, bq + 1024, dw + 8192);
#pragma unroll
            for (int i = 0; i < 4; ++i) glds16s(vz, bz + zc[i], dw + (3 + i) * 4096);
        };
        dma(0);
        WAIT_BAR(0);
        dma(1); dma(2);
#pragma unroll
        for (int st = 1; st < 8; ++st) {
            if (st < 7) WAIT_BAR(11); else WAIT_BAR(0);
            if (st + 2 < 8) dma(st + 2);
        }
        {
            if ((idx & 3) == 0) {
                unsigned long long pc;
                asm volatile("s_getpc_b64 s[98:99]\n\ts_add_u32 s98, s98, _Z6k2_mixPKtS0_S0_S0_S0_PcPt@rel32@lo+4\n\ts_addc_u32 s99, s99, _Z6k2_mixPKtS0_S0_S0_S0_PcPt@rel32@hi+12\n\ts_mov_b64 %0, s[98:99]" : "=s"(pc) :: "scc", "s98", "s99");
                const u32x4* cp = (const u32x4*)(pc + (size_t)l * 4096) + lane;
                u32x4 a = {0u, 0u, 0u, 0u};
#pragma unroll
                for (int i = 0; i < 4; ++i) a ^= cp[i * 64];
                asm volatile("" :: "v"(a));
            }
        }
        return;
    }
    const int mw = wid & 3, nh = wid >> 2;
    gu32* epw = (gu32*)(ws + WS_EPOCH) + panel * 64;
    unsigned tag = __hip_atomic_load(epw, RLX_AGENT);
    u32x4 xr = *(const u32x4*)(x + (size_t)(panel * 128 + 8 * j + wid) * D + lane * 8);
    auto rowstats = [&]() __attribute__((always_inline)) {
        PIN(xr); PIN(tag); tag += 1u;
        float v[8];
#pragma unroll
        for (int i = 0; i < 4; ++i) { v[2 * i] = bflo(xr[i]); v[2 * i + 1] = bfhi(xr[i]); }
        const float m = wavesum(((v[0] + v[1]) + (v[2] + v[3])) + ((v[4] + v[5]) + (v[6] + v[7]))) * (1.0f / 512.0f);
        float q = 0.f;
#pragma unroll
        for (int i = 0; i < 8; ++i) q = fmaf(v[i] - m, v[i] - m, q);
        const float r = __builtin_amdgcn_rsqf(wavesum(q) * (1.0f / 512.0f) + 1e-5f);
        if (lane < 2) __hip_atomic_store((gu64*)(ws + WS_XST) + (size_t)(panel * 128 + 8 * j + wid) * 2 + lane, ((unsigned long long)tag << 32) | __float_as_uint(lane ? r : m), RLX_AGENT);
    };
    f32x16 acc[4];
#pragma unroll
    for (int s = 0; s < 4; ++s) acc[s] = (f32x16){};
    float cs0 = 0.f, cs1 = 0.f;
    const int g = lane >> 4, qq = (lane & 15) >> 2, pp = lane & 3;
    const int trow = 32 * mw + r32;
    unsigned xo[4];
#pragma unroll
    for (int ks = 0; ks < 4; ++ks) xo[ks] = (unsigned)(trow * 128 + (((2 * ks + hi) ^ ((trow >> 1) & 7)) * 16));
    const unsigned wlane = (unsigned)(K1_XB + (8 * (g >> 1) + qq) * 64 + (g & 1) * 32 + pp * 8);
    const __amdgpu_buffer_rsrc_t sr = __builtin_amdgcn_make_buffer_rsrc((void*)(ws + WS_XST + (size_t)panel * 128 * 16), 0, 128 * 16, 0x00020000);
    u32x4 pst;
    int tile[4];
    auto mainloop = [&](auto NHC) __attribute__((always_inline)) {
        constexpr int NHV = decltype(NHC)::value; constexpr int NS = NHV == 0 ? 4 : 3;
        unsigned wo[NS];
#pragma unroll
        for (int s = 0; s < NS; ++s) { int t = s + mw; t = t >= NS ? t - NS : t; t = t >= NS ? t - NS : t; tile[s] = 4 * NHV + t; wo[s] = wlane + tile[s] * 4096; }
        bf16x8 xa[2], wf[2][NS];
        auto rd = [&](const int set, const lds_cptr sb, const int ks) __attribute__((always_inline)) {
            xa[set] = ldsr128(sb + xo[ks]);
#pragma unroll
            for (int s = 0; s < NS; ++s) wf[set][s] = cat(ldstr(sb + wo[s] + ks * 1024), ldstr(sb + wo[s] + ks * 1024 + 256));
        };
        auto mm = [&](const int set) __attribute__((always_inline)) {
            SBAR();
#pragma unroll
            for (int s = 0; s < NS; ++s) acc[s] = MFMA32(wf[set][s], xa[set], acc[s]);
            const u32x4 w = __builtin_bit_cast(u32x4, wf[set][0]);
#pragma unroll
            for (int i = 0; i < 4; ++i) { cs0 += bflo(w[i]); cs1 += bfhi(w[i]); }
            PIN(cs0); PIN(cs1);
#pragma unroll
            for (int i = 0; i < NS; ++i) { __builtin_amdgcn_sched_group_barrier(0x008, 1, 0); __builtin_amdgcn_sched_group_barrier(0x002, NS == 4 ? 4 : 6, 0); }
            SBAR();
        };
        LBAR();
        STAMP(0, 1);
        rd(0, (lds_cptr)lds, 0);
#pragma unroll
        for (int st = 0; st < 8; ++st) {
            const lds_cptr sb = (lds_cptr)lds + (st % 3) * K1_STAGE;
            rd(1, sb, 1); mm(0);
            rd(0, sb, 2); mm(1);
            rd(1, sb, 3); mm(0);
            if (st < 7) { LBAR(); rd(0, (lds_cptr)lds + ((st + 1) % 3) * K1_STAGE, 0); }
            if (st == 1) rowstats();
            if (st == 5) pst = __builtin_amdgcn_raw_buffer_load_b128(sr, trow * 16, 0, 16);
            mm(1);
        }
    };
    if (nh == 0) mainloop(std::integral_constant<int, 0>{}); else mainloop(std::integral_constant<int, 1>{});
    STAMP(0, 2);
    float* csl = (float*)(lds + K1_CS);
    { const float c = xor32(cs0 + cs1); if (hi == 0) csl[tile[0] * 32 + r32] = c; }
    const int tok = panel * 128 + trow;
    float mu, rs;
    {   u32x4 p = pst;
        for (unsigned spins = 0; !__all((p[1] == tag) & (p[3] == tag)) && spins < 200000u; ++spins) {
            __builtin_amdgcn_s_sleep(1);
            p = __builtin_amdgcn_raw_buffer_load_b128(sr, trow * 16, 0, 16);
        }
        mu = __uint_as_float(p[0]); rs = __uint_as_float(p[2]); }
    LBAR();
    STAMP(0, 3);
    auto lnv = [&](int s, float (&v)[16], float sc) __attribute__((always_inline)) {
#pragma unroll
        for (int q = 0; q < 4; ++q) { const f32x4 c = *(const f32x4*)(csl + tile[s] * 32 + 8 * q + 4 * hi);
#pragma unroll
            for (int r = 0; r < 4; ++r) v[4 * q + r] = sc * (acc[s][4 * q + r] - mu * c[r]); } };
    const __amdgpu_buffer_rsrc_t wsr = __builtin_amdgcn_make_buffer_rsrc((void*)ws, 0, 32u << 20, 0x00020000);
    u16* Z1 = (u16*)(ws + WS_Z1);
    auto z1store = [&](int zt, const u32x2 (&w)[4]) __attribute__((always_inline)) {
        u16* p = Z1 + ((size_t)((tok >> 5) * 28 + zt) * 1024) + (hi * 32 + r32) * 4;
#pragma unroll
        for (int q = 0; q < 4; ++q) ST8(p + q * 256, w[q]); };
    auto gelu16 = [&](int s, u32x2 (&w)[4]) __attribute__((always_inline)) { float v[16]; lnv(s, v, rs);
#pragma unroll
        for (int q = 0; q < 4; ++q) w[q] = (u32x2){cvtpk(gelu_t(v[4 * q]), gelu_t(v[4 * q + 1])), cvtpk(gelu_t(v[4 * q + 2]), gelu_t(v[4 * q + 3]))}; };
    if (nh == 0) {
#pragma unroll
        for (int s = 0; s < 4; ++s) {
            const int T = tile[s];
            if (T == 3) { u32x2 w[4]; gelu16(s, w); z1store(z1t0, w); }
            else {
                float v[16]; lnv(s, v, T == 1 ? rs * (0.25f * LOG2E) : rs);
                u32x2 w[4];
#pragma unroll
                for (int q = 0; q < 4; ++q) w[q] = (u32x2){cvtpk(v[4 * q], v[4 * q + 1]), cvtpk(v[4 * q + 2], v[4 * q + 3])};
                if (T == 0) {
                    char* sc = lds + mw * 2048;
#pragma unroll
                    for (int q = 0; q < 4; ++q) *(u32x2*)(sc + r32 * 64 + (8 * q + 4 * hi) * 2) = w[q];
                    asm volatile("s_waitcnt lgkmcnt(0)" ::: "memory");
                    u16* Vt = (u16*)(ws + WS_V);
#pragma unroll
                    for (int hh = 0; hh < 2; ++hh)
#pragma unroll
                        for (int ks = 0; ks < 2; ++ks) {
                            const s16x4 t4 = ldstr((lds_cptr)lds + mw * 2048 + (16 * ks + 8 * (g >> 1) + 4 * (g & 1) + qq) * 64 + hh * 32 + pp * 8);
                            ST8(Vt + ((size_t)(2 * j + hh) * 128 + ((panel * 128 + 32 * mw) >> 4) + ks) * 256 + (g & 1) * 128 + (lane & 15) * 8 + (g >> 1) * 4, __builtin_bit_cast(u32x2, t4)); }
                } else {
                    const unsigned dsto = (unsigned)(T == 1 ? WS_Q : WS_K);
#pragma unroll
                    for (int hh = 0; hh < 2; ++hh) ST16(dsto + (((2 * j + hh) * S + tok) * 16 + 8 * hi) * 2, widen(w[2 * hh], w[2 * hh + 1]));
                    if (T == 2) {
                        float* kmax = (float*)(ws + WS_KMAX);
#pragma unroll
                        for (int hh = 0; hh < 2; ++hh) { float n = 0.f;
#pragma unroll
                            for (int q = 0; q < 2; ++q) { const u32x2 ww = w[2 * hh + q]; n = fmaf(bflo(ww[0]), bflo(ww[0]), n); n = fmaf(bfhi(ww[0]), bfhi(ww[0]), n); n = fmaf(bflo(ww[1]), bflo(ww[1]), n); n = fmaf(bfhi(ww[1]), bfhi(ww[1]), n); }
                            n = max16(rowmax16(xor32(n)));
                            if (lane == 0) kmax[(panel * 4 + mw) * H + 2 * j + hh] = n; }
                    }
                }
            }
        }
    } else {
        __builtin_amdgcn_s_setprio(2);
        float sm = 0.f;
#pragma unroll
        for (int s = 0; s < 3; ++s) { const int zi = tile[s] - 3;
            if (zi >= n1 && zi < n1 + n2) { float v[16]; lnv(s, v, rs);
#pragma unroll
                for (int r = 0; r < 16; ++r) { acc[s][r] = gelu_t(v[r]); sm += acc[s][r]; } } }
        sm = xor32(sm);
        const float ml = sm * (n2 == 2 ? (1.0f / 64.f) : (1.0f / 32.f)); float m2 = 0.f;
#pragma unroll
        for (int s = 0; s < 3; ++s) { const int zi = tile[s] - 3;
            if (zi >= n1 && zi < n1 + n2) {
#pragma unroll
                for (int r = 0; r < 16; ++r) { const float d = acc[s][r] - ml; m2 = fmaf(d, d, m2); } } }
        m2 = xor32(m2);
        __hip_atomic_store((gu64*)(ws + WS_XCH) + ((size_t)(panel * 16 + j) * 128 + trow) * 2 + hi, ((unsigned long long)tag << 32) | __float_as_uint(hi ? m2 : sm), RLX_AGENT);
        __builtin_amdgcn_s_setprio(0);
        STAMP(0, 4);
#pragma unroll
        for (int s = 0; s < 3; ++s) { const int zi = tile[s] - 3; if (zi < n1) { u32x2 w[4]; gelu16(s, w); z1store(z1t0 + zi, w); } }
        float mean, rstd;
        {
            const __amdgpu_buffer_rsrc_t xr = __builtin_amdgcn_make_buffer_rsrc((void*)(ws + WS_XCH + (size_t)panel * 16 * 128 * 16), 0, 16 * 128 * 16, 0x00020000);
            u32x4 p[8];
            for (unsigned spins = 0;; ++spins) {
                bool ok = true;
#pragma unroll
                for (int u = 0; u < 8; ++u) p[u] = __builtin_amdgcn_raw_buffer_load_b128(xr, ((8 * hi + u) * 128 + trow) * 16, 0, 16);
#pragma unroll
                for (int u = 0; u < 8; ++u) ok &= (p[u][1] == tag) & (p[u][3] == tag);
                if (__all(ok) || spins > 200000u) break;
                __builtin_amdgcn_s_sleep(6);
            }
            float tot = 0.f;
#pragma unroll
            for (int u = 0; u < 8; ++u) tot += __uint_as_float(p[u][0]);
            mean = xor32(tot) * (1.0f / FH);
            float mm2 = 0.f;
#pragma unroll
            for (int u = 0; u < 8; ++u) { const float nl = (u & 3) == 1 ? 32.f : 64.f; const float d = __uint_as_float(p[u][0]) * ((u & 3) == 1 ? (1.0f / 32.f) : (1.0f / 64.f)) - mean; mm2 += __uint_as_float(p[u][2]) + nl * d * d; }
            rstd = __builtin_amdgcn_rsqf(xor32(mm2) * (1.0f / FH) + 1e-5f);
        }
        STAMP(0, 5);
        if (wid == 4 && lane == 0) __hip_atomic_store(epw, tag, RLX_AGENT);
        const float nmr = -mean * rstd;
#pragma unroll
        for (int s = 0; s < 3; ++s) { const int zi = tile[s] - 3;
            if (zi >= n1 && zi < n1 + n2) {
                const int ft = z2t0 + zi - n1;
                u32x2 w[4];
#pragma unroll
                for (int q = 0; q < 4; ++q) w[q] = (u32x2){cvtpk(fmaf(acc[s][4 * q], rstd, nmr), fmaf(acc[s][4 * q + 1], rstd, nmr)), cvtpk(fmaf(acc[s][4 * q + 2], rstd, nmr), fmaf(acc[s][4 * q + 3], rstd, nmr))};
                char* sc = lds + 8192 + mw * 2048;
#pragma unroll
                for (int q = 0; q < 4; ++q) *(u32x2*)(sc + r32 * 64 + (8 * q + 4 * hi) * 2) = w[q];
                asm volatile("s_waitcnt lgkmcnt(0)" ::: "memory");
                const int tk0 = panel * 128 + 32 * mw;
#pragma unroll
                for (int ks = 0; ks < 2; ++ks) {
                    const lds_cptr rp = (lds_cptr)lds + 8192 + mw * 2048 + (16 * ks + 8 * (g >> 1) + qq) * 64 + (g & 1) * 32 + pp * 8;
                    const bf16x8 fr = cat(ldstr(rp), ldstr(rp + 256));
                    ST16((unsigned)WS_Z2N + (unsigned)(((((ft >> 2) * 32 + (tk0 >> 6)) * 4 + (ft & 3)) * 4 + ((tk0 >> 4) & 3) + ks) * 1024 + lane * 16), __builtin_bit_cast(u32x4, fr));
                }
                asm volatile("s_waitcnt lgkmcnt(0)" ::: "memory");
            } }
    }
    STAMP(0, 6);
}

constexpr int A_KR = 0, A_VR = 32768, A_CONST = 65536, A_FLAG = A_CONST + 4096, A_MX = A_FLAG + 64, K2_LDS = 81920;
constexpr float SAFE_T = 48.f;
#define EX(x) __builtin_amdgcn_exp2f(x)
#define BF(p) __builtin_bit_cast(bf16x8, p)

DI void attn_role(int b, char* __restrict__ ws, char* lds) {
    const u16* Qs = (const u16*)(ws + WS_Q); const u16* Kb = (const u16*)(ws + WS_K); const u16* Vt = (const u16*)(ws + WS_V); const float* kmax = (const float*)(ws + WS_KMAX); u16* Ot = (u16*)(ws + WS_O);
    const int tid = threadIdx.x, lane = tid & 63, r32 = lane & 31, hi = lane >> 5;
    const int wid = __builtin_amdgcn_readfirstlane(tid >> 6);
    const int xcd = b & 7, idx = b >> 3, head = xcd * 4 + (idx & 3), qt = idx >> 2;
    const int qp = wid & 3, kh = wid >> 2;
    const unsigned lds0 = (unsigned)(uintptr_t)lds;
    const char* kg = (const char*)(Kb + (size_t)head * S * DH);
    const char* vg = (const char*)(Vt + (size_t)head * S * DH);
    const unsigned kvoff = wid * 1024 + ((lane ^ ((lane >> 5) & 1)) * 16);
    const unsigned vvoff = wid * 1024 + lane * 16;
#define DMA_CHUNK(c) do { glds16s(kvoff, kg + (c) * 8192, lds0 + A_KR + ((c) & 3) * 8192 + wid * 1024); glds16s(vvoff, vg + (c) * 8192, lds0 + A_VR + ((c) & 3) * 8192 + wid * 1024); } while (0)
    STAMP(1, 0);
    const int q0 = qt * 256 + qp * 64;
    const bf16x8 qa = *(const bf16x8*)(Qs + ((size_t)head * S + q0 + r32) * DH + hi * 8);
    const bf16x8 qb = *(const bf16x8*)(Qs + ((size_t)head * S + q0 + 32 + r32) * DH + hi * 8);
    float km = kmax[lane * H + head];
    DMA_CHUNK(0);
    if (tid < 256) { const unsigned w = (tid & 31) ? 0u : 0x3F803F80u; u32x4 v = {w, w, w, w}; *(u32x4*)(lds + A_CONST + tid * 16) = v; }
    km = max32(max16(rowmax16(km)));
    float qna = 0.f, qnb = 0.f;
#pragma unroll
    for (int j = 0; j < 8; ++j) { const float va = bflo((u16)qa[j]), vb = bflo((u16)qb[j]); qna = fmaf(va, va, qna); qnb = fmaf(vb, vb, qnb); }
    qna = xor32(qna); qnb = xor32(qnb);
    const float mh = __builtin_amdgcn_sqrtf(fmaxf(qna, qnb) * km) * 1.001f;
    { const bool unsafe = __any(!(mh <= SAFE_T)); if (lane == 0) ((unsigned*)(lds + A_FLAG))[wid] = unsafe ? 1u : 0u; }
    const lds_cptr kp0 = (lds_cptr)lds + A_KR + kh * 4096 + r32 * 32 + ((hi ^ (r32 >> 4)) * 16);
    const bool vreal = r32 < 16;
    const unsigned vlane = vreal ? (unsigned)(A_VR + kh * 4096 + hi * 256 + r32 * 16) : (unsigned)(A_CONST + (r32 == 16 ? 0 : ((r32 >= 20 && r32 < 28) ? 64 : 16)));
    const unsigned vmask = vreal ? 0xFFFFFFFFu : 0u;
    f32x16 oa = {}, ob = {};
    WAIT_BAR(0);
    bool slow;
    { const u32x4 f0 = *(const u32x4*)(lds + A_FLAG), f1 = *(const u32x4*)(lds + A_FLAG + 16); slow = __builtin_amdgcn_readfirstlane(f0[0] | f0[1] | f0[2] | f0[3] | f1[0] | f1[1] | f1[2] | f1[3]) != 0; }
    if (!slow) {
        DMA_CHUNK(1); DMA_CHUNK(2); DMA_CHUNK(3);
        f32x16 cinit;
#pragma unroll
        for (int r = 0; r < 16; ++r) cinit[r] = -mh;
        PIN(cinit);
        bf16x8 kf, v0, v1; f32x16 sA, sB; u32x4 p0, p1;
        kf = ldsr128(kp0);
        { lds_cptr vp = (lds_cptr)lds + vlane; v0 = ldsr128(vp); v1 = ldsr128(vp + 512); }
        sA = MFMA32(kf, qa, cinit);
        p1 = (u32x4){0u, 0u, 0u, 0u};
#define HSTEP(SC, SN, QN, OP, OC, PRE1, PRE2, PRE3) do { \
        SN = MFMA32(kf, QN, cinit); PIN(SN); SBAR(); \
        PRE1; \
        SC[0] = EX(SC[0]); SC[1] = EX(SC[1]); SC[2] = EX(SC[2]); SC[3] = EX(SC[3]); SC[4] = EX(SC[4]); SC[5] = EX(SC[5]); PIN(SC); SBAR(); \
        OP = MFMA32(v1, BF(p1), OP); PIN(OP); SBAR(); \
        PRE2; \
        SC[6] = EX(SC[6]); SC[7] = EX(SC[7]); \
        p0[0] = cvtpk(SC[0], SC[1]); p0[1] = cvtpk(SC[2], SC[3]); p0[2] = cvtpk(SC[4], SC[5]); p0[3] = cvtpk(SC[6], SC[7]); PIN(p0); SBAR(); \
        SC[8] = EX(SC[8]); SC[9] = EX(SC[9]); SC[10] = EX(SC[10]); PIN(SC); SBAR(); \
        OC = MFMA32(v0, BF(p0), OC); PIN(OC); SBAR(); \
        PRE3; \
        SC[11] = EX(SC[11]); SC[12] = EX(SC[12]); SC[13] = EX(SC[13]); SC[14] = EX(SC[14]); SC[15] = EX(SC[15]); \
        p1[0] = cvtpk(SC[8], SC[9]); p1[1] = cvtpk(SC[10], SC[11]); p1[2] = cvtpk(SC[12], SC[13]); p1[3] = cvtpk(SC[14], SC[15]); PIN(p1); SBAR(); \
    } while (0)
#define BLOCK(kn, vc, vco, vn, vno) do { \
        HSTEP(sA, sB, qb, ob, oa, kf = ldsr128(kn), v1 = ldsr128((vc) + (vco) + 512), (void)0); \
        HSTEP(sB, sA, qa, oa, ob, (void)0, (void)0, v0 = ldsr128((vn) + (vno))); \
    } while (0)
        STAMP(1, 1);
        for (int c = 0; c < 8; ++c) {
            const unsigned ro = (unsigned)((c & 3) * 8192), rn = (unsigned)(((c + 1) & 3) * 8192);
            const lds_cptr vp = (lds_cptr)lds + (vlane + (ro & vmask)), vpn = (lds_cptr)lds + (vlane + (rn & vmask));
            const lds_cptr kp = kp0 + ro, kpn = kp0 + rn;
            BLOCK(kp + 1024, vp, 0, vp, 1024);
            BLOCK(kp + 2048, vp, 1024, vp, 2048);
            BLOCK(kp + 3072, vp, 2048, vp, 3072);
            if (c == 0) WAIT_BAR(4); else if (c < 6) WAIT_BAR(2); else if (c == 6) WAIT_BAR(0);
            if (c >= 1 && c + 3 < 8) DMA_CHUNK(c + 3);
            BLOCK(kpn, vp, 3072, vpn, 0);
        }
        ob = MFMA32(v1, BF(p1), ob);
#undef HSTEP
#undef BLOCK
    } else {
        float ma = -3.0e38f, mb = -3.0e38f;
        for (int pass = 0; pass < 2; ++pass) {
            for (int c = 0; c < 8; ++c) {
                LBAR();
                DMA_CHUNK(c);
                WAIT_BAR(0);
                const unsigned ro = (unsigned)((c & 3) * 8192);
                const lds_cptr vp = (lds_cptr)lds + (vlane + (ro & vmask)), kp = kp0 + ro;
                for (int j = 0; j < 4; ++j) {
                    const bf16x8 kf = ldsr128(kp + j * 1024);
                    const bf16x8 v0 = ldsr128(vp + j * 1024), v1 = ldsr128(vp + j * 1024 + 512);
#pragma unroll
                    for (int u = 0; u < 2; ++u) {
                        const f32x16 sc = MFMA32(kf, u ? qb : qa, (f32x16){});
                        float& m = u ? mb : ma; f32x16& o = u ? ob : oa;
                        if (pass == 0) {
#pragma unroll
                            for (int r = 0; r < 16; ++r) m = fmaxf(m, sc[r]);
                        } else {
                            u32x4 p0, p1;
#pragma unroll
                            for (int i = 0; i < 4; ++i) { p0[i] = cvtpk(EX(sc[2 * i] - m), EX(sc[2 * i + 1] - m)); p1[i] = cvtpk(EX(sc[8 + 2 * i] - m), EX(sc[9 + 2 * i] - m)); }
                            o = MFMA32(v0, BF(p0), o); o = MFMA32(v1, BF(p1), o);
                        }
                        SBAR();
                    }
                }
            }
            if (pass == 0) {
                ma = max32(ma); mb = max32(mb);
                float* mx = (float*)(lds + A_MX);
                if (hi == 0) { mx[(wid * 2) * 32 + r32] = ma; mx[(wid * 2 + 1) * 32 + r32] = mb; }
                LBAR();
                ma = fmaxf(ma, mx[((wid ^ 4) * 2) * 32 + r32]); mb = fmaxf(mb, mx[((wid ^ 4) * 2 + 1) * 32 + r32]);
            }
        }
    }
    STAMP(1, 2);
    __syncthreads();
    int tz = threadIdx.x; asm volatile("" : "+v"(tz));
    const int lz = tz & 63;
    float* red = (float*)lds;
    { float* dst = red + (size_t)(wid ^ 4) * 9 * 64 + lz;
#pragma unroll
      for (int r = 0; r < 9; ++r) dst[r * 64] = kh ? oa[r] : ob[r]; }
    __syncthreads();
    float o[9];
    { const float* src = red + (size_t)wid * 9 * 64 + lz;
#pragma unroll
      for (int r = 0; r < 9; ++r) o[r] = (kh ? ob[r] : oa[r]) + src[r * 64]; }
    const float rl = __builtin_amdgcn_rcpf(swap32(o[8], false));
    u16* op = Ot + ((size_t)(((q0 >> 5) + kh) * H + head) * 512) + (lz & 31) * 8 + (lz >> 5) * 4;
    st8_wt(op, (u32x2){cvtpk(o[0] * rl, o[1] * rl), cvtpk(o[2] * rl, o[3] * rl)});
    st8_wt(op + 256, (u32x2){cvtpk(o[4] * rl, o[5] * rl), cvtpk(o[6] * rl, o[7] * rl)});
    asm volatile("s_waitcnt vmcnt(0)\n\ts_barrier" ::: "memory");
    if (tz == 0) __hip_atomic_fetch_add((gu32*)(ws + WS_CNT) + (32 + qt) * 64, 1u, RLX_AGENT);
    STAMP(1, 3);
#undef DMA_CHUNK
}

DI void wait_cnt(gu32* cnt, unsigned want) {
    for (unsigned spins = 0; spins < 400000u; ++spins) {
        if ((unsigned)__builtin_amdgcn_readfirstlane(__hip_atomic_load(cnt, RLX_AGENT)) >= want) break;
        __builtin_amdgcn_s_sleep(1);
    }
}

#define O_AUX 16
#define H_AUX 16
constexpr int G_STAGE = 24576;
constexpr int W_STAGE = 16384;
constexpr int G_RED = 4 * W_STAGE;
DI void gate_role(int b, const u16* __restrict__ x, const u16* __restrict__ wo, const u16* __restrict__ wsp, const u16* __restrict__ bsp, const u16* __restrict__ wout, char* __restrict__ ws, u16* __restrict__ out, char* lds) {
    const int xcd = b & 7, idx = b >> 3;
    const int tt = xcd * 4 + (idx & 3), ct = idx >> 2;
    const int tid = threadIdx.x, lane = tid & 63, r32 = lane & 31, hi = lane >> 5;
    const int wid = __builtin_amdgcn_readfirstlane(tid >> 6);
    const unsigned lds0 = (unsigned)(uintptr_t)lds;
    gu32* hcnt = (gu32*)(ws + WS_CNT) + tt * 64; gu32* ocnt = (gu32*)(ws + WS_CNT) + (32 + (tt >> 2)) * 64;
    STAMP(1, 0);
    if (wid >= 4) {
        const int l = wid - 4;
        if (ct < 7) {
            const char* ag = ws + WS_Z2N + (size_t)ct * 32 * 16384 + l * 4096;
            const char* bg = (const char*)wsp + (size_t)(tt * 64 + l * 16) * 4096;
            const unsigned avoff = lane * 16;
            const unsigned bv0 = (unsigned)((lane >> 3) * 4096 + (((lane & 7) ^ (lane >> 4)) * 16)), bv1 = (unsigned)((8 + (lane >> 3)) * 4096 + (((lane & 7) ^ (4 + (lane >> 4))) * 16));
            auto dma = [&](int kt, int slot) __attribute__((always_inline)) { const unsigned d = lds0 + slot * G_STAGE; const char* a_ = ag + (size_t)kt * 16384;
#pragma unroll
                for (int e = 0; e < 4; ++e) glds16s(avoff, a_ + e * 1024, d + l * 4096 + e * 1024);
                glds16s(bv0, bg + kt * 128, d + 16384 + l * 2048); glds16s(bv1, bg + kt * 128, d + 16384 + l * 2048 + 1024); };
            dma(0, 0);
            WAIT_BAR(0);
            dma(1, 1); dma(2, 2);
            int slot = 0;
            for (int kt = 1; kt < 32; ++kt) {
                if (kt < 31) WAIT_BAR(6); else WAIT_BAR(0);
                if (kt + 2 < 32) dma(kt + 2, slot);
                slot = slot == 2 ? 0 : slot + 1;
            }
            LBAR();
        }
        unsigned wv[4];
#pragma unroll
        for (int e = 0; e < 4; ++e) { const int row = l * 32 + e * 8 + (lane >> 3); wv[e] = (unsigned)(row * 1024 + ct * 128 + (((lane & 7) ^ (((row >> 1) & 1) << 2)) * 16)); }
        auto dmw = [&](const u16* w, int st) __attribute__((always_inline)) { const char* wg = (const char*)w + (size_t)st * 128 * 1024; const unsigned d = lds0 + (st & 3) * W_STAGE + l * 4096;
#pragma unroll
            for (int e = 0; e < 4; ++e) glds16s(wv[e], wg, d + e * 1024); };
        dmw(wout, 0); dmw(wout, 1); dmw(wout, 2); dmw(wout, 3);
        if (l == 0) { wait_cnt(hcnt, 7);
        }
        WAIT_BAR(12);
#pragma unroll
        for (int st = 1; st < 7; ++st) {
            if (st < 4) WAIT_BAR(8); else if (st == 4) WAIT_BAR(8); else if (st == 5) WAIT_BAR(4); else WAIT_BAR(0);
            if (st + 3 < 7) dmw(wout, st + 3);
        }
        LBAR();
        dmw(wo, 0); dmw(wo, 1); dmw(wo, 2); dmw(wo, 3);
        if (l == 0) { wait_cnt(ocnt, 32);
        }
        WAIT_BAR(0);
        return;
    }
#define GPRIO 2
    __builtin_amdgcn_s_setprio(GPRIO);
    const int g = lane >> 4, qq = (lane & 15) >> 2, pp = lane & 3;
    const int th = wid & 1, fh = wid >> 1;
    const int trow = 32 * th + r32, t = tt * 64 + trow;
    if (ct < 7) {
        f32x16 acc0 = {}, acc1 = {};
        const unsigned aoff = (unsigned)(fh * 8192 + lane * 16);
        unsigned boff[4];
#pragma unroll
        for (int ks = 0; ks < 4; ++ks) boff[ks] = (unsigned)(16384 + trow * 128 + (((2 * ks + hi) ^ ((trow >> 1) & 7)) * 16));
        bf16x8 fa0[2], fa1[2], fb[2];
        auto rd = [&](int set, lds_cptr sb, int ks) __attribute__((always_inline)) {
            fb[set] = ldsr128(sb + boff[ks]);
            fa0[set] = ldsr128(sb + aoff + ks * 1024);
            fa1[set] = ldsr128(sb + aoff + 4096 + ks * 1024); };
        auto mm = [&](int set) __attribute__((always_inline)) { };
#define GSTEP(cur, nxt, sbn, ksn) do { SBAR(); acc0 = MFMA32(fa0[cur], fb[cur], acc0); SBAR(); rd(nxt, sbn, ksn); SBAR(); acc1 = MFMA32(fa1[cur], fb[cur], acc1); SBAR(); } while (0)
        const float bias = bflo(bsp[t]);
        u32x2 z[2][4];
#pragma unroll
        for (int i = 0; i < 2; ++i)
#pragma unroll
            for (int q = 0; q < 4; ++q) z[i][q] = *(const u32x2*)((const u16*)(ws + WS_Z1) + ((size_t)((t >> 5) * 28 + ct * 4 + 2 * fh + i) * 1024) + (hi * 32 + r32) * 4 + q * 256);
        LBAR();
        rd(0, (lds_cptr)lds, 0);
        int slot = 0;
        for (int kt = 0; kt < 32; ++kt) {
            const lds_cptr sb = (lds_cptr)lds + slot * G_STAGE;
            GSTEP(0, 1, sb, 1);
            GSTEP(1, 0, sb, 2);
            GSTEP(0, 1, sb, 3);
            slot = slot == 2 ? 0 : slot + 1;
            if (kt < 31) { LBAR(); GSTEP(1, 0, (lds_cptr)lds + slot * G_STAGE, 0); }
            else { acc0 = MFMA32(fa0[1], fb[1], acc0); acc1 = MFMA32(fa1[1], fb[1], acc1); }
        }
        STAMP(1, 2);
#pragma unroll
        for (int i = 0; i < 2; ++i) {
            u16* hp = (u16*)(ws + WS_H) + ((size_t)((t >> 5) * 56 + (ct * 4 + 2 * fh + i) * 2) * 512) + r32 * 8 + hi * 4;
#pragma unroll
            for (int q = 0; q < 4; ++q) {
                float v[4];
#pragma unroll
                for (int r = 0; r < 4; ++r) v[r] = (i ? acc1[4 * q + r] : acc0[4 * q + r]) + bias;
                st8_wt(hp + (q >> 1) * 512 + (q & 1) * 256, (u32x2){cvtpk(v[0] * bflo(z[i][q][0]), v[1] * bfhi(z[i][q][0])), cvtpk(v[2] * bflo(z[i][q][1]), v[3] * bfhi(z[i][q][1]))});
            }
        }
        asm volatile("s_waitcnt vmcnt(0) lgkmcnt(0)\n\ts_barrier" ::: "memory");
        if (tid == 0) __hip_atomic_fetch_add(hcnt, 1u, RLX_AGENT);
    }
    STAMP(1, 3);
    const int kh = fh;
    const int kr = 8 * (g >> 1) + qq;
    unsigned wof[2][2];
#pragma unroll
    for (int n = 0; n < 2; ++n) { wof[n][0] = (unsigned)((64 * kh + kr) * 128 + ((n ^ ((kr >> 1) & 1)) * 64) + (g & 1) * 32 + pp * 8); wof[n][1] = wof[n][0] + 512; }
    auto gemm = [&](const __amdgpu_buffer_rsrc_t tr, auto NST, bool bars, f32x16& c0, f32x16& c1) __attribute__((always_inline)) {
        constexpr int N = decltype(NST)::value;
        u32x4 tf[4][4];
#pragma unroll
        for (int st = 0; st < 4; ++st)
#pragma unroll
            for (int i = 0; i < 4; ++i) tf[st][i] = __builtin_amdgcn_raw_buffer_load_b128(tr, lane * 16 + (st * 8 + 4 * kh + i) * 1024, 0, H_AUX);
        SBAR();
#pragma unroll
        for (int st = 0; st < N; ++st) {
            if (bars && st > 0) LBAR();
            const lds_cptr sb = (lds_cptr)lds + (st & 3) * W_STAGE;
#pragma unroll
            for (int i = 0; i < 4; ++i) {
                const bf16x8 w0 = cat(ldstr(sb + wof[0][0] + i * 2048), ldstr(sb + wof[0][1] + i * 2048)), w1 = cat(ldstr(sb + wof[1][0] + i * 2048), ldstr(sb + wof[1][1] + i * 2048));
                c0 = MFMA32(w0, BF(tf[st & 3][i]), c0); c1 = MFMA32(w1, BF(tf[st & 3][i]), c1);
            }
            SBAR();
            if (st + 4 < N) {
#pragma unroll
                for (int i = 0; i < 4; ++i) tf[st & 3][i] = __builtin_amdgcn_raw_buffer_load_b128(tr, lane * 16 + ((st + 4) * 8 + 4 * kh + i) * 1024, 0, H_AUX);
                SBAR(); }
        }
    };
    auto merge = [&](f32x16& c0, f32x16& c1) __attribute__((always_inline)) {
        f32x4* dst = (f32x4*)(lds + G_RED + (wid ^ 2) * 4096) + lane;
#pragma unroll
        for (int q = 0; q < 4; ++q) dst[q * 64] = kh ? (f32x4){c0[4 * q], c0[4 * q + 1], c0[4 * q + 2], c0[4 * q + 3]} : (f32x4){c1[4 * q], c1[4 * q + 1], c1[4 * q + 2], c1[4 * q + 3]};
    };
    f32x16 gb0 = {}, gb1 = {};
    LBAR();
    gemm(__builtin_amdgcn_make_buffer_rsrc((void*)(ws + WS_H + (size_t)(tt * 2 + th) * 57344), 0, 57344, 0x00020000), std::integral_constant<int, 7>{}, true, gb0, gb1);
    merge(gb0, gb1);
    LBAR();
    { char* sc = lds + G_RED + wid * 4096;
      float gv[16];
#pragma unroll
      for (int q = 0; q < 4; ++q) { const f32x4 o4 = ((const f32x4*)sc)[q * 64 + lane];
#pragma unroll
          for (int r = 0; r < 4; ++r) { const float bb = (kh ? gb1[4 * q + r] : gb0[4 * q + r]) + o4[r]; gv[4 * q + r] = bb * __builtin_amdgcn_rcpf(1.0f + EX(-LOG2E * bb)); } }
      asm volatile("s_waitcnt lgkmcnt(0)" ::: "memory");
#pragma unroll
      for (int u = 0; u < 2; ++u) ((u32x4*)sc)[u * 64 + lane] = (u32x4){cvtpk(gv[8 * u], gv[8 * u + 1]), cvtpk(gv[8 * u + 2], gv[8 * u + 3]), cvtpk(gv[8 * u + 4], gv[8 * u + 5]), cvtpk(gv[8 * u + 6], gv[8 * u + 7])}; }
    const int n0 = ct * 64 + kh * 32 + 4 * hi;
    u32x2 xv[4];
#pragma unroll
    for (int q = 0; q < 4; ++q) xv[q] = *(const u32x2*)(x + (size_t)t * D + n0 + 8 * q);
    PIN(xv[0]); PIN(xv[1]); PIN(xv[2]); PIN(xv[3]);
    STAMP(1, 4);
    LBAR();
    STAMP(1, 5);
    f32x16 s0 = {}, s1 = {};
    {
        const __amdgpu_buffer_rsrc_t tr = __builtin_amdgcn_make_buffer_rsrc((void*)(ws + WS_O + (size_t)(tt * 2 + th) * 32768), 0, 32768, 0x00020000);
        u32x4 tf[16];
#pragma unroll
        for (int i = 0; i < 16; ++i) tf[i] = __builtin_amdgcn_raw_buffer_load_b128(tr, lane * 16 + ((i >> 2) * 8 + 4 * kh + (i & 3)) * 1024, 0, O_AUX);
        SBAR();
        bf16x8 w0[2], w1[2];
        w0[0] = cat(ldstr((lds_cptr)lds + wof[0][0]), ldstr((lds_cptr)lds + wof[0][1])); w1[0] = cat(ldstr((lds_cptr)lds + wof[1][0]), ldstr((lds_cptr)lds + wof[1][1]));
#pragma unroll
        for (int i = 0; i < 16; ++i) {
            if (i + 1 < 16) { const lds_cptr sb = (lds_cptr)lds + ((i + 1) >> 2) * W_STAGE + ((i + 1) & 3) * 2048;
                w0[(i + 1) & 1] = cat(ldstr(sb + wof[0][0]), ldstr(sb + wof[0][1])); w1[(i + 1) & 1] = cat(ldstr(sb + wof[1][0]), ldstr(sb + wof[1][1])); }
            s0 = MFMA32(w0[i & 1], BF(tf[i]), s0); s1 = MFMA32(w1[i & 1], BF(tf[i]), s1);
        }
    }
    STAMP(1, 7);
    u32x4 gq[2];
    { const u32x4* gt = (const u32x4*)(lds + G_RED + wid * 4096) + lane; gq[0] = gt[0]; gq[1] = gt[64]; }
    LBAR();
    { f32x4* dst = (f32x4*)(lds + (wid ^ 2) * 4096) + lane;
#pragma unroll
      for (int q = 0; q < 4; ++q) dst[q * 64] = kh ? (f32x4){s0[4 * q], s0[4 * q + 1], s0[4 * q + 2], s0[4 * q + 3]} : (f32x4){s1[4 * q], s1[4 * q + 1], s1[4 * q + 2], s1[4 * q + 3]}; }
    LBAR();
    { const f32x4* src = (const f32x4*)(lds + wid * 4096) + lane;
#pragma unroll
      for (int q = 0; q < 4; ++q) { const f32x4 o4 = src[q * 64]; float v[4]; const unsigned g0 = gq[q >> 1][2 * (q & 1)], g1 = gq[q >> 1][2 * (q & 1) + 1]; const float gg[4] = {bflo(g0), bfhi(g0), bflo(g1), bfhi(g1)};
#pragma unroll
          for (int r = 0; r < 4; ++r) v[r] = ((kh ? s1[4 * q + r] : s0[4 * q + r]) + o4[r]) * gg[r];
          *(u32x2*)(out + (size_t)t * D + n0 + 8 * q) = (u32x2){cvtpk(bflo(xv[q][0]) + v[0], bfhi(xv[q][0]) + v[1]), cvtpk(bflo(xv[q][1]) + v[2], bfhi(xv[q][1]) + v[3])}; } }
    STAMP(1, 6);
}

__global__ __launch_bounds__(512, 4) void k2_mix(const u16* __restrict__ x, const u16* __restrict__ wo, const u16* __restrict__ wsp, const u16* __restrict__ bsp, const u16* __restrict__ wout, char* __restrict__ ws, u16* __restrict__ out) {
    extern __shared__ __attribute__((aligned(16))) char lds[];
    if (blockIdx.x < 256) attn_role(blockIdx.x, ws, lds);
    if (blockIdx.x >= 256) gate_role(blockIdx.x - 256, x, wo, wsp, bsp, wout, ws, out, lds);
}

extern "C" void kernel_launch(void* const* d_in, const int* in_sizes, int n_in, void* d_out, int out_size, void* d_ws, size_t ws_size, hipStream_t stream) {
    const u16* x = (const u16*)d_in[0]; const u16* wqkv = (const u16*)d_in[1]; const u16* wo = (const u16*)d_in[2];
    const u16* win = (const u16*)d_in[4]; const u16* wsp = (const u16*)d_in[5]; const u16* bsp = (const u16*)d_in[6]; const u16* wout = (const u16*)d_in[7];
    char* ws = (char*)d_ws;
    (void)hipFuncSetAttribute((const void*)k1_proj, hipFuncAttributeMaxDynamicSharedMemorySize, K1_LDS);
    (void)hipFuncSetAttribute((const void*)k2_mix, hipFuncAttributeMaxDynamicSharedMemorySize, K2_LDS);
    k1_proj<<<256, 768, K1_LDS, stream>>>(x, wqkv, win, ws);
    k2_mix<<<512, 512, K2_LDS, stream>>>(x, wo, wsp, bsp, wout, ws, (u16*)d_out);
}
```

```cpp
#include <hip/hip_runtime.h>
#include <hip/hip_bf16.h>
#include <stdint.h>
#include <type_traits>

typedef unsigned short u16;
using bf16x8 = __attribute__((ext_vector_type(8))) short;
using s16x4 = __attribute__((ext_vector_type(4))) short;
using f32x16 = __attribute__((ext_vector_type(16))) float;
using f32x4 = __attribute__((ext_vector_type(4))) float;
using u32x4 = __attribute__((ext_vector_type(4))) unsigned;
using u32x2 = __attribute__((ext_vector_type(2))) unsigned;
typedef __bf16 bf16x2_t __attribute__((ext_vector_type(2)));
typedef float f32x2_t __attribute__((ext_vector_type(2)));
typedef short v4i16_t __attribute__((ext_vector_type(4)));
#define LDSAS __attribute__((address_space(3)))
typedef LDSAS const char* lds_cptr;
#define MFMA32(a, b, c) __builtin_amdgcn_mfma_f32_32x32x16_bf16(a, b, c, 0, 0, 0)
#define MFMA16(a, b, c) __builtin_amdgcn_mfma_f32_16x16x32_bf16(a, b, c, 0, 0, 0)
#define SBAR() __builtin_amdgcn_sched_barrier(0)
#define PIN(x) asm volatile("" : "+v"(x))
#define WAIT_BAR(N) asm volatile("s_waitcnt vmcnt(" #N ") lgkmcnt(0)\n\ts_barrier" ::: "memory")
#define LBAR() asm volatile("s_waitcnt lgkmcnt(0)\n\ts_barrier" ::: "memory")
#define DI __device__ __forceinline__
#define ST8(p, w) st8_wt((void*)(p), (w))
#define ST16(off, v) __builtin_amdgcn_raw_buffer_store_b128((v), wsr, (off), 0, 16)
#define STAMP(kern, i) do { } while (0)

constexpr int S = 2048, D = 512, H = 32, DH = 16, FH = 896;
constexpr float LOG2E = 1.4426950408889634f;

constexpr size_t WS_Q = 0, WS_K = 2u << 20, WS_V = 4u << 20, WS_Z1 = 6u << 20, WS_Z2R = 10u << 20, WS_Z2N = 14u << 20, WS_H = 18u << 20, WS_O = 22u << 20, WS_KMAX = 24u << 20, WS_EPOCH = 25u << 20, WS_XCH = 26u << 20, WS_XST = 27u << 20, WS_CNT = 28u << 20;

DI unsigned cvtpk(float lo, float hi) { f32x2_t v = {lo, hi}; return __builtin_bit_cast(unsigned, __builtin_convertvector(v, bf16x2_t)); }
DI float bflo(unsigned w) { return __uint_as_float(w << 16); }
DI float bfhi(unsigned w) { return __uint_as_float(w & 0xFFFF0000u); }
DI float dot2(unsigned a, unsigned b, float c) { return __builtin_amdgcn_fdot2_f32_bf16(__builtin_bit_cast(bf16x2_t, a), __builtin_bit_cast(bf16x2_t, b), c, false); }
DI void glds16s(unsigned voff, const void* sbase, unsigned lds_base) {
    lds_base = __builtin_amdgcn_readfirstlane(lds_base);
    unsigned sv; asm volatile("s_mov_b32 %0, m0\n\ts_mov_b32 m0, %3\n\ts_nop 0\n\tglobal_load_lds_dwordx4 %1, %2\n\ts_mov_b32 m0, %0" : "=&s"(sv) : "v"(voff), "s"(sbase), "s"(lds_base) : "memory"); }
DI bf16x8 ldsr128(lds_cptr p) { return *(const LDSAS bf16x8*)p; }
DI s16x4 ldstr(lds_cptr p) { return __builtin_bit_cast(s16x4, __builtin_amdgcn_ds_read_tr16_b64_v4i16((LDSAS v4i16_t*)p)); }
DI bf16x8 cat(s16x4 a, s16x4 b) { return __builtin_shufflevector(a, b, 0, 1, 2, 3, 4, 5, 6, 7); }
DI float swap32(float x, bool sum) { auto rr = __builtin_amdgcn_permlane32_swap(__float_as_uint(x), __float_as_uint(x), false, false); return sum ? __uint_as_float(rr[0]) + __uint_as_float(rr[1]) : __uint_as_float(rr[0]); }
DI float gelu_t(float x) {
    const float c1 = -2.0f * 0.7978845608028654f * LOG2E, c3 = c1 * 0.044715f;
    const float u = x * fmaf(x * x, c3, c1);
    return x * __builtin_amdgcn_rcpf(1.0f + __builtin_amdgcn_exp2f(u));
}

constexpr int K1_XB = 16384;
constexpr int K1_STAGE = K1_XB + 7 * 4096;
constexpr int K1_CS = 3 * K1_STAGE;
constexpr int K1_LDS = K1_CS + 1024;
typedef __attribute__((address_space(1))) unsigned gu32; typedef __attribute__((address_space(1))) unsigned long long gu64;
#define RLX_AGENT __ATOMIC_RELAXED, __HIP_MEMORY_SCOPE_AGENT

DI float xor16(float x) { auto r = __builtin_amdgcn_permlane16_swap(__float_as_uint(x), __float_as_uint(x), false, false); return __uint_as_float(r[0]) + __uint_as_float(r[1]); }
DI float xor32(float x) { auto r = __builtin_amdgcn_permlane32_swap(__float_as_uint(x), __float_as_uint(x), false, false); return __uint_as_float(r[0]) + __uint_as_float(r[1]); }
DI float max16(float x) { auto r = __builtin_amdgcn_permlane16_swap(__float_as_uint(x), __float_as_uint(x), false, false); return fmaxf(__uint_as_float(r[0]), __uint_as_float(r[1])); }
DI float max32(float x) { auto r = __builtin_amdgcn_permlane32_swap(__float_as_uint(x), __float_as_uint(x), false, false); return fmaxf(__uint_as_float(r[0]), __uint_as_float(r[1])); }
template <int CTRL> DI float dppf(float x) { return __uint_as_float(__builtin_amdgcn_update_dpp(0, __float_as_uint(x), CTRL, 0xF, 0xF, false)); }
DI float rowmax16(float x) { x = fmaxf(x, dppf<0x128>(x)); x = fmaxf(x, dppf<0x124>(x)); x = fmaxf(x, dppf<0x122>(x)); x = fmaxf(x, dppf<0x121>(x)); return x; }
DI float wavesum(float x) { x += dppf<0x128>(x); x += dppf<0x124>(x); x += dppf<0x122>(x); x += dppf<0x121>(x); return xor32(xor16(x)); }
DI u32x4 widen(u32x2 a, u32x2 b) { auto r0 = __builtin_amdgcn_permlane32_swap(a[0], b[0], false, false); auto r1 = __builtin_amdgcn_permlane32_swap(a[1], b[1], false, false); return (u32x4){r0[0], r1[0], r0[1], r1[1]}; }

DI void st8_wt(void* p, u32x2 w) { __hip_atomic_store((gu64*)p, ((unsigned long long)w[1] << 32) | w[0], RLX_AGENT); }
__global__ __launch_bounds__(768, 3) void k1_proj(const u16* __restrict__ x, const u16* __restrict__ wqkv, const u16* __restrict__ win, char* __restrict__ ws) {
    extern __shared__ __attribute__((aligned(16))) char lds[];
    const int tid = threadIdx.x, lane = tid & 63, r32 = lane & 31, hi = lane >> 5;
    const int wid = __builtin_amdgcn_readfirstlane(tid >> 6);
    const int b = blockIdx.x, xcd = b & 7, idx = b >> 3;
    const int panel = 8 * (xcd >> 2) + (idx & 7), j = 4 * (xcd & 3) + (idx >> 3);
    const unsigned lds0 = (unsigned)(uintptr_t)lds;
    const int jq = j & 3, u7 = 7 * (j >> 2);
    const int n1 = jq == 3 ? 1 : 2, n2 = jq == 1 ? 1 : 2;
    const int z1t0 = u7 + 2 * jq, z2t0 = u7 + (jq == 0 ? 0 : (jq == 1 ? 2 : (jq == 2 ? 3 : 5)));
    STAMP(0, 0);
    if (b == 0 && tid < 40) ((unsigned*)(ws + WS_CNT))[tid * 64] = 0u;
    if (wid >= 8) {
        const int l = wid - 8;
        const char* xb = (const char*)x + (size_t)(panel * 128 + l * 32) * 1024;
        const unsigned xv0 = (unsigned)((lane >> 3) * 1024 + (((lane & 7) ^ (lane >> 4)) * 16)), xv1 = (unsigned)((lane >> 3) * 1024 + (((lane & 7) ^ (4 + (lane >> 4))) * 16));
        const unsigned vq = (unsigned)((16 * l + (lane >> 2)) * 3072 + (lane & 3) * 16), vz = (unsigned)((16 * l + (lane >> 2)) * 3584 + (lane & 3) * 16);
        const char* wq = (const char*)wqkv + j * 64;
        int zc[4];
#pragma unroll
        for (int i = 0; i < 4; ++i) { const int ii = i < n1 + n2 ? i : n1 + n2 - 1; zc[i] = ii < n1 ? (z1t0 + ii) * 64 : FH * 2 + (z2t0 + ii - n1) * 64; }
        auto dma = [&](int st) __attribute__((always_inline)) {
            const unsigned d = lds0 + (st % 3) * K1_STAGE;
#pragma unroll
            for (int p = 0; p < 4; ++p) glds16s((p & 1) ? xv1 : xv0, xb + st * 128 + p * 8192, d + l * 4096 + p * 1024);
            const char* bq = wq + (size_t)st * 64 * 3072; const char* bz = (const char*)win + (size_t)st * 64 * 3584; const unsigned dw = d + K1_XB + l * 1024;
            glds16s(vq, bq + 2048, dw); glds16s(vq, bq, dw + 4096); glds16s(vq, bq + 1024, dw + 8192);
#pragma unroll
            for (int i = 0; i < 4; ++i) glds16s(vz, bz + zc[i], dw + (3 + i) * 4096);
        };
        dma(0);
        WAIT_BAR(0);
        dma(1); dma(2);
#pragma unroll
        for (int st = 1; st < 8; ++st) {
            if (st < 7) WAIT_BAR(11); else WAIT_BAR(0);
            if (st + 2 < 8) dma(st + 2);
        }
        LBAR();
        return;
    }
    const int mw = wid & 3, nh = wid >> 2;
    gu32* epw = (gu32*)(ws + WS_EPOCH) + panel * 64;
    unsigned tag = __hip_atomic_load(epw, RLX_AGENT);
    u32x4 xr = *(const u32x4*)(x + (size_t)(panel * 128 + 8 * j + wid) * D + lane * 8);
    auto rowstats = [&]() __attribute__((always_inline)) {
        PIN(xr); PIN(tag); tag += 1u;
        float v[8];
#pragma unroll
        for (int i = 0; i < 4; ++i) { v[2 * i] = bflo(xr[i]); v[2 * i + 1] = bfhi(xr[i]); }
        const float m = wavesum(((v[0] + v[1]) + (v[2] + v[3])) + ((v[4] + v[5]) + (v[6] + v[7]))) * (1.0f / 512.0f);
        float q = 0.f;
#pragma unroll
        for (int i = 0; i < 8; ++i) q = fmaf(v[i] - m, v[i] - m, q);
        const float r = __builtin_amdgcn_rsqf(wavesum(q) * (1.0f / 512.0f) + 1e-5f);
        if (lane < 2) __hip_atomic_store((gu64*)(ws + WS_XST) + (size_t)(panel * 128 + 8 * j + wid) * 2 + lane, ((unsigned long long)tag << 32) | __float_as_uint(lane ? r : m), RLX_AGENT);
    };
    f32x16 acc[4];
#pragma unroll
    for (int s = 0; s < 4; ++s) acc[s] = (f32x16){};
    float cs0 = 0.f, cs1 = 0.f;
    const int g = lane >> 4, qq = (lane & 15) >> 2, pp = lane & 3;
    const int trow = 32 * mw + r32;
    unsigned xo[4];
#pragma unroll
    for (int ks = 0; ks < 4; ++ks) xo[ks] = (unsigned)(trow * 128 + (((2 * ks + hi) ^ ((trow >> 1) & 7)) * 16));
    const unsigned wlane = (unsigned)(K1_XB + (8 * (g >> 1) + qq) * 64 + (g & 1) * 32 + pp * 8);
    const __amdgpu_buffer_rsrc_t sr = __builtin_amdgcn_make_buffer_rsrc((void*)(ws + WS_XST + (size_t)panel * 128 * 16), 0, 128 * 16, 0x00020000);
    u32x4 pst;
    int tile[4];
    auto mainloop = [&](auto NHC) __attribute__((always_inline)) {
        constexpr int NHV = decltype(NHC)::value; constexpr int NS = NHV == 0 ? 4 : 3;
        unsigned wo[NS];
#pragma unroll
        for (int s = 0; s < NS; ++s) { int t = s + mw; t = t >= NS ? t - NS : t; t = t >= NS ? t - NS : t; tile[s] = 4 * NHV + t; wo[s] = wlane + tile[s] * 4096; }
        bf16x8 xa[2], wf[2][NS];
        auto rd = [&](const int set, const lds_cptr sb, const int ks) __attribute__((always_inline)) {
            xa[set] = ldsr128(sb + xo[ks]);
#pragma unroll
            for (int s = 0; s < NS; ++s) wf[set][s] = cat(ldstr(sb + wo[s] + ks * 1024), ldstr(sb + wo[s] + ks * 1024 + 256));
        };
        auto mm = [&](const int set) __attribute__((always_inline)) {
            SBAR();
#pragma unroll
            for (int s = 0; s < NS; ++s) acc[s] = MFMA32(wf[set][s], xa[set], acc[s]);
            const u32x4 w = __builtin_bit_cast(u32x4, wf[set][0]);
#pragma unroll
            for (int i = 0; i < 4; ++i) { cs0 += bflo(w[i]); cs1 += bfhi(w[i]); }
            PIN(cs0); PIN(cs1);
#pragma unroll
            for (int i = 0; i < NS; ++i) { __builtin_amdgcn_sched_group_barrier(0x008, 1, 0); __builtin_amdgcn_sched_group_barrier(0x002, NS == 4 ? 4 : 6, 0); }
            SBAR();
        };
        LBAR();
        STAMP(0, 1);
        rd(0, (lds_cptr)lds, 0);
#pragma unroll
        for (int st = 0; st < 8; ++st) {
            const lds_cptr sb = (lds_cptr)lds + (st % 3) * K1_STAGE;
            rd(1, sb, 1); mm(0);
            rd(0, sb, 2); mm(1);
            rd(1, sb, 3); mm(0);
            if (st < 7) { LBAR(); rd(0, (lds_cptr)lds + ((st + 1) % 3) * K1_STAGE, 0); }
            if (st == 1) rowstats();
            if (st == 5) pst = __builtin_amdgcn_raw_buffer_load_b128(sr, trow * 16, 0, 16);
            mm(1);
        }
    };
    if (nh == 0) mainloop(std::integral_constant<int, 0>{}); else mainloop(std::integral_constant<int, 1>{});
    STAMP(0, 2);
    float* csl = (float*)(lds + K1_CS);
    { const float c = xor32(cs0 + cs1); if (hi == 0) csl[tile[0] * 32 + r32] = c; }
    const int tok = panel * 128 + trow;
    float mu, rs;
    {   u32x4 p = pst;
        for (unsigned spins = 0; !__all((p[1] == tag) & (p[3] == tag)) && spins < 200000u; ++spins) {
            __builtin_amdgcn_s_sleep(1);
            p = __builtin_amdgcn_raw_buffer_load_b128(sr, trow * 16, 0, 16);
        }
        mu = __uint_as_float(p[0]); rs = __uint_as_float(p[2]); }
    LBAR();
    STAMP(0, 3);
    auto lnv = [&](int s, float (&v)[16], float sc) __attribute__((always_inline)) {
#pragma unroll
        for (int q = 0; q < 4; ++q) { const f32x4 c = *(const f32x4*)(csl + tile[s] * 32 + 8 * q + 4 * hi);
#pragma unroll
            for (int r = 0; r < 4; ++r) v[4 * q + r] = sc * (acc[s][4 * q + r] - mu * c[r]); } };
    const __amdgpu_buffer_rsrc_t wsr = __builtin_amdgcn_make_buffer_rsrc((void*)ws, 0, 32u << 20, 0x00020000);
    u16* Z1 = (u16*)(ws + WS_Z1);
    auto z1store = [&](int zt, const u32x2 (&w)[4]) __attribute__((always_inline)) {
        u16* p = Z1 + ((size_t)((tok >> 5) * 28 + zt) * 1024) + (hi * 32 + r32) * 4;
#pragma unroll
        for (int q = 0; q < 4; ++q) ST8(p + q * 256, w[q]); };
    auto gelu16 = [&](int s, u32x2 (&w)[4]) __attribute__((always_inline)) { float v[16]; lnv(s, v, rs);
#pragma unroll
        for (int q = 0; q < 4; ++q) w[q] = (u32x2){cvtpk(gelu_t(v[4 * q]), gelu_t(v[4 * q + 1])), cvtpk(gelu_t(v[4 * q + 2]), gelu_t(v[4 * q + 3]))}; };
    if (nh == 0) {
#pragma unroll
        for (int s = 0; s < 4; ++s) {
            const int T = tile[s];
            if (T == 3) { u32x2 w[4]; gelu16(s, w); z1store(z1t0, w); }
            else {
                float v[16]; lnv(s, v, T == 1 ? rs * (0.25f * LOG2E) : rs);
                u32x2 w[4];
#pragma unroll
                for (int q = 0; q < 4; ++q) w[q] = (u32x2){cvtpk(v[4 * q], v[4 * q + 1]), cvtpk(v[4 * q + 2], v[4 * q + 3])};
                if (T == 0) {
                    char* sc = lds + mw * 2048;
#pragma unroll
                    for (int q = 0; q < 4; ++q) *(u32x2*)(sc + r32 * 64 + (8 * q + 4 * hi) * 2) = w[q];
                    asm volatile("s_waitcnt lgkmcnt(0)" ::: "memory");
                    u16* Vt = (u16*)(ws + WS_V);
#pragma unroll
                    for (int hh = 0; hh < 2; ++hh)
#pragma unroll
                        for (int ks = 0; ks < 2; ++ks) {
                            const s16x4 t4 = ldstr((lds_cptr)lds + mw * 2048 + (16 * ks + 8 * (g >> 1) + 4 * (g & 1) + qq) * 64 + hh * 32 + pp * 8);
                            ST8(Vt + ((size_t)(2 * j + hh) * 128 + ((panel * 128 + 32 * mw) >> 4) + ks) * 256 + (g & 1) * 128 + (lane & 15) * 8 + (g >> 1) * 4, __builtin_bit_cast(u32x2, t4)); }
                } else {
                    const unsigned dsto = (unsigned)(T == 1 ? WS_Q : WS_K);
#pragma unroll
                    for (int hh = 0; hh < 2; ++hh) ST16(dsto + (((2 * j + hh) * S + tok) * 16 + 8 * hi) * 2, widen(w[2 * hh], w[2 * hh + 1]));
                    if (T == 2) {
                        float* kmax = (float*)(ws + WS_KMAX);
#pragma unroll
                        for (int hh = 0; hh < 2; ++hh) { float n = 0.f;
#pragma unroll
                            for (int q = 0; q < 2; ++q) { const u32x2 ww = w[2 * hh + q]; n = fmaf(bflo(ww[0]), bflo(ww[0]), n); n = fmaf(bfhi(ww[0]), bfhi(ww[0]), n); n = fmaf(bflo(ww[1]), bflo(ww[1]), n); n = fmaf(bfhi(ww[1]), bfhi(ww[1]), n); }
                            n = max16(rowmax16(xor32(n)));
                            if (lane == 0) kmax[(panel * 4 + mw) * H + 2 * j + hh] = n; }
                    }
                }
            }
        }
    } else {
        __builtin_amdgcn_s_setprio(2);
        float sm = 0.f;
#pragma unroll
        for (int s = 0; s < 3; ++s) { const int zi = tile[s] - 3;
            if (zi >= n1 && zi < n1 + n2) { float v[16]; lnv(s, v, rs);
#pragma unroll
                for (int r = 0; r < 16; ++r) { acc[s][r] = gelu_t(v[r]); sm += acc[s][r]; } } }
        sm = xor32(sm);
        const float ml = sm * (n2 == 2 ? (1.0f / 64.f) : (1.0f / 32.f)); float m2 = 0.f;
#pragma unroll
        for (int s = 0; s < 3; ++s) { const int zi = tile[s] - 3;
            if (zi >= n1 && zi < n1 + n2) {
#pragma unroll
                for (int r = 0; r < 16; ++r) { const float d = acc[s][r] - ml; m2 = fmaf(d, d, m2); } } }
        m2 = xor32(m2);
        __hip_atomic_store((gu64*)(ws + WS_XCH) + ((size_t)(panel * 16 + j) * 128 + trow) * 2 + hi, ((unsigned long long)tag << 32) | __float_as_uint(hi ? m2 : sm), RLX_AGENT);
        __builtin_amdgcn_s_setprio(0);
        STAMP(0, 4);
#pragma unroll
        for (int s = 0; s < 3; ++s) { const int zi = tile[s] - 3; if (zi < n1) { u32x2 w[4]; gelu16(s, w); z1store(z1t0 + zi, w); } }
        float mean, rstd;
        {
            const __amdgpu_buffer_rsrc_t xr = __builtin_amdgcn_make_buffer_rsrc((void*)(ws + WS_XCH + (size_t)panel * 16 * 128 * 16), 0, 16 * 128 * 16, 0x00020000);
            u32x4 p[8];
            for (unsigned spins = 0;; ++spins) {
                bool ok = true;
#pragma unroll
                for (int u = 0; u < 8; ++u) p[u] = __builtin_amdgcn_raw_buffer_load_b128(xr, ((8 * hi + u) * 128 + trow) * 16, 0, 16);
#pragma unroll
                for (int u = 0; u < 8; ++u) ok &= (p[u][1] == tag) & (p[u][3] == tag);
                if (__all(ok) || spins > 200000u) break;
                __builtin_amdgcn_s_sleep(6);
            }
            float tot = 0.f;
#pragma unroll
            for (int u = 0; u < 8; ++u) tot += __uint_as_float(p[u][0]);
            mean = xor32(tot) * (1.0f / FH);
            float mm2 = 0.f;
#pragma unroll
            for (int u = 0; u < 8; ++u) { const float nl = (u & 3) == 1 ? 32.f : 64.f; const float d = __uint_as_float(p[u][0]) * ((u & 3) == 1 ? (1.0f / 32.f) : (1.0f / 64.f)) - mean; mm2 += __uint_as_float(p[u][2]) + nl * d * d; }
            rstd = __builtin_amdgcn_rsqf(xor32(mm2) * (1.0f / FH) + 1e-5f);
        }
        STAMP(0, 5);
        if (wid == 4 && lane == 0) __hip_atomic_store(epw, tag, RLX_AGENT);
        const float nmr = -mean * rstd;
#pragma unroll
        for (int s = 0; s < 3; ++s) { const int zi = tile[s] - 3;
            if (zi >= n1 && zi < n1 + n2) {
                const int ft = z2t0 + zi - n1;
                u32x2 w[4];
#pragma unroll
                for (int q = 0; q < 4; ++q) w[q] = (u32x2){cvtpk(fmaf(acc[s][4 * q], rstd, nmr), fmaf(acc[s][4 * q + 1], rstd, nmr)), cvtpk(fmaf(acc[s][4 * q + 2], rstd, nmr), fmaf(acc[s][4 * q + 3], rstd, nmr))};
                char* sc = lds + 8192 + mw * 2048;
#pragma unroll
                for (int q = 0; q < 4; ++q) *(u32x2*)(sc + r32 * 64 + (8 * q + 4 * hi) * 2) = w[q];
                asm volatile("s_waitcnt lgkmcnt(0)" ::: "memory");
                const int tk0 = panel * 128 + 32 * mw;
#pragma unroll
                for (int ks = 0; ks < 2; ++ks) {
                    const lds_cptr rp = (lds_cptr)lds + 8192 + mw * 2048 + (16 * ks + 8 * (g >> 1) + qq) * 64 + (g & 1) * 32 + pp * 8;
                    const bf16x8 fr = cat(ldstr(rp), ldstr(rp + 256));
                    ST16((unsigned)WS_Z2N + (unsigned)(((((ft >> 2) * 32 + (tk0 >> 6)) * 4 + (ft & 3)) * 4 + ((tk0 >> 4) & 3) + ks) * 1024 + lane * 16), __builtin_bit_cast(u32x4, fr));
                }
                asm volatile("s_waitcnt lgkmcnt(0)" ::: "memory");
            } }
    }
    STAMP(0, 6);
}

constexpr int A_KR = 0, A_VR = 32768, A_CONST = 65536, A_FLAG = A_CONST + 4096, A_MX = A_FLAG + 64, K2_LDS = 81920;
constexpr float SAFE_T = 48.f;
#define EX(x) __builtin_amdgcn_exp2f(x)
#define BF(p) __builtin_bit_cast(bf16x8, p)

DI void attn_role(int b, char* __restrict__ ws, char* lds) {
    const u16* Qs = (const u16*)(ws + WS_Q); const u16* Kb = (const u16*)(ws + WS_K); const u16* Vt = (const u16*)(ws + WS_V); const float* kmax = (const float*)(ws + WS_KMAX); u16* Ot = (u16*)(ws + WS_O);
    const int tid = threadIdx.x, lane = tid & 63, r32 = lane & 31, hi = lane >> 5;
    const int wid = __builtin_amdgcn_readfirstlane(tid >> 6);
    const int xcd = b & 7, idx = b >> 3, head = xcd * 4 + (idx & 3), qt = idx >> 2;
    const int qp = wid & 3, kh = wid >> 2;
    const unsigned lds0 = (unsigned)(uintptr_t)lds;
    const char* kg = (const char*)(Kb + (size_t)head * S * DH);
    const char* vg = (const char*)(Vt + (size_t)head * S * DH);
    const unsigned kvoff = wid * 1024 + ((lane ^ ((lane >> 5) & 1)) * 16);
    const unsigned vvoff = wid * 1024 + lane * 16;
#define DMA_CHUNK(c) do { glds16s(kvoff, kg + (c) * 8192, lds0 + A_KR + ((c) & 3) * 8192 + wid * 1024); glds16s(vvoff, vg + (c) * 8192, lds0 + A_VR + ((c) & 3) * 8192 + wid * 1024); } while (0)
    STAMP(1, 0);
    const int q0 = qt * 256 + qp * 64;
    const bf16x8 qa = *(const bf16x8*)(Qs + ((size_t)head * S + q0 + r32) * DH + hi * 8);
    const bf16x8 qb = *(const bf16x8*)(Qs + ((size_t)head * S + q0 + 32 + r32) * DH + hi * 8);
    float km = kmax[lane * H + head];
    DMA_CHUNK(0);
    if (tid < 256) { const unsigned w = (tid & 31) ? 0u : 0x3F803F80u; u32x4 v = {w, w, w, w}; *(u32x4*)(lds + A_CONST + tid * 16) = v; }
    km = max32(max16(rowmax16(km)));
    float qna = 0.f, qnb = 0.f;
#pragma unroll
    for (int j = 0; j < 8; ++j) { const float va = bflo((u16)qa[j]), vb = bflo((u16)qb[j]); qna = fmaf(va, va, qna); qnb = fmaf(vb, vb, qnb); }
    qna = xor32(qna); qnb = xor32(qnb);
    const float mh = __builtin_amdgcn_sqrtf(fmaxf(qna, qnb) * km) * 1.001f;
    { const bool unsafe = __any(!(mh <= SAFE_T)); if (lane == 0) ((unsigned*)(lds + A_FLAG))[wid] = unsafe ? 1u : 0u; }
    const lds_cptr kp0 = (lds_cptr)lds + A_KR + kh * 4096 + r32 * 32 + ((hi ^ (r32 >> 4)) * 16);
    const bool vreal = r32 < 16;
    const unsigned vlane = vreal ? (unsigned)(A_VR + kh * 4096 + hi * 256 + r32 * 16) : (unsigned)(A_CONST + (r32 == 16 ? 0 : ((r32 >= 20 && r32 < 28) ? 64 : 16)));
    const unsigned vmask = vreal ? 0xFFFFFFFFu : 0u;
    f32x16 oa = {}, ob = {};
    WAIT_BAR(0);
    bool slow;
    { const u32x4 f0 = *(const u32x4*)(lds + A_FLAG), f1 = *(const u32x4*)(lds + A_FLAG + 16); slow = __builtin_amdgcn_readfirstlane(f0[0] | f0[1] | f0[2] | f0[3] | f1[0] | f1[1] | f1[2] | f1[3]) != 0; }
    if (!slow) {
        DMA_CHUNK(1); DMA_CHUNK(2); DMA_CHUNK(3);
        f32x16 cinit;
#pragma unroll
        for (int r = 0; r < 16; ++r) cinit[r] = -mh;
        PIN(cinit);
        bf16x8 kf, v0, v1; f32x16 sA, sB; u32x4 p0, p1;
        kf = ldsr128(kp0);
        { lds_cptr vp = (lds_cptr)lds + vlane; v0 = ldsr128(vp); v1 = ldsr128(vp + 512); }
        sA = MFMA32(kf, qa, cinit);
        p1 = (u32x4){0u, 0u, 0u, 0u};
#define HSTEP(SC, SN, QN, OP, OC, PRE1, PRE2, PRE3) do { \
        SN = MFMA32(kf, QN, cinit); PIN(SN); SBAR(); \
        PRE1; \
        SC[0] = EX(SC[0]); SC[1] = EX(SC[1]); SC[2] = EX(SC[2]); SC[3] = EX(SC[3]); SC[4] = EX(SC[4]); SC[5] = EX(SC[5]); PIN(SC); SBAR(); \
        OP = MFMA32(v1, BF(p1), OP); PIN(OP); SBAR(); \
        PRE2; \
        SC[6] = EX(SC[6]); SC[7] = EX(SC[7]); \
        p0[0] = cvtpk(SC[0], SC[1]); p0[1] = cvtpk(SC[2], SC[3]); p0[2] = cvtpk(SC[4], SC[5]); p0[3] = cvtpk(SC[6], SC[7]); PIN(p0); SBAR(); \
        SC[8] = EX(SC[8]); SC[9] = EX(SC[9]); SC[10] = EX(SC[10]); PIN(SC); SBAR(); \
        OC = MFMA32(v0, BF(p0), OC); PIN(OC); SBAR(); \
        PRE3; \
        SC[11] = EX(SC[11]); SC[12] = EX(SC[12]); SC[13] = EX(SC[13]); SC[14] = EX(SC[14]); SC[15] = EX(SC[15]); \
        p1[0] = cvtpk(SC[8], SC[9]); p1[1] = cvtpk(SC[10], SC[11]); p1[2] = cvtpk(SC[12], SC[13]); p1[3] = cvtpk(SC[14], SC[15]); PIN(p1); SBAR(); \
    } while (0)
#define BLOCK(kn, vc, vco, vn, vno) do { \
        HSTEP(sA, sB, qb, ob, oa, kf = ldsr128(kn), v1 = ldsr128((vc) + (vco) + 512), (void)0); \
        HSTEP(sB, sA, qa, oa, ob, (void)0, (void)0, v0 = ldsr128((vn) + (vno))); \
    } while (0)
        STAMP(1, 1);
        for (int c = 0; c < 8; ++c) {
            const unsigned ro = (unsigned)((c & 3) * 8192), rn = (unsigned)(((c + 1) & 3) * 8192);
            const lds_cptr vp = (lds_cptr)lds + (vlane + (ro & vmask)), vpn = (lds_cptr)lds + (vlane + (rn & vmask));
            const lds_cptr kp = kp0 + ro, kpn = kp0 + rn;
            BLOCK(kp + 1024, vp, 0, vp, 1024);
            BLOCK(kp + 2048, vp, 1024, vp, 2048);
            BLOCK(kp + 3072, vp, 2048, vp, 3072);
            if (c == 0) WAIT_BAR(4); else if (c < 6) WAIT_BAR(2); else if (c == 6) WAIT_BAR(0);
            if (c >= 1 && c + 3 < 8) DMA_CHUNK(c + 3);
            BLOCK(kpn, vp, 3072, vpn, 0);
        }
        ob = MFMA32(v1, BF(p1), ob);
#undef HSTEP
#undef BLOCK
    } else {
        float ma = -3.0e38f, mb = -3.0e38f;
        for (int pass = 0; pass < 2; ++pass) {
            for (int c = 0; c < 8; ++c) {
                LBAR();
                DMA_CHUNK(c);
                WAIT_BAR(0);
                const unsigned ro = (unsigned)((c & 3) * 8192);
                const lds_cptr vp = (lds_cptr)lds + (vlane + (ro & vmask)), kp = kp0 + ro;
                for (int j = 0; j < 4; ++j) {
                    const bf16x8 kf = ldsr128(kp + j * 1024);
                    const bf16x8 v0 = ldsr128(vp + j * 1024), v1 = ldsr128(vp + j * 1024 + 512);
#pragma unroll
                    for (int u = 0; u < 2; ++u) {
                        const f32x16 sc = MFMA32(kf, u ? qb : qa, (f32x16){});
                        float& m = u ? mb : ma; f32x16& o = u ? ob : oa;
                        if (pass == 0) {
#pragma unroll
                            for (int r = 0; r < 16; ++r) m = fmaxf(m, sc[r]);
                        } else {
                            u32x4 p0, p1;
#pragma unroll
                            for (int i = 0; i < 4; ++i) { p0[i] = cvtpk(EX(sc[2 * i] - m), EX(sc[2 * i + 1] - m)); p1[i] = cvtpk(EX(sc[8 + 2 * i] - m), EX(sc[9 + 2 * i] - m)); }
                            o = MFMA32(v0, BF(p0), o); o = MFMA32(v1, BF(p1), o);
                        }
                        SBAR();
                    }
                }
            }
            if (pass == 0) {
                ma = max32(ma); mb = max32(mb);
                float* mx = (float*)(lds + A_MX);
                if (hi == 0) { mx[(wid * 2) * 32 + r32] = ma; mx[(wid * 2 + 1) * 32 + r32] = mb; }
                LBAR();
                ma = fmaxf(ma, mx[((wid ^ 4) * 2) * 32 + r32]); mb = fmaxf(mb, mx[((wid ^ 4) * 2 + 1) * 32 + r32]);
            }
        }
    }
    STAMP(1, 2);
    __syncthreads();
    int tz = threadIdx.x; asm volatile("" : "+v"(tz));
    const int lz = tz & 63;
    float* red = (float*)lds;
    { float* dst = red + (size_t)(wid ^ 4) * 9 * 64 + lz;
#pragma unroll
      for (int r = 0; r < 9; ++r) dst[r * 64] = kh ? oa[r] : ob[r]; }
    __syncthreads();
    float o[9];
    { const float* src = red + (size_t)wid * 9 * 64 + lz;
#pragma unroll
      for (int r = 0; r < 9; ++r) o[r] = (kh ? ob[r] : oa[r]) + src[r * 64]; }
    const float rl = __builtin_amdgcn_rcpf(swap32(o[8], false));
    u16* op = Ot + ((size_t)(((q0 >> 5) + kh) * H + head) * 512) + (lz & 31) * 8 + (lz >> 5) * 4;
    st8_wt(op, (u32x2){cvtpk(o[0] * rl, o[1] * rl), cvtpk(o[2] * rl, o[3] * rl)});
    st8_wt(op + 256, (u32x2){cvtpk(o[4] * rl, o[5] * rl), cvtpk(o[6] * rl, o[7] * rl)});
    asm volatile("s_waitcnt vmcnt(0)\n\ts_barrier" ::: "memory");
    if (tz == 0) __hip_atomic_fetch_add((gu32*)(ws + WS_CNT) + (32 + qt) * 64, 1u, RLX_AGENT);
    STAMP(1, 3);
#undef DMA_CHUNK
}

DI void wait_cnt(gu32* cnt, unsigned want) {
    for (unsigned spins = 0; spins < 400000u; ++spins) {
        if ((unsigned)__builtin_amdgcn_readfirstlane(__hip_atomic_load(cnt, RLX_AGENT)) >= want) break;
        __builtin_amdgcn_s_sleep(1);
    }
}

#define O_AUX 16
#define H_AUX 16
constexpr int G_STAGE = 24576;
constexpr int W_STAGE = 16384;
constexpr int G_RED = 4 * W_STAGE;
DI void gate_role(int b, const u16* __restrict__ x, const u16* __restrict__ wo, const u16* __restrict__ wsp, const u16* __restrict__ bsp, const u16* __restrict__ wout, char* __restrict__ ws, u16* __restrict__ out, char* lds) {
    const int xcd = b & 7, idx = b >> 3;
    const int tt = xcd * 4 + (idx & 3), ct = idx >> 2;
    const int tid = threadIdx.x, lane = tid & 63, r32 = lane & 31, hi = lane >> 5;
    const int wid = __builtin_amdgcn_readfirstlane(tid >> 6);
    const unsigned lds0 = (unsigned)(uintptr_t)lds;
    gu32* hcnt = (gu32*)(ws + WS_CNT) + tt * 64; gu32* ocnt = (gu32*)(ws + WS_CNT) + (32 + (tt >> 2)) * 64;
    STAMP(1, 0);
    if (wid >= 4) {
        const int l = wid - 4;
        if (ct < 7) {
            const char* ag = ws + WS_Z2N + (size_t)ct * 32 * 16384 + l * 4096;
            const char* bg = (const char*)wsp + (size_t)(tt * 64 + l * 16) * 4096;
            const unsigned avoff = lane * 16;
            const unsigned bv0 = (unsigned)((lane >> 3) * 4096 + (((lane & 7) ^ (lane >> 4)) * 16)), bv1 = (unsigned)((8 + (lane >> 3)) * 4096 + (((lane & 7) ^ (4 + (lane >> 4))) * 16));
            auto dma = [&](int kt, int slot) __attribute__((always_inline)) { const unsigned d = lds0 + slot * G_STAGE; const char* a_ = ag + (size_t)kt * 16384;
#pragma unroll
                for (int e = 0; e < 4; ++e) glds16s(avoff, a_ + e * 1024, d + l * 4096 + e * 1024);
                glds16s(bv0, bg + kt * 128, d + 16384 + l * 2048); glds16s(bv1, bg + kt * 128, d + 16384 + l * 2048 + 1024); };
            dma(0, 0);
            WAIT_BAR(0);
            dma(1, 1); dma(2, 2);
            int slot = 0;
            for (int kt = 1; kt < 32; ++kt) {
                if (kt < 31) WAIT_BAR(6); else WAIT_BAR(0);
                if (kt + 2 < 32) dma(kt + 2, slot);
                slot = slot == 2 ? 0 : slot + 1;
            }
            LBAR();
        }
        unsigned wv[4];
#pragma unroll
        for (int e = 0; e < 4; ++e) { const int row = l * 32 + e * 8 + (lane >> 3); wv[e] = (unsigned)(row * 1024 + ct * 128 + (((lane & 7) ^ (((row >> 1) & 1) << 2)) * 16)); }
        auto dmw = [&](const u16* w, int st) __attribute__((always_inline)) { const char* wg = (const char*)w + (size_t)st * 128 * 1024; const unsigned d = lds0 + (st & 3) * W_STAGE + l * 4096;
#pragma unroll
            for (int e = 0; e < 4; ++e) glds16s(wv[e], wg, d + e * 1024); };
        dmw(wout, 0); dmw(wout, 1); dmw(wout, 2); dmw(wout, 3);
        if (l == 0) { wait_cnt(hcnt, 7);
        }
        WAIT_BAR(12);
#pragma unroll
        for (int st = 1; st < 7; ++st) {
            if (st < 4) WAIT_BAR(8); else if (st == 4) WAIT_BAR(8); else if (st == 5) WAIT_BAR(4); else WAIT_BAR(0);
            if (st + 3 < 7) dmw(wout, st + 3);
        }
        LBAR();
        dmw(wo, 0); dmw(wo, 1); dmw(wo, 2); dmw(wo, 3);
        if (l == 0) { wait_cnt(ocnt, 32);
        }
        WAIT_BAR(0);
        return;
    }
#define GPRIO 2
    __builtin_amdgcn_s_setprio(GPRIO);
    const int g = lane >> 4, qq = (lane & 15) >> 2, pp = lane & 3;
    const int th = wid & 1, fh = wid >> 1;
    const int trow = 32 * th + r32, t = tt * 64 + trow;
    if (ct < 7) {
        f32x16 acc0 = {}, acc1 = {};
        const unsigned aoff = (unsigned)(fh * 8192 + lane * 16);
        unsigned boff[4];
#pragma unroll
        for (int ks = 0; ks < 4; ++ks) boff[ks] = (unsigned)(16384 + trow * 128 + (((2 * ks + hi) ^ ((trow >> 1) & 7)) * 16));
        bf16x8 fa0[2], fa1[2], fb[2];
        auto rd = [&](int set, lds_cptr sb, int ks) __attribute__((always_inline)) {
            fb[set] = ldsr128(sb + boff[ks]);
            fa0[set] = ldsr128(sb + aoff + ks * 1024);
            fa1[set] = ldsr128(sb + aoff + 4096 + ks * 1024); };
        auto mm = [&](int set) __attribute__((always_inline)) { };
#define GSTEP(cur, nxt, sbn, ksn) do { SBAR(); acc0 = MFMA32(fa0[cur], fb[cur], acc0); SBAR(); rd(nxt, sbn, ksn); SBAR(); acc1 = MFMA32(fa1[cur], fb[cur], acc1); SBAR(); } while (0)
        const float bias = bflo(bsp[t]);
        u32x2 z[2][4];
#pragma unroll
        for (int i = 0; i < 2; ++i)
#pragma unroll
            for (int q = 0; q < 4; ++q) z[i][q] = *(const u32x2*)((const u16*)(ws + WS_Z1) + ((size_t)((t >> 5) * 28 + ct * 4 + 2 * fh + i) * 1024) + (hi * 32 + r32) * 4 + q * 256);
        LBAR();
        rd(0, (lds_cptr)lds, 0);
        int slot = 0;
        for (int kt = 0; kt < 32; ++kt) {
            const lds_cptr sb = (lds_cptr)lds + slot * G_STAGE;
            GSTEP(0, 1, sb, 1);
            GSTEP(1, 0, sb, 2);
            GSTEP(0, 1, sb, 3);
            slot = slot == 2 ? 0 : slot + 1;
            if (kt < 31) { LBAR(); GSTEP(1, 0, (lds_cptr)lds + slot * G_STAGE, 0); }
            else { acc0 = MFMA32(fa0[1], fb[1], acc0); acc1 = MFMA32(fa1[1], fb[1], acc1); }
        }
        STAMP(1, 2);
#pragma unroll
        for (int i = 0; i < 2; ++i) {
            u16* hp = (u16*)(ws + WS_H) + ((size_t)((t >> 5) * 56 + (ct * 4 + 2 * fh + i) * 2) * 512) + r32 * 8 + hi * 4;
#pragma unroll
            for (int q = 0; q < 4; ++q) {
                float v[4];
#pragma unroll
                for (int r = 0; r < 4; ++r) v[r] = (i ? acc1[4 * q + r] : acc0[4 * q + r]) + bias;
                st8_wt(hp + (q >> 1) * 512 + (q & 1) * 256, (u32x2){cvtpk(v[0] * bflo(z[i][q][0]), v[1] * bfhi(z[i][q][0])), cvtpk(v[2] * bflo(z[i][q][1]), v[3] * bfhi(z[i][q][1]))});
            }
        }
        asm volatile("s_waitcnt vmcnt(0) lgkmcnt(0)\n\ts_barrier" ::: "memory");
        if (tid == 0) __hip_atomic_fetch_add(hcnt, 1u, RLX_AGENT);
    }
    STAMP(1, 3);
    const int kh = fh;
    const int kr = 8 * (g >> 1) + qq;
    unsigned wof[2][2];
#pragma unroll
    for (int n = 0; n < 2; ++n) { wof[n][0] = (unsigned)((64 * kh + kr) * 128 + ((n ^ ((kr >> 1) & 1)) * 64) + (g & 1) * 32 + pp * 8); wof[n][1] = wof[n][0] + 512; }
    auto gemm = [&](const __amdgpu_buffer_rsrc_t tr, auto NST, bool bars, f32x16& c0, f32x16& c1) __attribute__((always_inline)) {
        constexpr int N = decltype(NST)::value;
        u32x4 tf[4][4];
#pragma unroll
        for (int st = 0; st < 4; ++st)
#pragma unroll
            for (int i = 0; i < 4; ++i) tf[st][i] = __builtin_amdgcn_raw_buffer_load_b128(tr, lane * 16 + (st * 8 + 4 * kh + i) * 1024, 0, H_AUX);
        SBAR();
#pragma unroll
        for (int st = 0; st < N; ++st) {
            if (bars && st > 0) LBAR();
            const lds_cptr sb = (lds_cptr)lds + (st & 3) * W_STAGE;
#pragma unroll
            for (int i = 0; i < 4; ++i) {
                const bf16x8 w0 = cat(ldstr(sb + wof[0][0] + i * 2048), ldstr(sb + wof[0][1] + i * 2048)), w1 = cat(ldstr(sb + wof[1][0] + i * 2048), ldstr(sb + wof[1][1] + i * 2048));
                c0 = MFMA32(w0, BF(tf[st & 3][i]), c0); c1 = MFMA32(w1, BF(tf[st & 3][i]), c1);
            }
            SBAR();
            if (st + 4 < N) {
#pragma unroll
                for (int i = 0; i < 4; ++i) tf[st & 3][i] = __builtin_amdgcn_raw_buffer_load_b128(tr, lane * 16 + ((st + 4) * 8 + 4 * kh + i) * 1024, 0, H_AUX);
                SBAR(); }
        }
    };
    auto merge = [&](f32x16& c0, f32x16& c1) __attribute__((always_inline)) {
        f32x4* dst = (f32x4*)(lds + G_RED + (wid ^ 2) * 4096) + lane;
#pragma unroll
        for (int q = 0; q < 4; ++q) dst[q * 64] = kh ? (f32x4){c0[4 * q], c0[4 * q + 1], c0[4 * q + 2], c0[4 * q + 3]} : (f32x4){c1[4 * q], c1[4 * q + 1], c1[4 * q + 2], c1[4 * q + 3]};
    };
    f32x16 gb0 = {}, gb1 = {};
    LBAR();
    gemm(__builtin_amdgcn_make_buffer_rsrc((void*)(ws + WS_H + (size_t)(tt * 2 + th) * 57344), 0, 57344, 0x00020000), std::integral_constant<int, 7>{}, true, gb0, gb1);
    merge(gb0, gb1);
    LBAR();
    { char* sc = lds + G_RED + wid * 4096;
      float gv[16];
#pragma unroll
      for (int q = 0; q < 4; ++q) { const f32x4 o4 = ((const f32x4*)sc)[q * 64 + lane];
#pragma unroll
          for (int r = 0; r < 4; ++r) { const float bb = (kh ? gb1[4 * q + r] : gb0[4 * q + r]) + o4[r]; gv[4 * q + r] = bb * __builtin_amdgcn_rcpf(1.0f + EX(-LOG2E * bb)); } }
      asm volatile("s_waitcnt lgkmcnt(0)" ::: "memory");
#pragma unroll
      for (int u = 0; u < 2; ++u) ((u32x4*)sc)[u * 64 + lane] = (u32x4){cvtpk(gv[8 * u], gv[8 * u + 1]), cvtpk(gv[8 * u + 2], gv[8 * u + 3]), cvtpk(gv[8 * u + 4], gv[8 * u + 5]), cvtpk(gv[8 * u + 6], gv[8 * u + 7])}; }
    const int n0 = ct * 64 + kh * 32 + 4 * hi;
    u32x2 xv[4];
#pragma unroll
    for (int q = 0; q < 4; ++q) xv[q] = *(const u32x2*)(x + (size_t)t * D + n0 + 8 * q);
    PIN(xv[0]); PIN(xv[1]); PIN(xv[2]); PIN(xv[3]);
    STAMP(1, 4);
    LBAR();
    STAMP(1, 5);
    f32x16 s0 = {}, s1 = {};
    {
        const __amdgpu_buffer_rsrc_t tr = __builtin_amdgcn_make_buffer_rsrc((void*)(ws + WS_O + (size_t)(tt * 2 + th) * 32768), 0, 32768, 0x00020000);
        u32x4 tf[16];
#pragma unroll
        for (int i = 0; i < 16; ++i) tf[i] = __builtin_amdgcn_raw_buffer_load_b128(tr, lane * 16 + ((i >> 2) * 8 + 4 * kh + (i & 3)) * 1024, 0, O_AUX);
        SBAR();
        bf16x8 w0[2], w1[2];
        w0[0] = cat(ldstr((lds_cptr)lds + wof[0][0]), ldstr((lds_cptr)lds + wof[0][1])); w1[0] = cat(ldstr((lds_cptr)lds + wof[1][0]), ldstr((lds_cptr)lds + wof[1][1]));
#pragma unroll
        for (int i = 0; i < 16; ++i) {
            if (i + 1 < 16) { const lds_cptr sb = (lds_cptr)lds + ((i + 1) >> 2) * W_STAGE + ((i + 1) & 3) * 2048;
                w0[(i + 1) & 1] = cat(ldstr(sb + wof[0][0]), ldstr(sb + wof[0][1])); w1[(i + 1) & 1] = cat(ldstr(sb + wof[1][0]), ldstr(sb + wof[1][1])); }
            s0 = MFMA32(w0[i & 1], BF(tf[i]), s0); s1 = MFMA32(w1[i & 1], BF(tf[i]), s1);
        }
    }
    STAMP(1, 7);
    u32x4 gq[2];
    { const u32x4* gt = (const u32x4*)(lds + G_RED + wid * 4096) + lane; gq[0] = gt[0]; gq[1] = gt[64]; }
    LBAR();
    { f32x4* dst = (f32x4*)(lds + (wid ^ 2) * 4096) + lane;
#pragma unroll
      for (int q = 0; q < 4; ++q) dst[q * 64] = kh ? (f32x4){s0[4 * q], s0[4 * q + 1], s0[4 * q + 2], s0[4 * q + 3]} : (f32x4){s1[4 * q], s1[4 * q + 1], s1[4 * q + 2], s1[4 * q + 3]}; }
    LBAR();
    { const f32x4* src = (const f32x4*)(lds + wid * 4096) + lane;
#pragma unroll
      for (int q = 0; q < 4; ++q) { const f32x4 o4 = src[q * 64]; float v[4]; const unsigned g0 = gq[q >> 1][2 * (q & 1)], g1 = gq[q >> 1][2 * (q & 1) + 1]; const float gg[4] = {bflo(g0), bfhi(g0), bflo(g1), bfhi(g1)};
#pragma unroll
          for (int r = 0; r < 4; ++r) v[r] = ((kh ? s1[4 * q + r] : s0[4 * q + r]) + o4[r]) * gg[r];
          *(u32x2*)(out + (size_t)t * D + n0 + 8 * q) = (u32x2){cvtpk(bflo(xv[q][0]) + v[0], bfhi(xv[q][0]) + v[1]), cvtpk(bflo(xv[q][1]) + v[2], bfhi(xv[q][1]) + v[3])}; } }
    STAMP(1, 6);
}

__global__ __launch_bounds__(512, 4) void k2_mix(const u16* __restrict__ x, const u16* __restrict__ wo, const u16* __restrict__ wsp, const u16* __restrict__ bsp, const u16* __restrict__ wout, char* __restrict__ ws, u16* __restrict__ out) {
    extern __shared__ __attribute__((aligned(16))) char lds[];
    if (blockIdx.x < 256) attn_role(blockIdx.x, ws, lds);
    if (blockIdx.x >= 256) gate_role(blockIdx.x - 256, x, wo, wsp, bsp, wout, ws, out, lds);
}

extern "C" void kernel_launch(void* const* d_in, const int* in_sizes, int n_in, void* d_out, int out_size, void* d_ws, size_t ws_size, hipStream_t stream) {
    const u16* x = (const u16*)d_in[0]; const u16* wqkv = (const u16*)d_in[1]; const u16* wo = (const u16*)d_in[2];
    const u16* win = (const u16*)d_in[4]; const u16* wsp = (const u16*)d_in[5]; const u16* bsp = (const u16*)d_in[6]; const u16* wout = (const u16*)d_in[7];
    char* ws = (char*)d_ws;
    (void)hipFuncSetAttribute((const void*)k1_proj, hipFuncAttributeMaxDynamicSharedMemorySize, K1_LDS);
    (void)hipFuncSetAttribute((const void*)k2_mix, hipFuncAttributeMaxDynamicSharedMemorySize, K2_LDS);
    k1_proj<<<256, 768, K1_LDS, stream>>>(x, wqkv, win, ws);
    k2_mix<<<512, 512, K2_LDS, stream>>>(x, wo, wsp, bsp, wout, ws, (u16*)d_out);
}
```

```cpp
#include <hip/hip_runtime.h>
#include <hip/hip_bf16.h>
#include <stdint.h>
#include <type_traits>

typedef unsigned short u16;
using bf16x8 = __attribute__((ext_vector_type(8))) short;
using s16x4 = __attribute__((ext_vector_type(4))) short;
using f32x16 = __attribute__((ext_vector_type(16))) float;
using f32x4 = __attribute__((ext_vector_type(4))) float;
using u32x4 = __attribute__((ext_vector_type(4))) unsigned;
using u32x2 = __attribute__((ext_vector_type(2))) unsigned;
typedef __bf16 bf16x2_t __attribute__((ext_vector_type(2)));
typedef float f32x2_t __attribute__((ext_vector_type(2)));
typedef short v4i16_t __attribute__((ext_vector_type(4)));
#define LDSAS __attribute__((address_space(3)))
typedef LDSAS const char* lds_cptr;
#define MFMA32(a, b, c) __builtin_amdgcn_mfma_f32_32x32x16_bf16(a, b, c, 0, 0, 0)
#define MFMA16(a, b, c) __builtin_amdgcn_mfma_f32_16x16x32_bf16(a, b, c, 0, 0, 0)
#define SBAR() __builtin_amdgcn_sched_barrier(0)
#define PIN(x) asm volatile("" : "+v"(x))
#define WAIT_BAR(N) asm volatile("s_waitcnt vmcnt(" #N ") lgkmcnt(0)\n\ts_barrier" ::: "memory")
#define LBAR() asm volatile("s_waitcnt lgkmcnt(0)\n\ts_barrier" ::: "memory")
#define DI __device__ __forceinline__
#define ST8(p, w) st8_wt((void*)(p), (w))
#define ST16(off, v) __builtin_amdgcn_raw_buffer_store_b128((v), wsr, (off), 0, 16)
#define STAMP(kern, i) do { } while (0)

constexpr int S = 2048, D = 512, H = 32, DH = 16, FH = 896;
constexpr float LOG2E = 1.4426950408889634f;

constexpr size_t WS_Q = 0, WS_K = 2u << 20, WS_V = 4u << 20, WS_Z1 = 6u << 20, WS_Z2R = 10u << 20, WS_Z2N = 14u << 20, WS_H = 18u << 20, WS_O = 22u << 20, WS_KMAX = 24u << 20, WS_EPOCH = 25u << 20, WS_XCH = 26u << 20, WS_XST = 27u << 20, WS_CNT = 28u << 20;

DI unsigned cvtpk(float lo, float hi) { f32x2_t v = {lo, hi}; return __builtin_bit_cast(unsigned, __builtin_convertvector(v, bf16x2_t)); }
DI float bflo(unsigned w) { return __uint_as_float(w << 16); }
DI float bfhi(unsigned w) { return __uint_as_float(w & 0xFFFF0000u); }
DI float dot2(unsigned a, unsigned b, float c) { return __builtin_amdgcn_fdot2_f32_bf16(__builtin_bit_cast(bf16x2_t, a), __builtin_bit_cast(bf16x2_t, b), c, false); }
DI void glds16s(unsigned voff, const void* sbase, unsigned lds_base) {
    lds_base = __builtin_amdgcn_readfirstlane(lds_base);
    unsigned sv; asm volatile("s_mov_b32 %0, m0\n\ts_mov_b32 m0, %3\n\ts_nop 0\n\tglobal_load_lds_dwordx4 %1, %2\n\ts_mov_b32 m0, %0" : "=&s"(sv) : "v"(voff), "s"(sbase), "s"(lds_base) : "memory"); }
DI bf16x8 ldsr128(lds_cptr p) { return *(const LDSAS bf16x8*)p; }
DI s16x4 ldstr(lds_cptr p) { return __builtin_bit_cast(s16x4, __builtin_amdgcn_ds_read_tr16_b64_v4i16((LDSAS v4i16_t*)p)); }
DI bf16x8 cat(s16x4 a, s16x4 b) { return __builtin_shufflevector(a, b, 0, 1, 2, 3, 4, 5, 6, 7); }
DI float swap32(float x, bool sum) { auto rr = __builtin_amdgcn_permlane32_swap(__float_as_uint(x), __float_as_uint(x), false, false); return sum ? __uint_as_float(rr[0]) + __uint_as_float(rr[1]) : __uint_as_float(rr[0]); }
DI float gelu_t(float x) {
    const float c1 = -2.0f * 0.7978845608028654f * LOG2E, c3 = c1 * 0.044715f;
    const float u = x * fmaf(x * x, c3, c1);
    return x * __builtin_amdgcn_rcpf(1.0f + __builtin_amdgcn_exp2f(u));
}

constexpr int K1_XB = 16384;
constexpr int K1_STAGE = K1_XB + 7 * 4096;
constexpr int K1_CS = 3 * K1_STAGE;
constexpr int K1_LDS = K1_CS + 1024;
typedef __attribute__((address_space(1))) unsigned gu32; typedef __attribute__((address_space(1))) unsigned long long gu64;
#define RLX_AGENT __ATOMIC_RELAXED, __HIP_MEMORY_SCOPE_AGENT

DI float xor16(float x) { auto r = __builtin_amdgcn_permlane16_swap(__float_as_uint(x), __float_as_uint(x), false, false); return __uint_as_float(r[0]) + __uint_as_float(r[1]); }
DI float xor32(float x) { auto r = __builtin_amdgcn_permlane32_swap(__float_as_uint(x), __float_as_uint(x), false, false); return __uint_as_float(r[0]) + __uint_as_float(r[1]); }
DI float max16(float x) { auto r = __builtin_amdgcn_permlane16_swap(__float_as_uint(x), __float_as_uint(x), false, false); return fmaxf(__uint_as_float(r[0]), __uint_as_float(r[1])); }
DI float max32(float x) { auto r = __builtin_amdgcn_permlane32_swap(__float_as_uint(x), __float_as_uint(x), false, false); return fmaxf(__uint_as_float(r[0]), __uint_as_float(r[1])); }
template <int CTRL> DI float dppf(float x) { return __uint_as_float(__builtin_amdgcn_update_dpp(0, __float_as_uint(x), CTRL, 0xF, 0xF, false)); }
DI float rowmax16(float x) { x = fmaxf(x, dppf<0x128>(x)); x = fmaxf(x, dppf<0x124>(x)); x = fmaxf(x, dppf<0x122>(x)); x = fmaxf(x, dppf<0x121>(x)); return x; }
DI float wavesum(float x) { x += dppf<0x128>(x); x += dppf<0x124>(x); x += dppf<0x122>(x); x += dppf<0x121>(x); return xor32(xor16(x)); }
DI u32x4 widen(u32x2 a, u32x2 b) { auto r0 = __builtin_amdgcn_permlane32_swap(a[0], b[0], false, false); auto r1 = __builtin_amdgcn_permlane32_swap(a[1], b[1], false, false); return (u32x4){r0[0], r1[0], r0[1], r1[1]}; }

DI void st8_wt(void* p, u32x2 w) { __hip_atomic_store((gu64*)p, ((unsigned long long)w[1] << 32) | w[0], RLX_AGENT); }
__global__ __launch_bounds__(768, 3) void k1_proj(const u16* __restrict__ x, const u16* __restrict__ wqkv, const u16* __restrict__ win, char* __restrict__ ws) {
    extern __shared__ __attribute__((aligned(16))) char lds[];
    const int tid = threadIdx.x, lane = tid & 63, r32 = lane & 31, hi = lane >> 5;
    const int wid = __builtin_amdgcn_readfirstlane(tid >> 6);
    const int b = blockIdx.x, xcd = b & 7, idx = b >> 3;
    const int panel = 8 * (xcd >> 2) + (idx & 7), j = 4 * (xcd & 3) + (idx >> 3);
    const unsigned lds0 = (unsigned)(uintptr_t)lds;
    const int jq = j & 3, u7 = 7 * (j >> 2);
    const int n1 = jq == 3 ? 1 : 2, n2 = jq == 1 ? 1 : 2;
    const int z1t0 = u7 + 2 * jq, z2t0 = u7 + (jq == 0 ? 0 : (jq == 1 ? 2 : (jq == 2 ? 3 : 5)));
    STAMP(0, 0);
    if (b == 0 && tid < 40) ((unsigned*)(ws + WS_CNT))[tid * 64] = 0u;
    if (wid >= 8) {
        const int l = wid - 8;
        const char* xb = (const char*)x + (size_t)(panel * 128 + l * 32) * 1024;
        const unsigned xv0 = (unsigned)((lane >> 3) * 1024 + (((lane & 7) ^ (lane >> 4)) * 16)), xv1 = (unsigned)((lane >> 3) * 1024 + (((lane & 7) ^ (4 + (lane >> 4))) * 16));
        const unsigned vq = (unsigned)((16 * l + (lane >> 2)) * 3072 + (lane & 3) * 16), vz = (unsigned)((16 * l + (lane >> 2)) * 3584 + (lane & 3) * 16);
        const char* wq = (const char*)wqkv + j * 64;
        int zc[4];
#pragma unroll
        for (int i = 0; i < 4; ++i) { const int ii = i < n1 + n2 ? i : n1 + n2 - 1; zc[i] = ii < n1 ? (z1t0 + ii) * 64 : FH * 2 + (z2t0 + ii - n1) * 64; }
        auto dma = [&](int st) __attribute__((always_inline)) {
            const unsigned d = lds0 + (st % 3) * K1_STAGE;
#pragma unroll
            for (int p = 0; p < 4; ++p) glds16s((p & 1) ? xv1 : xv0, xb + st * 128 + p * 8192, d + l * 4096 + p * 1024);
            const char* bq = wq + (size_t)st * 64 * 3072; const char* bz = (const char*)win + (size_t)st * 64 * 3584; const unsigned dw = d + K1_XB + l * 1024;
            glds16s(vq, bq + 2048, dw); glds16s(vq, bq, dw + 4096); glds16s(vq, bq + 1024, dw + 8192);
#pragma unroll
            for (int i = 0; i < 4; ++i) glds16s(vz, bz + zc[i], dw + (3 + i) * 4096);
        };
        dma(0);
        WAIT_BAR(0);
        dma(1); dma(2);
#pragma unroll
        for (int st = 1; st < 8; ++st) {
            if (st < 7) WAIT_BAR(11); else WAIT_BAR(0);
            if (st + 2 < 8) dma(st + 2);
        }
        {
            if ((idx & 3) == 0) {
                unsigned long long pc;
                asm volatile("s_getpc_b64 s[98:99]\n\ts_add_u32 s98, s98, _Z6k2_mixPKtS0_S0_S0_S0_PcPt@rel32@lo+4\n\ts_addc_u32 s99, s99, _Z6k2_mixPKtS0_S0_S0_S0_PcPt@rel32@hi+12\n\ts_mov_b64 %0, s[98:99]" : "=s"(pc) :: "scc", "s98", "s99");
                const u32x4* cp = (const u32x4*)(pc + (size_t)l * 4096) + lane;
                u32x4 a = {0u, 0u, 0u, 0u};
#pragma unroll
                for (int i = 0; i < 4; ++i) a ^= cp[i * 64];
                asm volatile("" :: "v"(a));
            }
        }
        return;
    }
    const int mw = wid & 3, nh = wid >> 2;
    gu32* epw = (gu32*)(ws + WS_EPOCH) + panel * 64;
    unsigned tag = __hip_atomic_load(epw, RLX_AGENT);
    u32x4 xr = *(const u32x4*)(x + (size_t)(panel * 128 + 8 * j + wid) * D + lane * 8);
    auto rowstats = [&]() __attribute__((always_inline)) {
        PIN(xr); PIN(tag); tag += 1u;
        float v[8];
#pragma unroll
        for (int i = 0; i < 4; ++i) { v[2 * i] = bflo(xr[i]); v[2 * i + 1] = bfhi(xr[i]); }
        const float m = wavesum(((v[0] + v[1]) + (v[2] + v[3])) + ((v[4] + v[5]) + (v[6] + v[7]))) * (1.0f / 512.0f);
        float q = 0.f;
#pragma unroll
        for (int i = 0; i < 8; ++i) q = fmaf(v[i] - m, v[i] - m, q);
        const float r = __builtin_amdgcn_rsqf(wavesum(q) * (1.0f / 512.0f) + 1e-5f);
        if (lane < 2) __hip_atomic_store((gu64*)(ws + WS_XST) + (size_t)(panel * 128 + 8 * j + wid) * 2 + lane, ((unsigned long long)tag << 32) | __float_as_uint(lane ? r : m), RLX_AGENT);
    };
    f32x16 acc[4];
#pragma unroll
    for (int s = 0; s < 4; ++s) acc[s] = (f32x16){};
    float cs0 = 0.f, cs1 = 0.f;
    const int g = lane >> 4, qq = (lane & 15) >> 2, pp = lane & 3;
    const int trow = 32 * mw + r32;
    unsigned xo[4];
#pragma unroll
    for (int ks = 0; ks < 4; ++ks) xo[ks] = (unsigned)(trow * 128 + (((2 * ks + hi) ^ ((trow >> 1) & 7)) * 16));
    const unsigned wlane = (unsigned)(K1_XB + (8 * (g >> 1) + qq) * 64 + (g & 1) * 32 + pp * 8);
    const __amdgpu_buffer_rsrc_t sr = __builtin_amdgcn_make_buffer_rsrc((void*)(ws + WS_XST + (size_t)panel * 128 * 16), 0, 128 * 16, 0x00020000);
    u32x4 pst;
    int tile[4];
    auto mainloop = [&](auto NHC) __attribute__((always_inline)) {
        constexpr int NHV = decltype(NHC)::value; constexpr int NS = NHV == 0 ? 4 : 3;
        unsigned wo[NS];
#pragma unroll
        for (int s = 0; s < NS; ++s) { int t = s + mw; t = t >= NS ? t - NS : t; t = t >= NS ? t - NS : t; tile[s] = 4 * NHV + t; wo[s] = wlane + tile[s] * 4096; }
        bf16x8 xa[2], wf[2][NS];
        auto rd = [&](const int set, const lds_cptr sb, const int ks) __attribute__((always_inline)) {
            xa[set] = ldsr128(sb + xo[ks]);
#pragma unroll
            for (int s = 0; s < NS; ++s) wf[set][s] = cat(ldstr(sb + wo[s] + ks * 1024), ldstr(sb + wo[s] + ks * 1024 + 256));
        };
        auto mm = [&](const int set) __attribute__((always_inline)) {
            SBAR();
#pragma unroll
            for (int s = 0; s < NS; ++s) acc[s] = MFMA32(wf[set][s], xa[set], acc[s]);
            const u32x4 w = __builtin_bit_cast(u32x4, wf[set][0]);
#pragma unroll
            for (int i = 0; i < 4; ++i) { cs0 += bflo(w[i]); cs1 += bfhi(w[i]); }
            PIN(cs0); PIN(cs1);
#pragma unroll
            for (int i = 0; i < NS; ++i) { __builtin_amdgcn_sched_group_barrier(0x008, 1, 0); __builtin_amdgcn_sched_group_barrier(0x002, NS == 4 ? 4 : 6, 0); }
            SBAR();
        };
        LBAR();
        STAMP(0, 1);
        rd(0, (lds_cptr)lds, 0);
#pragma unroll
        for (int st = 0; st < 8; ++st) {
            const lds_cptr sb = (lds_cptr)lds + (st % 3) * K1_STAGE;
            rd(1, sb, 1); mm(0);
            rd(0, sb, 2); mm(1);
            rd(1, sb, 3); mm(0);
            if (st < 7) { LBAR(); rd(0, (lds_cptr)lds + ((st + 1) % 3) * K1_STAGE, 0); }
            if (st == 1) rowstats();
            if (st == 5) pst = __builtin_amdgcn_raw_buffer_load_b128(sr, trow * 16, 0, 16);
            mm(1);
        }
    };
    if (nh == 0) mainloop(std::integral_constant<int, 0>{}); else mainloop(std::integral_constant<int, 1>{});
    STAMP(0, 2);
    float* csl = (float*)(lds + K1_CS);
    { const float c = xor32(cs0 + cs1); if (hi == 0) csl[tile[0] * 32 + r32] = c; }
    const int tok = panel * 128 + trow;
    float mu, rs;
    {   u32x4 p = pst;
        for (unsigned spins = 0; !__all((p[1] == tag) & (p[3] == tag)) && spins < 200000u; ++spins) {
            __builtin_amdgcn_s_sleep(1);
            asm volatile("" ::: "memory");
            p = __builtin_amdgcn_raw_buffer_load_b128(sr, trow * 16, 0, 16);
        }
        mu = __uint_as_float(p[0]); rs = __uint_as_float(p[2]); }
    LBAR();
    STAMP(0, 3);
    auto lnv = [&](int s, float (&v)[16], float sc) __attribute__((always_inline)) {
#pragma unroll
        for (int q = 0; q < 4; ++q) { const f32x4 c = *(const f32x4*)(csl + tile[s] * 32 + 8 * q + 4 * hi);
#pragma unroll
            for (int r = 0; r < 4; ++r) v[4 * q + r] = sc * (acc[s][4 * q + r] - mu * c[r]); } };
    const __amdgpu_buffer_rsrc_t wsr = __builtin_amdgcn_make_buffer_rsrc((void*)ws, 0, 32u << 20, 0x00020000);
    u16* Z1 = (u16*)(ws + WS_Z1);
    auto z1store = [&](int zt, const u32x2 (&w)[4]) __attribute__((always_inline)) {
        u16* p = Z1 + ((size_t)((tok >> 5) * 28 + zt) * 1024) + (hi * 32 + r32) * 4;
#pragma unroll
        for (int q = 0; q < 4; ++q) ST8(p + q * 256, w[q]); };
    auto gelu16 = [&](int s, u32x2 (&w)[4]) __attribute__((always_inline)) { float v[16]; lnv(s, v, rs);
#pragma unroll
        for (int q = 0; q < 4; ++q) w[q] = (u32x2){cvtpk(gelu_t(v[4 * q]), gelu_t(v[4 * q + 1])), cvtpk(gelu_t(v[4 * q + 2]), gelu_t(v[4 * q + 3]))}; };
    if (nh == 0) {
#pragma unroll
        for (int s = 0; s < 4; ++s) {
            const int T = tile[s];
            if (T == 3) { u32x2 w[4]; gelu16(s, w); z1store(z1t0, w); }
            else {
                float v[16]; lnv(s, v, T == 1 ? rs * (0.25f * LOG2E) : rs);
                u32x2 w[4];
#pragma unroll
                for (int q = 0; q < 4; ++q) w[q] = (u32x2){cvtpk(v[4 * q], v[4 * q + 1]), cvtpk(v[4 * q + 2], v[4 * q + 3])};
                if (T == 0) {
                    char* sc = lds + mw * 2048;
#pragma unroll
                    for (int q = 0; q < 4; ++q) *(u32x2*)(sc + r32 * 64 + (8 * q + 4 * hi) * 2) = w[q];
                    asm volatile("s_waitcnt lgkmcnt(0)" ::: "memory");
                    u16* Vt = (u16*)(ws + WS_V);
#pragma unroll
                    for (int hh = 0; hh < 2; ++hh)
#pragma unroll
                        for (int ks = 0; ks < 2; ++ks) {
                            const s16x4 t4 = ldstr((lds_cptr)lds + mw * 2048 + (16 * ks + 8 * (g >> 1) + 4 * (g & 1) + qq) * 64 + hh * 32 + pp * 8);
                            ST8(Vt + ((size_t)(2 * j + hh) * 128 + ((panel * 128 + 32 * mw) >> 4) + ks) * 256 + (g & 1) * 128 + (lane & 15) * 8 + (g >> 1) * 4, __builtin_bit_cast(u32x2, t4)); }
                } else {
                    const unsigned dsto = (unsigned)(T == 1 ? WS_Q : WS_K);
#pragma unroll
                    for (int hh = 0; hh < 2; ++hh) ST16(dsto + (((2 * j + hh) * S + tok) * 16 + 8 * hi) * 2, widen(w[2 * hh], w[2 * hh + 1]));
                    if (T == 2) {
                        float* kmax = (float*)(ws + WS_KMAX);
#pragma unroll
                        for (int hh = 0; hh < 2; ++hh) { float n = 0.f;
#pragma unroll
                            for (int q = 0; q < 2; ++q) { const u32x2 ww = w[2 * hh + q]; n = fmaf(bflo(ww[0]), bflo(ww[0]), n); n = fmaf(bfhi(ww[0]), bfhi(ww[0]), n); n = fmaf(bflo(ww[1]), bflo(ww[1]), n); n = fmaf(bfhi(ww[1]), bfhi(ww[1]), n); }
                            n = max16(rowmax16(xor32(n)));
                            if (lane == 0) kmax[(panel * 4 + mw) * H + 2 * j + hh] = n; }
                    }
                }
            }
        }
    } else {
        __builtin_amdgcn_s_setprio(2);
        float sm = 0.f;
#pragma unroll
        for (int s = 0; s < 3; ++s) { const int zi = tile[s] - 3;
            if (zi >= n1 && zi < n1 + n2) { float v[16]; lnv(s, v, rs);
#pragma unroll
                for (int r = 0; r < 16; ++r) { acc[s][r] = gelu_t(v[r]); sm += acc[s][r]; } } }
        sm = xor32(sm);
        const float ml = sm * (n2 == 2 ? (1.0f / 64.f) : (1.0f / 32.f)); float m2 = 0.f;
#pragma unroll
        for (int s = 0; s < 3; ++s) { const int zi = tile[s] - 3;
            if (zi >= n1 && zi < n1 + n2) {
#pragma unroll
                for (int r = 0; r < 16; ++r) { const float d = acc[s][r] - ml; m2 = fmaf(d, d, m2); } } }
        m2 = xor32(m2);
        __hip_atomic_store((gu64*)(ws + WS_XCH) + ((size_t)(panel * 16 + j) * 128 + trow) * 2 + hi, ((unsigned long long)tag << 32) | __float_as_uint(hi ? m2 : sm), RLX_AGENT);
        __builtin_amdgcn_s_setprio(0);
        STAMP(0, 4);
#pragma unroll
        for (int s = 0; s < 3; ++s) { const int zi = tile[s] - 3; if (zi < n1) { u32x2 w[4]; gelu16(s, w); z1store(z1t0 + zi, w); } }
        float mean, rstd;
        {
            const __amdgpu_buffer_rsrc_t xr = __builtin_amdgcn_make_buffer_rsrc((void*)(ws + WS_XCH + (size_t)panel * 16 * 128 * 16), 0, 16 * 128 * 16, 0x00020000);
            u32x4 p[8];
            for (unsigned spins = 0;; ++spins) {
                bool ok = true;
                asm volatile("" ::: "memory");
#pragma unroll
                for (int u = 0; u < 8; ++u) p[u] = __builtin_amdgcn_raw_buffer_load_b128(xr, ((8 * hi + u) * 128 + trow) * 16, 0, 16);
#pragma unroll
                for (int u = 0; u < 8; ++u) ok &= (p[u][1] == tag) & (p[u][3] == tag);
                if (__all(ok) || spins > 200000u) break;
                __builtin_amdgcn_s_sleep(6);
            }
            float tot = 0.f;
#pragma unroll
            for (int u = 0; u < 8; ++u) tot += __uint_as_float(p[u][0]);
            mean = xor32(tot) * (1.0f / FH);
            float mm2 = 0.f;
#pragma unroll
            for (int u = 0; u < 8; ++u) { const float nl = (u & 3) == 1 ? 32.f : 64.f; const float d = __uint_as_float(p[u][0]) * ((u & 3) == 1 ? (1.0f / 32.f) : (1.0f / 64.f)) - mean; mm2 += __uint_as_float(p[u][2]) + nl * d * d; }
            rstd = __builtin_amdgcn_rsqf(xor32(mm2) * (1.0f / FH) + 1e-5f);
        }
        STAMP(0, 5);
        if (wid == 4 && lane == 0) __hip_atomic_store(epw, tag, RLX_AGENT);
        const float nmr = -mean * rstd;
#pragma unroll
        for (int s = 0; s < 3; ++s) { const int zi = tile[s] - 3;
            if (zi >= n1 && zi < n1 + n2) {
                const int ft = z2t0 + zi - n1;
                u32x2 w[4];
#pragma unroll
                for (int q = 0; q < 4; ++q) w[q] = (u32x2){cvtpk(fmaf(acc[s][4 * q], rstd, nmr), fmaf(acc[s][4 * q + 1], rstd, nmr)), cvtpk(fmaf(acc[s][4 * q + 2], rstd, nmr), fmaf(acc[s][4 * q + 3], rstd, nmr))};
                char* sc = lds + 8192 + mw * 2048;
#pragma unroll
                for (int q = 0; q < 4; ++q) *(u32x2*)(sc + r32 * 64 + (8 * q + 4 * hi) * 2) = w[q];
                asm volatile("s_waitcnt lgkmcnt(0)" ::: "memory");
                const int tk0 = panel * 128 + 32 * mw;
#pragma unroll
                for (int ks = 0; ks < 2; ++ks) {
                    const lds_cptr rp = (lds_cptr)lds + 8192 + mw * 2048 + (16 * ks + 8 * (g >> 1) + qq) * 64 + (g & 1) * 32 + pp * 8;
                    const bf16x8 fr = cat(ldstr(rp), ldstr(rp + 256));
                    ST16((unsigned)WS_Z2N + (unsigned)(((((ft >> 2) * 32 + (tk0 >> 6)) * 4 + (ft & 3)) * 4 + ((tk0 >> 4) & 3) + ks) * 1024 + lane * 16), __builtin_bit_cast(u32x4, fr));
                }
                asm volatile("s_waitcnt lgkmcnt(0)" ::: "memory");
            } }
    }
    STAMP(0, 6);
}

constexpr int A_KR = 0, A_VR = 32768, A_CONST = 65536, A_FLAG = A_CONST + 4096, A_MX = A_FLAG + 64, K2_LDS = 81920;
constexpr float SAFE_T = 48.f;
#define EX(x) __builtin_amdgcn_exp2f(x)
#define BF(p) __builtin_bit_cast(bf16x8, p)

DI void attn_role(int b, char* __restrict__ ws, char* lds) {
    const u16* Qs = (const u16*)(ws + WS_Q); const u16* Kb = (const u16*)(ws + WS_K); const u16* Vt = (const u16*)(ws + WS_V); const float* kmax = (const float*)(ws + WS_KMAX); u16* Ot = (u16*)(ws + WS_O);
    const int tid = threadIdx.x, lane = tid & 63, r32 = lane & 31, hi = lane >> 5;
    const int wid = __builtin_amdgcn_readfirstlane(tid >> 6);
    const int xcd = b & 7, idx = b >> 3, head = xcd * 4 + (idx & 3), qt = idx >> 2;
    const int qp = wid & 3, kh = wid >> 2;
    const unsigned lds0 = (unsigned)(uintptr_t)lds;
    const char* kg = (const char*)(Kb + (size_t)head * S * DH);
    const char* vg = (const char*)(Vt + (size_t)head * S * DH);
    const unsigned kvoff = wid * 1024 + ((lane ^ ((lane >> 5) & 1)) * 16);
    const unsigned vvoff = wid * 1024 + lane * 16;
#define DMA_CHUNK(c) do { glds16s(kvoff, kg + (c) * 8192, lds0 + A_KR + ((c) & 3) * 8192 + wid * 1024); glds16s(vvoff, vg + (c) * 8192, lds0 + A_VR + ((c) & 3) * 8192 + wid * 1024); } while (0)
    STAMP(1, 0);
    const int q0 = qt * 256 + qp * 64;
    const bf16x8 qa = *(const bf16x8*)(Qs + ((size_t)head * S + q0 + r32) * DH + hi * 8);
    const bf16x8 qb = *(const bf16x8*)(Qs + ((size_t)head * S + q0 + 32 + r32) * DH + hi * 8);
    float km = kmax[lane * H + head];
    DMA_CHUNK(0);
    if (tid < 256) { const unsigned w = (tid & 31) ? 0u : 0x3F803F80u; u32x4 v = {w, w, w, w}; *(u32x4*)(lds + A_CONST + tid * 16) = v; }
    km = max32(max16(rowmax16(km)));
    float qna = 0.f, qnb = 0.f;
#pragma unroll
    for (int j = 0; j < 8; ++j) { const float va = bflo((u16)qa[j]), vb = bflo((u16)qb[j]); qna = fmaf(va, va, qna); qnb = fmaf(vb, vb, qnb); }
    qna = xor32(qna); qnb = xor32(qnb);
    const float mh = __builtin_amdgcn_sqrtf(fmaxf(qna, qnb) * km) * 1.001f;
    { const bool unsafe = __any(!(mh <= SAFE_T)); if (lane == 0) ((unsigned*)(lds + A_FLAG))[wid] = unsafe ? 1u : 0u; }
    const lds_cptr kp0 = (lds_cptr)lds + A_KR + kh * 4096 + r32 * 32 + ((hi ^ (r32 >> 4)) * 16);
    const bool vreal = r32 < 16;
    const unsigned vlane = vreal ? (unsigned)(A_VR + kh * 4096 + hi * 256 + r32 * 16) : (unsigned)(A_CONST + (r32 == 16 ? 0 : ((r32 >= 20 && r32 < 28) ? 64 : 16)));
    const unsigned vmask = vreal ? 0xFFFFFFFFu : 0u;
    f32x16 oa = {}, ob = {};
    WAIT_BAR(0);
    bool slow;
    { const u32x4 f0 = *(const u32x4*)(lds + A_FLAG), f1 = *(const u32x4*)(lds + A_FLAG + 16); slow = __builtin_amdgcn_readfirstlane(f0[0] | f0[1] | f0[2] | f0[3] | f1[0] | f1[1] | f1[2] | f1[3]) != 0; }
    if (!slow) {
        DMA_CHUNK(1); DMA_CHUNK(2); DMA_CHUNK(3);
        f32x16 cinit;
#pragma unroll
        for (int r = 0; r < 16; ++r) cinit[r] = -mh;
        PIN(cinit);
        bf16x8 kf, v0, v1; f32x16 sA, sB; u32x4 p0, p1;
        kf = ldsr128(kp0);
        { lds_cptr vp = (lds_cptr)lds + vlane; v0 = ldsr128(vp); v1 = ldsr128(vp + 512); }
        sA = MFMA32(kf, qa, cinit);
        p1 = (u32x4){0u, 0u, 0u, 0u};
#define HSTEP(SC, SN, QN, OP, OC, PRE1, PRE2, PRE3) do { \
        SN = MFMA32(kf, QN, cinit); PIN(SN); SBAR(); \
        PRE1; \
        SC[0] = EX(SC[0]); SC[1] = EX(SC[1]); SC[2] = EX(SC[2]); SC[3] = EX(SC[3]); SC[4] = EX(SC[4]); SC[5] = EX(SC[5]); PIN(SC); SBAR(); \
        OP = MFMA32(v1, BF(p1), OP); PIN(OP); SBAR(); \
        PRE2; \
        SC[6] = EX(SC[6]); SC[7] = EX(SC[7]); \
        p0[0] = cvtpk(SC[0], SC[1]); p0[1] = cvtpk(SC[2], SC[3]); p0[2] = cvtpk(SC[4], SC[5]); p0[3] = cvtpk(SC[6], SC[7]); PIN(p0); SBAR(); \
        SC[8] = EX(SC[8]); SC[9] = EX(SC[9]); SC[10] = EX(SC[10]); PIN(SC); SBAR(); \
        OC = MFMA32(v0, BF(p0), OC); PIN(OC); SBAR(); \
        PRE3; \
        SC[11] = EX(SC[11]); SC[12] = EX(SC[12]); SC[13] = EX(SC[13]); SC[14] = EX(SC[14]); SC[15] = EX(SC[15]); \
        p1[0] = cvtpk(SC[8], SC[9]); p1[1] = cvtpk(SC[10], SC[11]); p1[2] = cvtpk(SC[12], SC[13]); p1[3] = cvtpk(SC[14], SC[15]); PIN(p1); SBAR(); \
    } while (0)
#define BLOCK(kn, vc, vco, vn, vno) do { \
        HSTEP(sA, sB, qb, ob, oa, kf = ldsr128(kn), v1 = ldsr128((vc) + (vco) + 512), (void)0); \
        HSTEP(sB, sA, qa, oa, ob, (void)0, (void)0, v0 = ldsr128((vn) + (vno))); \
    } while (0)
        STAMP(1, 1);
        for (int c = 0; c < 8; ++c) {
            const unsigned ro = (unsigned)((c & 3) * 8192), rn = (unsigned)(((c + 1) & 3) * 8192);
            const lds_cptr vp = (lds_cptr)lds + (vlane + (ro & vmask)), vpn = (lds_cptr)lds + (vlane + (rn & vmask));
            const lds_cptr kp = kp0 + ro, kpn = kp0 + rn;
            BLOCK(kp + 1024, vp, 0, vp, 1024);
            BLOCK(kp + 2048, vp, 1024, vp, 2048);
            BLOCK(kp + 3072, vp, 2048, vp, 3072);
            if (c == 0) WAIT_BAR(4); else if (c < 6) WAIT_BAR(2); else if (c == 6) WAIT_BAR(0);
            if (c >= 1 && c + 3 < 8) DMA_CHUNK(c + 3);
            BLOCK(kpn, vp, 3072, vpn, 0);
        }
        ob = MFMA32(v1, BF(p1), ob);
#undef HSTEP
#undef BLOCK
    } else {
        float ma = -3.0e38f, mb = -3.0e38f;
        for (int pass = 0; pass < 2; ++pass) {
            for (int c = 0; c < 8; ++c) {
                LBAR();
                DMA_CHUNK(c);
                WAIT_BAR(0);
                const unsigned ro = (unsigned)((c & 3) * 8192);
                const lds_cptr vp = (lds_cptr)lds + (vlane + (ro & vmask)), kp = kp0 + ro;
                for (int j = 0; j < 4; ++j) {
                    const bf16x8 kf = ldsr128(kp + j * 1024);
                    const bf16x8 v0 = ldsr128(vp + j * 1024), v1 = ldsr128(vp + j * 1024 + 512);
#pragma unroll
                    for (int u = 0; u < 2; ++u) {
                        const f32x16 sc = MFMA32(kf, u ? qb : qa, (f32x16){});
                        float& m = u ? mb : ma; f32x16& o = u ? ob : oa;
                        if (pass == 0) {
#pragma unroll
                            for (int r = 0; r < 16; ++r) m = fmaxf(m, sc[r]);
                        } else {
                            u32x4 p0, p1;
#pragma unroll
                            for (int i = 0; i < 4; ++i) { p0[i] = cvtpk(EX(sc[2 * i] - m), EX(sc[2 * i + 1] - m)); p1[i] = cvtpk(EX(sc[8 + 2 * i] - m), EX(sc[9 + 2 * i] - m)); }
                            o = MFMA32(v0, BF(p0), o); o = MFMA32(v1, BF(p1), o);
                        }
                        SBAR();
                    }
                }
            }
            if (pass == 0) {
                ma = max32(ma); mb = max32(mb);
                float* mx = (float*)(lds + A_MX);
                if (hi == 0) { mx[(wid * 2) * 32 + r32] = ma; mx[(wid * 2 + 1) * 32 + r32] = mb; }
                LBAR();
                ma = fmaxf(ma, mx[((wid ^ 4) * 2) * 32 + r32]); mb = fmaxf(mb, mx[((wid ^ 4) * 2 + 1) * 32 + r32]);
            }
        }
    }
    STAMP(1, 2);
    __syncthreads();
    int tz = threadIdx.x; asm volatile("" : "+v"(tz));
    const int lz = tz & 63;
    float* red = (float*)lds;
    { float* dst = red + (size_t)(wid ^ 4) * 9 * 64 + lz;
#pragma unroll
      for (int r = 0; r < 9; ++r) dst[r * 64] = kh ? oa[r] : ob[r]; }
    __syncthreads();
    float o[9];
    { const float* src = red + (size_t)wid * 9 * 64 + lz;
#pragma unroll
      for (int r = 0; r < 9; ++r) o[r] = (kh ? ob[r] : oa[r]) + src[r * 64]; }
    const float rl = __builtin_amdgcn_rcpf(swap32(o[8], false));
    u16* op = Ot + ((size_t)(((q0 >> 5) + kh) * H + head) * 512) + (lz & 31) * 8 + (lz >> 5) * 4;
    st8_wt(op, (u32x2){cvtpk(o[0] * rl, o[1] * rl), cvtpk(o[2] * rl, o[3] * rl)});
    st8_wt(op + 256, (u32x2){cvtpk(o[4] * rl, o[5] * rl), cvtpk(o[6] * rl, o[7] * rl)});
    asm volatile("s_waitcnt vmcnt(0)\n\ts_barrier" ::: "memory");
    if (tz == 0) __hip_atomic_fetch_add((gu32*)(ws + WS_CNT) + (32 + qt) * 64, 1u, RLX_AGENT);
    STAMP(1, 3);
#undef DMA_CHUNK
}

DI void wait_cnt(gu32* cnt, unsigned want) {
    for (unsigned spins = 0; spins < 400000u; ++spins) {
        if ((unsigned)__builtin_amdgcn_readfirstlane(__hip_atomic_load(cnt, RLX_AGENT)) >= want) break;
        __builtin_amdgcn_s_sleep(1);
    }
}

#define O_AUX 16
#define H_AUX 16
constexpr int G_STAGE = 24576;
constexpr int W_STAGE = 16384;
constexpr int G_RED = 4 * W_STAGE;
DI void gate_role(int b, const u16* __restrict__ x, const u16* __restrict__ wo, const u16* __restrict__ wsp, const u16* __restrict__ bsp, const u16* __restrict__ wout, char* __restrict__ ws, u16* __restrict__ out, char* lds) {
    const int xcd = b & 7, idx = b >> 3;
    const int tt = xcd * 4 + (idx & 3), ct = idx >> 2;
    const int tid = threadIdx.x, lane = tid & 63, r32 = lane & 31, hi = lane >> 5;
    const int wid = __builtin_amdgcn_readfirstlane(tid >> 6);
    const unsigned lds0 = (unsigned)(uintptr_t)lds;
    gu32* hcnt = (gu32*)(ws + WS_CNT) + tt * 64; gu32* ocnt = (gu32*)(ws + WS_CNT) + (32 + (tt >> 2)) * 64;
    STAMP(1, 0);
    if (wid >= 4) {
        const int l = wid - 4;
        if (ct < 7) {
            const char* ag = ws + WS_Z2N + (size_t)ct * 32 * 16384 + l * 4096;
            const char* bg = (const char*)wsp + (size_t)(tt * 64 + l * 16) * 4096;
            const unsigned avoff = lane * 16;
            const unsigned bv0 = (unsigned)((lane >> 3) * 4096 + (((lane & 7) ^ (lane >> 4)) * 16)), bv1 = (unsigned)((8 + (lane >> 3)) * 4096 + (((lane & 7) ^ (4 + (lane >> 4))) * 16));
            auto dma = [&](int kt, int slot) __attribute__((always_inline)) { const unsigned d = lds0 + slot * G_STAGE; const char* a_ = ag + (size_t)kt * 16384;
#pragma unroll
                for (int e = 0; e < 4; ++e) glds16s(avoff, a_ + e * 1024, d + l * 4096 + e * 1024);
                glds16s(bv0, bg + kt * 128, d + 16384 + l * 2048); glds16s(bv1, bg + kt * 128, d + 16384 + l * 2048 + 1024); };
            dma(0, 0);
            WAIT_BAR(0);
            dma(1, 1); dma(2, 2);
            int slot = 0;
            for (int kt = 1; kt < 32; ++kt) {
                if (kt < 31) WAIT_BAR(6); else WAIT_BAR(0);
                if (kt + 2 < 32) dma(kt + 2, slot);
                slot = slot == 2 ? 0 : slot + 1;
            }
            LBAR();
        }
        unsigned wv[4];
#pragma unroll
        for (int e = 0; e < 4; ++e) { const int row = l * 32 + e * 8 + (lane >> 3); wv[e] = (unsigned)(row * 1024 + ct * 128 + (((lane & 7) ^ (((row >> 1) & 1) << 2)) * 16)); }
        auto dmw = [&](const u16* w, int st) __attribute__((always_inline)) { const char* wg = (const char*)w + (size_t)st * 128 * 1024; const unsigned d = lds0 + (st & 3) * W_STAGE + l * 4096;
#pragma unroll
            for (int e = 0; e < 4; ++e) glds16s(wv[e], wg, d + e * 1024); };
        dmw(wout, 0); dmw(wout, 1); dmw(wout, 2); dmw(wout, 3);
        if (l == 0) { wait_cnt(hcnt, 7);
        }
        WAIT_BAR(12);
#pragma unroll
        for (int st = 1; st < 7; ++st) {
            if (st < 4) WAIT_BAR(8); else if (st == 4) WAIT_BAR(8); else if (st == 5) WAIT_BAR(4); else WAIT_BAR(0);
            if (st + 3 < 7) dmw(wout, st + 3);
        }
        LBAR();
        dmw(wo, 0); dmw(wo, 1); dmw(wo, 2); dmw(wo, 3);
        if (l == 0) { wait_cnt(ocnt, 32);
        }
        WAIT_BAR(0);
        return;
    }
#define GPRIO 2
    __builtin_amdgcn_s_setprio(GPRIO);
    const int g = lane >> 4, qq = (lane & 15) >> 2, pp = lane & 3;
    const int th = wid & 1, fh = wid >> 1;
    const int trow = 32 * th + r32, t = tt * 64 + trow;
    if (ct < 7) {
        f32x16 acc0 = {}, acc1 = {};
        const unsigned aoff = (unsigned)(fh * 8192 + lane * 16);
        unsigned boff[4];
#pragma unroll
        for (int ks = 0; ks < 4; ++ks) boff[ks] = (unsigned)(16384 + trow * 128 + (((2 * ks + hi) ^ ((trow >> 1) & 7)) * 16));
        bf16x8 fa0[2], fa1[2], fb[2];
        auto rd = [&](int set, lds_cptr sb, int ks) __attribute__((always_inline)) {
            fb[set] = ldsr128(sb + boff[ks]);
            fa0[set] = ldsr128(sb + aoff + ks * 1024);
            fa1[set] = ldsr128(sb + aoff + 4096 + ks * 1024); };
        auto mm = [&](int set) __attribute__((always_inline)) { };
#define GSTEP(cur, nxt, sbn, ksn) do { SBAR(); acc0 = MFMA32(fa0[cur], fb[cur], acc0); SBAR(); rd(nxt, sbn, ksn); SBAR(); acc1 = MFMA32(fa1[cur], fb[cur], acc1); SBAR(); } while (0)
        const float bias = bflo(bsp[t]);
        u32x2 z[2][4];
#pragma unroll
        for (int i = 0; i < 2; ++i)
#pragma unroll
            for (int q = 0; q < 4; ++q) z[i][q] = *(const u32x2*)((const u16*)(ws + WS_Z1) + ((size_t)((t >> 5) * 28 + ct * 4 + 2 * fh + i) * 1024) + (hi * 32 + r32) * 4 + q * 256);
        LBAR();
        rd(0, (lds_cptr)lds, 0);
        int slot = 0;
        for (int kt = 0; kt < 32; ++kt) {
            const lds_cptr sb = (lds_cptr)lds + slot * G_STAGE;
            GSTEP(0, 1, sb, 1);
            GSTEP(1, 0, sb, 2);
            GSTEP(0, 1, sb, 3);
            slot = slot == 2 ? 0 : slot + 1;
            if (kt < 31) { LBAR(); GSTEP(1, 0, (lds_cptr)lds + slot * G_STAGE, 0); }
            else { acc0 = MFMA32(fa0[1], fb[1], acc0); acc1 = MFMA32(fa1[1], fb[1], acc1); }
        }
        STAMP(1, 2);
#pragma unroll
        for (int i = 0; i < 2; ++i) {
            u16* hp = (u16*)(ws + WS_H) + ((size_t)((t >> 5) * 56 + (ct * 4 + 2 * fh + i) * 2) * 512) + r32 * 8 + hi * 4;
#pragma unroll
            for (int q = 0; q < 4; ++q) {
                float v[4];
#pragma unroll
                for (int r = 0; r < 4; ++r) v[r] = (i ? acc1[4 * q + r] : acc0[4 * q + r]) + bias;
                st8_wt(hp + (q >> 1) * 512 + (q & 1) * 256, (u32x2){cvtpk(v[0] * bflo(z[i][q][0]), v[1] * bfhi(z[i][q][0])), cvtpk(v[2] * bflo(z[i][q][1]), v[3] * bfhi(z[i][q][1]))});
            }
        }
        asm volatile("s_waitcnt vmcnt(0) lgkmcnt(0)\n\ts_barrier" ::: "memory");
        if (tid == 0) __hip_atomic_fetch_add(hcnt, 1u, RLX_AGENT);
    }
    STAMP(1, 3);
    const int kh = fh;
    const int kr = 8 * (g >> 1) + qq;
    unsigned wof[2][2];
#pragma unroll
    for (int n = 0; n < 2; ++n) { wof[n][0] = (unsigned)((64 * kh + kr) * 128 + ((n ^ ((kr >> 1) & 1)) * 64) + (g & 1) * 32 + pp * 8); wof[n][1] = wof[n][0] + 512; }
    auto gemm = [&](const __amdgpu_buffer_rsrc_t tr, auto NST, bool bars, f32x16& c0, f32x16& c1) __attribute__((always_inline)) {
        constexpr int N = decltype(NST)::value;
        u32x4 tf[4][4];
#pragma unroll
        for (int st = 0; st < 4; ++st)
#pragma unroll
            for (int i = 0; i < 4; ++i) tf[st][i] = __builtin_amdgcn_raw_buffer_load_b128(tr, lane * 16 + (st * 8 + 4 * kh + i) * 1024, 0, H_AUX);
        SBAR();
#pragma unroll
        for (int st = 0; st < N; ++st) {
            if (bars && st > 0) LBAR();
            const lds_cptr sb = (lds_cptr)lds + (st & 3) * W_STAGE;
#pragma unroll
            for (int i = 0; i < 4; ++i) {
                const bf16x8 w0 = cat(ldstr(sb + wof[0][0] + i * 2048), ldstr(sb + wof[0][1] + i * 2048)), w1 = cat(ldstr(sb + wof[1][0] + i * 2048), ldstr(sb + wof[1][1] + i * 2048));
                c0 = MFMA32(w0, BF(tf[st & 3][i]), c0); c1 = MFMA32(w1, BF(tf[st & 3][i]), c1);
            }
            SBAR();
            if (st + 4 < N) {
#pragma unroll
                for (int i = 0; i < 4; ++i) tf[st & 3][i] = __builtin_amdgcn_raw_buffer_load_b128(tr, lane * 16 + ((st + 4) * 8 + 4 * kh + i) * 1024, 0, H_AUX);
                SBAR(); }
        }
    };
    auto merge = [&](f32x16& c0, f32x16& c1) __attribute__((always_inline)) {
        f32x4* dst = (f32x4*)(lds + G_RED + (wid ^ 2) * 4096) + lane;
#pragma unroll
        for (int q = 0; q < 4; ++q) dst[q * 64] = kh ? (f32x4){c0[4 * q], c0[4 * q + 1], c0[4 * q + 2], c0[4 * q + 3]} : (f32x4){c1[4 * q], c1[4 * q + 1], c1[4 * q + 2], c1[4 * q + 3]};
    };
    f32x16 gb0 = {}, gb1 = {};
    LBAR();
    gemm(__builtin_amdgcn_make_buffer_rsrc((void*)(ws + WS_H + (size_t)(tt * 2 + th) * 57344), 0, 57344, 0x00020000), std::integral_constant<int, 7>{}, true, gb0, gb1);
    merge(gb0, gb1);
    LBAR();
    { char* sc = lds + G_RED + wid * 4096;
      float gv[16];
#pragma unroll
      for (int q = 0; q < 4; ++q) { const f32x4 o4 = ((const f32x4*)sc)[q * 64 + lane];
#pragma unroll
          for (int r = 0; r < 4; ++r) { const float bb = (kh ? gb1[4 * q + r] : gb0[4 * q + r]) + o4[r]; gv[4 * q + r] = bb * __builtin_amdgcn_rcpf(1.0f + EX(-LOG2E * bb)); } }
      asm volatile("s_waitcnt lgkmcnt(0)" ::: "memory");
#pragma unroll
      for (int u = 0; u < 2; ++u) ((u32x4*)sc)[u * 64 + lane] = (u32x4){cvtpk(gv[8 * u], gv[8 * u + 1]), cvtpk(gv[8 * u + 2], gv[8 * u + 3]), cvtpk(gv[8 * u + 4], gv[8 * u + 5]), cvtpk(gv[8 * u + 6], gv[8 * u + 7])}; }
    const int n0 = ct * 64 + kh * 32 + 4 * hi;
    u32x2 xv[4];
#pragma unroll
    for (int q = 0; q < 4; ++q) xv[q] = *(const u32x2*)(x + (size_t)t * D + n0 + 8 * q);
    PIN(xv[0]); PIN(xv[1]); PIN(xv[2]); PIN(xv[3]);
    STAMP(1, 4);
    LBAR();
    STAMP(1, 5);
    f32x16 s0 = {}, s1 = {};
    {
        const __amdgpu_buffer_rsrc_t tr = __builtin_amdgcn_make_buffer_rsrc((void*)(ws + WS_O + (size_t)(tt * 2 + th) * 32768), 0, 32768, 0x00020000);
        u32x4 tf[16];
#pragma unroll
        for (int i = 0; i < 16; ++i) tf[i] = __builtin_amdgcn_raw_buffer_load_b128(tr, lane * 16 + ((i >> 2) * 8 + 4 * kh + (i & 3)) * 1024, 0, O_AUX);
        SBAR();
        bf16x8 w0[2], w1[2];
        w0[0] = cat(ldstr((lds_cptr)lds + wof[0][0]), ldstr((lds_cptr)lds + wof[0][1])); w1[0] = cat(ldstr((lds_cptr)lds + wof[1][0]), ldstr((lds_cptr)lds + wof[1][1]));
#pragma unroll
        for (int i = 0; i < 16; ++i) {
            if (i + 1 < 16) { const lds_cptr sb = (lds_cptr)lds + ((i + 1) >> 2) * W_STAGE + ((i + 1) & 3) * 2048;
                w0[(i + 1) & 1] = cat(ldstr(sb + wof[0][0]), ldstr(sb + wof[0][1])); w1[(i + 1) & 1] = cat(ldstr(sb + wof[1][0]), ldstr(sb + wof[1][1])); }
            s0 = MFMA32(w0[i & 1], BF(tf[i]), s0); s1 = MFMA32(w1[i & 1], BF(tf[i]), s1);
        }
    }
    STAMP(1, 7);
    u32x4 gq[2];
    { const u32x4* gt = (const u32x4*)(lds + G_RED + wid * 4096) + lane; gq[0] = gt[0]; gq[1] = gt[64]; }
    LBAR();
    { f32x4* dst = (f32x4*)(lds + (wid ^ 2) * 4096) + lane;
#pragma unroll
      for (int q = 0; q < 4; ++q) dst[q * 64] = kh ? (f32x4){s0[4 * q], s0[4 * q + 1], s0[4 * q + 2], s0[4 * q + 3]} : (f32x4){s1[4 * q], s1[4 * q + 1], s1[4 * q + 2], s1[4 * q + 3]}; }
    LBAR();
    { const f32x4* src = (const f32x4*)(lds + wid * 4096) + lane;
#pragma unroll
      for (int q = 0; q < 4; ++q) { const f32x4 o4 = src[q * 64]; float v[4]; const unsigned g0 = gq[q >> 1][2 * (q & 1)], g1 = gq[q >> 1][2 * (q & 1) + 1]; const float gg[4] = {bflo(g0), bfhi(g0), bflo(g1), bfhi(g1)};
#pragma unroll
          for (int r = 0; r < 4; ++r) v[r] = ((kh ? s1[4 * q + r] : s0[4 * q + r]) + o4[r]) * gg[r];
          *(u32x2*)(out + (size_t)t * D + n0 + 8 * q) = (u32x2){cvtpk(bflo(xv[q][0]) + v[0], bfhi(xv[q][0]) + v[1]), cvtpk(bflo(xv[q][1]) + v[2], bfhi(xv[q][1]) + v[3])}; } }
    STAMP(1, 6);
}

__global__ __launch_bounds__(512, 4) void k2_mix(const u16* __restrict__ x, const u16* __restrict__ wo, const u16* __restrict__ wsp, const u16* __restrict__ bsp, const u16* __restrict__ wout, char* __restrict__ ws, u16* __restrict__ out) {
    extern __shared__ __attribute__((aligned(16))) char lds[];
    if (blockIdx.x < 256) attn_role(blockIdx.x, ws, lds);
    if (blockIdx.x >= 256) gate_role(blockIdx.x - 256, x, wo, wsp, bsp, wout, ws, out, lds);
}

extern "C" void kernel_launch(void* const* d_in, const int* in_sizes, int n_in, void* d_out, int out_size, void* d_ws, size_t ws_size, hipStream_t stream) {
    const u16* x = (const u16*)d_in[0]; const u16* wqkv = (const u16*)d_in[1]; const u16* wo = (const u16*)d_in[2];
    const u16* win = (const u16*)d_in[4]; const u16* wsp = (const u16*)d_in[5]; const u16* bsp = (const u16*)d_in[6]; const u16* wout = (const u16*)d_in[7];
    char* ws = (char*)d_ws;
    (void)hipFuncSetAttribute((const void*)k1_proj, hipFuncAttributeMaxDynamicSharedMemorySize, K1_LDS);
    (void)hipFuncSetAttribute((const void*)k2_mix, hipFuncAttributeMaxDynamicSharedMemorySize, K2_LDS);
    k1_proj<<<256, 768, K1_LDS, stream>>>(x, wqkv, win, ws);
    k2_mix<<<512, 512, K2_LDS, stream>>>(x, wo, wsp, bsp, wout, ws, (u16*)d_out);
}
```

```cpp
#include <hip/hip_runtime.h>
#include <hip/hip_bf16.h>
#include <stdint.h>
#include <type_traits>

typedef unsigned short u16;
using bf16x8 = __attribute__((ext_vector_type(8))) short;
using s16x4 = __attribute__((ext_vector_type(4))) short;
using f32x16 = __attribute__((ext_vector_type(16))) float;
using f32x4 = __attribute__((ext_vector_type(4))) float;
using u32x4 = __attribute__((ext_vector_type(4))) unsigned;
using u32x2 = __attribute__((ext_vector_type(2))) unsigned;
typedef __bf16 bf16x2_t __attribute__((ext_vector_type(2)));
typedef float f32x2_t __attribute__((ext_vector_type(2)));
typedef short v4i16_t __attribute__((ext_vector_type(4)));
#define LDSAS __attribute__((address_space(3)))
typedef LDSAS const char* lds_cptr;
#define MFMA32(a, b, c) __builtin_amdgcn_mfma_f32_32x32x16_bf16(a, b, c, 0, 0, 0)
#define MFMA16(a, b, c) __builtin_amdgcn_mfma_f32_16x16x32_bf16(a, b, c, 0, 0, 0)
#define SBAR() __builtin_amdgcn_sched_barrier(0)
#define PIN(x) asm volatile("" : "+v"(x))
#define WAIT_BAR(N) asm volatile("s_waitcnt vmcnt(" #N ") lgkmcnt(0)\n\ts_barrier" ::: "memory")
#define LBAR() asm volatile("s_waitcnt lgkmcnt(0)\n\ts_barrier" ::: "memory")
#define DI __device__ __forceinline__
#define ST8(p, w) st8_wt((void*)(p), (w))
#define ST16(off, v) __builtin_amdgcn_raw_buffer_store_b128((v), wsr, (off), 0, 16)
#define STAMP(kern, i) do { } while (0)

constexpr int S = 2048, D = 512, H = 32, DH = 16, FH = 896;
constexpr float LOG2E = 1.4426950408889634f;

constexpr size_t WS_Q = 0, WS_K = 2u << 20, WS_V = 4u << 20, WS_Z1 = 6u << 20, WS_Z2R = 10u << 20, WS_Z2N = 14u << 20, WS_H = 18u << 20, WS_O = 22u << 20, WS_KMAX = 24u << 20, WS_EPOCH = 25u << 20, WS_XCH = 26u << 20, WS_XST = 27u << 20, WS_CNT = 28u << 20;

DI unsigned cvtpk(float lo, float hi) { f32x2_t v = {lo, hi}; return __builtin_bit_cast(unsigned, __builtin_convertvector(v, bf16x2_t)); }
DI float bflo(unsigned w) { return __uint_as_float(w << 16); }
DI float bfhi(unsigned w) { return __uint_as_float(w & 0xFFFF0000u); }
DI float dot2(unsigned a, unsigned b, float c) { return __builtin_amdgcn_fdot2_f32_bf16(__builtin_bit_cast(bf16x2_t, a), __builtin_bit_cast(bf16x2_t, b), c, false); }
DI void glds16s(unsigned voff, const void* sbase, unsigned lds_base) {
    lds_base = __builtin_amdgcn_readfirstlane(lds_base);
    unsigned sv; asm volatile("s_mov_b32 %0, m0\n\ts_mov_b32 m0, %3\n\ts_nop 0\n\tglobal_load_lds_dwordx4 %1, %2\n\ts_mov_b32 m0, %0" : "=&s"(sv) : "v"(voff), "s"(sbase), "s"(lds_base) : "memory"); }
DI bf16x8 ldsr128(lds_cptr p) { return *(const LDSAS bf16x8*)p; }
DI s16x4 ldstr(lds_cptr p) { return __builtin_bit_cast(s16x4, __builtin_amdgcn_ds_read_tr16_b64_v4i16((LDSAS v4i16_t*)p)); }
DI bf16x8 cat(s16x4 a, s16x4 b) { return __builtin_shufflevector(a, b, 0, 1, 2, 3, 4, 5, 6, 7); }
DI float swap32(float x, bool sum) { auto rr = __builtin_amdgcn_permlane32_swap(__float_as_uint(x), __float_as_uint(x), false, false); return sum ? __uint_as_float(rr[0]) + __uint_as_float(rr[1]) : __uint_as_float(rr[0]); }
DI float gelu_t(float x) {
    const float c1 = -2.0f * 0.7978845608028654f * LOG2E, c3 = c1 * 0.044715f;
    const float u = x * fmaf(x * x, c3, c1);
    return x * __builtin_amdgcn_rcpf(1.0f + __builtin_amdgcn_exp2f(u));
}

constexpr int K1_XB = 16384;
constexpr int K1_STAGE = K1_XB + 7 * 4096;
constexpr int K1_CS = 3 * K1_STAGE;
constexpr int K1_LDS = K1_CS + 1024;
typedef __attribute__((address_space(1))) unsigned gu32; typedef __attribute__((address_space(1))) unsigned long long gu64;
#define RLX_AGENT __ATOMIC_RELAXED, __HIP_MEMORY_SCOPE_AGENT

DI float xor16(float x) { auto r = __builtin_amdgcn_permlane16_swap(__float_as_uint(x), __float_as_uint(x), false, false); return __uint_as_float(r[0]) + __uint_as_float(r[1]); }
DI float xor32(float x) { auto r = __builtin_amdgcn_permlane32_swap(__float_as_uint(x), __float_as_uint(x), false, false); return __uint_as_float(r[0]) + __uint_as_float(r[1]); }
DI float max16(float x) { auto r = __builtin_amdgcn_permlane16_swap(__float_as_uint(x), __float_as_uint(x), false, false); return fmaxf(__uint_as_float(r[0]), __uint_as_float(r[1])); }
DI float max32(float x) { auto r = __builtin_amdgcn_permlane32_swap(__float_as_uint(x), __float_as_uint(x), false, false); return fmaxf(__uint_as_float(r[0]), __uint_as_float(r[1])); }
template <int CTRL> DI float dppf(float x) { return __uint_as_float(__builtin_amdgcn_update_dpp(0, __float_as_uint(x), CTRL, 0xF, 0xF, false)); }
DI float rowmax16(float x) { x = fmaxf(x, dppf<0x128>(x)); x = fmaxf(x, dppf<0x124>(x)); x = fmaxf(x, dppf<0x122>(x)); x = fmaxf(x, dppf<0x121>(x)); return x; }
DI float wavesum(float x) { x += dppf<0x128>(x); x += dppf<0x124>(x); x += dppf<0x122>(x); x += dppf<0x121>(x); return xor32(xor16(x)); }
DI u32x4 widen(u32x2 a, u32x2 b) { auto r0 = __builtin_amdgcn_permlane32_swap(a[0], b[0], false, false); auto r1 = __builtin_amdgcn_permlane32_swap(a[1], b[1], false, false); return (u32x4){r0[0], r1[0], r0[1], r1[1]}; }

DI void gather8(u32x4 (&p)[8], unsigned voff, const void* sbase) {
    asm volatile("global_load_dwordx4 %0, %8, %9 sc1\n\tglobal_load_dwordx4 %1, %8, %9 offset:2048 sc1\n\tglobal_load_dwordx4 %2, %10, %9 sc1\n\tglobal_load_dwordx4 %3, %10, %9 offset:2048 sc1\n\t"
                 "global_load_dwordx4 %4, %11, %9 sc1\n\tglobal_load_dwordx4 %5, %11, %9 offset:2048 sc1\n\tglobal_load_dwordx4 %6, %12, %9 sc1\n\tglobal_load_dwordx4 %7, %12, %9 offset:2048 sc1\n\ts_waitcnt vmcnt(0)"
                 : "=&v"(p[0]), "=&v"(p[1]), "=&v"(p[2]), "=&v"(p[3]), "=&v"(p[4]), "=&v"(p[5]), "=&v"(p[6]), "=&v"(p[7])
                 : "v"(voff), "s"(sbase), "v"(voff + 4096u), "v"(voff + 8192u), "v"(voff + 12288u) : "memory"); }
DI void st8_wt(void* p, u32x2 w) { __hip_atomic_store((gu64*)p, ((unsigned long long)w[1] << 32) | w[0], RLX_AGENT); }
__global__ __launch_bounds__(768, 3) void k1_proj(const u16* __restrict__ x, const u16* __restrict__ wqkv, const u16* __restrict__ win, char* __restrict__ ws) {
    extern __shared__ __attribute__((aligned(16))) char lds[];
    const int tid = threadIdx.x, lane = tid & 63, r32 = lane & 31, hi = lane >> 5;
    const int wid = __builtin_amdgcn_readfirstlane(tid >> 6);
    const int b = blockIdx.x, xcd = b & 7, idx = b >> 3;
    const int panel = 8 * (xcd >> 2) + (idx & 7), j = 4 * (xcd & 3) + (idx >> 3);
    const unsigned lds0 = (unsigned)(uintptr_t)lds;
    const int jq = j & 3, u7 = 7 * (j >> 2);
    const int n1 = jq == 3 ? 1 : 2, n2 = jq == 1 ? 1 : 2;
    const int z1t0 = u7 + 2 * jq, z2t0 = u7 + (jq == 0 ? 0 : (jq == 1 ? 2 : (jq == 2 ? 3 : 5)));
    STAMP(0, 0);
    if (b == 0 && tid < 40) ((unsigned*)(ws + WS_CNT))[tid * 64] = 0u;
    if (wid >= 8) {
        const int l = wid - 8;
        const char* xb = (const char*)x + (size_t)(panel * 128 + l * 32) * 1024;
        const unsigned xv0 = (unsigned)((lane >> 3) * 1024 + (((lane & 7) ^ (lane >> 4)) * 16)), xv1 = (unsigned)((lane >> 3) * 1024 + (((lane & 7) ^ (4 + (lane >> 4))) * 16));
        const unsigned vq = (unsigned)((16 * l + (lane >> 2)) * 3072 + (lane & 3) * 16), vz = (unsigned)((16 * l + (lane >> 2)) * 3584 + (lane & 3) * 16);
        const char* wq = (const char*)wqkv + j * 64;
        int zc[4];
#pragma unroll
        for (int i = 0; i < 4; ++i) { const int ii = i < n1 + n2 ? i : n1 + n2 - 1; zc[i] = ii < n1 ? (z1t0 + ii) * 64 : FH * 2 + (z2t0 + ii - n1) * 64; }
        auto dma = [&](int st) __attribute__((always_inline)) {
            const unsigned d = lds0 + (st % 3) * K1_STAGE;
#pragma unroll
            for (int p = 0; p < 4; ++p) glds16s((p & 1) ? xv1 : xv0, xb + st * 128 + p * 8192, d + l * 4096 + p * 1024);
            const char* bq = wq + (size_t)st * 64 * 3072; const char* bz = (const char*)win + (size_t)st * 64 * 3584; const unsigned dw = d + K1_XB + l * 1024;
            glds16s(vq, bq + 2048, dw); glds16s(vq, bq, dw + 4096); glds16s(vq, bq + 1024, dw + 8192);
#pragma unroll
            for (int i = 0; i < 4; ++i) glds16s(vz, bz + zc[i], dw + (3 + i) * 4096);
        };
        dma(0);
        WAIT_BAR(0);
        dma(1); dma(2);
#pragma unroll
        for (int st = 1; st < 8; ++st) {
            if (st < 7) WAIT_BAR(11); else WAIT_BAR(0);
            if (st + 2 < 8) dma(st + 2);
        }
        {
            if ((idx & 3) == 0) {
                unsigned long long pc;
                asm volatile("s_getpc_b64 s[98:99]\n\ts_add_u32 s98, s98, _Z6k2_mixPKtS0_S0_S0_S0_PcPt@rel32@lo+4\n\ts_addc_u32 s99, s99, _Z6k2_mixPKtS0_S0_S0_S0_PcPt@rel32@hi+12\n\ts_mov_b64 %0, s[98:99]" : "=s"(pc) :: "scc", "s98", "s99");
                const u32x4* cp = (const u32x4*)(pc + (size_t)l * 4096) + lane;
                u32x4 a = {0u, 0u, 0u, 0u};
#pragma unroll
                for (int i = 0; i < 4; ++i) a ^= cp[i * 64];
                asm volatile("" :: "v"(a));
            }
        }
        return;
    }
    const int mw = wid & 3, nh = wid >> 2;
    gu32* epw = (gu32*)(ws + WS_EPOCH) + panel * 64;
    unsigned tag = __hip_atomic_load(epw, RLX_AGENT);
    u32x4 xr = *(const u32x4*)(x + (size_t)(panel * 128 + 8 * j + wid) * D + lane * 8);
    auto rowstats = [&]() __attribute__((always_inline)) {
        PIN(xr); PIN(tag); tag += 1u;
        float v[8];
#pragma unroll
        for (int i = 0; i < 4; ++i) { v[2 * i] = bflo(xr[i]); v[2 * i + 1] = bfhi(xr[i]); }
        const float m = wavesum(((v[0] + v[1]) + (v[2] + v[3])) + ((v[4] + v[5]) + (v[6] + v[7]))) * (1.0f / 512.0f);
        float q = 0.f;
#pragma unroll
        for (int i = 0; i < 8; ++i) q = fmaf(v[i] - m, v[i] - m, q);
        const float r = __builtin_amdgcn_rsqf(wavesum(q) * (1.0f / 512.0f) + 1e-5f);
        if (lane < 2) __hip_atomic_store((gu64*)(ws + WS_XST) + (size_t)(panel * 128 + 8 * j + wid) * 2 + lane, ((unsigned long long)tag << 32) | __float_as_uint(lane ? r : m), RLX_AGENT);
    };
    f32x16 acc[4];
#pragma unroll
    for (int s = 0; s < 4; ++s) acc[s] = (f32x16){};
    float cs0 = 0.f, cs1 = 0.f;
    const int g = lane >> 4, qq = (lane & 15) >> 2, pp = lane & 3;
    const int trow = 32 * mw + r32;
    unsigned xo[4];
#pragma unroll
    for (int ks = 0; ks < 4; ++ks) xo[ks] = (unsigned)(trow * 128 + (((2 * ks + hi) ^ ((trow >> 1) & 7)) * 16));
    const unsigned wlane = (unsigned)(K1_XB + (8 * (g >> 1) + qq) * 64 + (g & 1) * 32 + pp * 8);
    const __amdgpu_buffer_rsrc_t sr = __builtin_amdgcn_make_buffer_rsrc((void*)(ws + WS_XST + (size_t)panel * 128 * 16), 0, 128 * 16, 0x00020000);
    u32x4 pst;
    int tile[4];
    auto mainloop = [&](auto NHC) __attribute__((always_inline)) {
        constexpr int NHV = decltype(NHC)::value; constexpr int NS = NHV == 0 ? 4 : 3;
        unsigned wo[NS];
#pragma unroll
        for (int s = 0; s < NS; ++s) { int t = s + mw; t = t >= NS ? t - NS : t; t = t >= NS ? t - NS : t; tile[s] = 4 * NHV + t; wo[s] = wlane + tile[s] * 4096; }
        bf16x8 xa[2], wf[2][NS];
        auto rd = [&](const int set, const lds_cptr sb, const int ks) __attribute__((always_inline)) {
            xa[set] = ldsr128(sb + xo[ks]);
#pragma unroll
            for (int s = 0; s < NS; ++s) wf[set][s] = cat(ldstr(sb + wo[s] + ks * 1024), ldstr(sb + wo[s] + ks * 1024 + 256));
        };
        auto mm = [&](const int set) __attribute__((always_inline)) {
            SBAR();
#pragma unroll
            for (int s = 0; s < NS; ++s) acc[s] = MFMA32(wf[set][s], xa[set], acc[s]);
            const u32x4 w = __builtin_bit_cast(u32x4, wf[set][0]);
#pragma unroll
            for (int i = 0; i < 4; ++i) { cs0 += bflo(w[i]); cs1 += bfhi(w[i]); }
            PIN(cs0); PIN(cs1);
#pragma unroll
            for (int i = 0; i < NS; ++i) { __builtin_amdgcn_sched_group_barrier(0x008, 1, 0); __builtin_amdgcn_sched_group_barrier(0x002, NS == 4 ? 4 : 6, 0); }
            SBAR();
        };
        LBAR();
        STAMP(0, 1);
        rd(0, (lds_cptr)lds, 0);
#pragma unroll
        for (int st = 0; st < 8; ++st) {
            const lds_cptr sb = (lds_cptr)lds + (st % 3) * K1_STAGE;
            rd(1, sb, 1); mm(0);
            rd(0, sb, 2); mm(1);
            rd(1, sb, 3); mm(0);
            if (st < 7) { LBAR(); rd(0, (lds_cptr)lds + ((st + 1) % 3) * K1_STAGE, 0); }
            if (st == 1) rowstats();
            if (st == 5) pst = __builtin_amdgcn_raw_buffer_load_b128(sr, trow * 16, 0, 16);
            mm(1);
        }
    };
    if (nh == 0) mainloop(std::integral_constant<int, 0>{}); else mainloop(std::integral_constant<int, 1>{});
    STAMP(0, 2);
    float* csl = (float*)(lds + K1_CS);
    { const float c = xor32(cs0 + cs1); if (hi == 0) csl[tile[0] * 32 + r32] = c; }
    const int tok = panel * 128 + trow;
    float mu, rs;
    {   unsigned long long g0 = ((unsigned long long)pst[1] << 32) | pst[0], g1 = ((unsigned long long)pst[3] << 32) | pst[2];
        gu64* gp = (gu64*)(ws + WS_XST) + (size_t)(panel * 128 + trow) * 2;
        for (unsigned spins = 0; !__all(((unsigned)(g0 >> 32) == tag) & ((unsigned)(g1 >> 32) == tag)) && spins < 200000u; ++spins) {
            __builtin_amdgcn_s_sleep(1);
            g0 = __hip_atomic_load(gp, RLX_AGENT); g1 = __hip_atomic_load(gp + 1, RLX_AGENT);
        }
        mu = __uint_as_float((unsigned)g0); rs = __uint_as_float((unsigned)g1); }
    LBAR();
    STAMP(0, 3);
    auto lnv = [&](int s, float (&v)[16], float sc) __attribute__((always_inline)) {
#pragma unroll
        for (int q = 0; q < 4; ++q) { const f32x4 c = *(const f32x4*)(csl + tile[s] * 32 + 8 * q + 4 * hi);
#pragma unroll
            for (int r = 0; r < 4; ++r) v[4 * q + r] = sc * (acc[s][4 * q + r] - mu * c[r]); } };
    const __amdgpu_buffer_rsrc_t wsr = __builtin_amdgcn_make_buffer_rsrc((void*)ws, 0, 32u << 20, 0x00020000);
    u16* Z1 = (u16*)(ws + WS_Z1);
    auto z1store = [&](int zt, const u32x2 (&w)[4]) __attribute__((always_inline)) {
        u16* p = Z1 + ((size_t)((tok >> 5) * 28 + zt) * 1024) + (hi * 32 + r32) * 4;
#pragma unroll
        for (int q = 0; q < 4; ++q) ST8(p + q * 256, w[q]); };
    auto gelu16 = [&](int s, u32x2 (&w)[4]) __attribute__((always_inline)) { float v[16]; lnv(s, v, rs);
#pragma unroll
        for (int q = 0; q < 4; ++q) w[q] = (u32x2){cvtpk(gelu_t(v[4 * q]), gelu_t(v[4 * q + 1])), cvtpk(gelu_t(v[4 * q + 2]), gelu_t(v[4 * q + 3]))}; };
    if (nh == 0) {
#pragma unroll
        for (int s = 0; s < 4; ++s) {
            const int T = tile[s];
            if (T == 3) { u32x2 w[4]; gelu16(s, w); z1store(z1t0, w); }
            else {
                float v[16]; lnv(s, v, T == 1 ? rs * (0.25f * LOG2E) : rs);
                u32x2 w[4];
#pragma unroll
                for (int q = 0; q < 4; ++q) w[q] = (u32x2){cvtpk(v[4 * q], v[4 * q + 1]), cvtpk(v[4 * q + 2], v[4 * q + 3])};
                if (T == 0) {
                    char* sc = lds + mw * 2048;
#pragma unroll
                    for (int q = 0; q < 4; ++q) *(u32x2*)(sc + r32 * 64 + (8 * q + 4 * hi) * 2) = w[q];
                    asm volatile("s_waitcnt lgkmcnt(0)" ::: "memory");
                    u16* Vt = (u16*)(ws + WS_V);
#pragma unroll
                    for (int hh = 0; hh < 2; ++hh)
#pragma unroll
                        for (int ks = 0; ks < 2; ++ks) {
                            const s16x4 t4 = ldstr((lds_cptr)lds + mw * 2048 + (16 * ks + 8 * (g >> 1) + 4 * (g & 1) + qq) * 64 + hh * 32 + pp * 8);
                            ST8(Vt + ((size_t)(2 * j + hh) * 128 + ((panel * 128 + 32 * mw) >> 4) + ks) * 256 + (g & 1) * 128 + (lane & 15) * 8 + (g >> 1) * 4, __builtin_bit_cast(u32x2, t4)); }
                } else {
                    const unsigned dsto = (unsigned)(T == 1 ? WS_Q : WS_K);
#pragma unroll
                    for (int hh = 0; hh < 2; ++hh) ST16(dsto + (((2 * j + hh) * S + tok) * 16 + 8 * hi) * 2, widen(w[2 * hh], w[2 * hh + 1]));
                    if (T == 2) {
                        float* kmax = (float*)(ws + WS_KMAX);
#pragma unroll
                        for (int hh = 0; hh < 2; ++hh) { float n = 0.f;
#pragma unroll
                            for (int q = 0; q < 2; ++q) { const u32x2 ww = w[2 * hh + q]; n = fmaf(bflo(ww[0]), bflo(ww[0]), n); n = fmaf(bfhi(ww[0]), bfhi(ww[0]), n); n = fmaf(bflo(ww[1]), bflo(ww[1]), n); n = fmaf(bfhi(ww[1]), bfhi(ww[1]), n); }
                            n = max16(rowmax16(xor32(n)));
                            if (lane == 0) kmax[(panel * 4 + mw) * H + 2 * j + hh] = n; }
                    }
                }
            }
        }
    } else {
        __builtin_amdgcn_s_setprio(2);
        float sm = 0.f;
#pragma unroll
        for (int s = 0; s < 3; ++s) { const int zi = tile[s] - 3;
            if (zi >= n1 && zi < n1 + n2) { float v[16]; lnv(s, v, rs);
#pragma unroll
                for (int r = 0; r < 16; ++r) { acc[s][r] = gelu_t(v[r]); sm += acc[s][r]; } } }
        sm = xor32(sm);
        const float ml = sm * (n2 == 2 ? (1.0f / 64.f) : (1.0f / 32.f)); float m2 = 0.f;
#pragma unroll
        for (int s = 0; s < 3; ++s) { const int zi = tile[s] - 3;
            if (zi >= n1 && zi < n1 + n2) {
#pragma unroll
                for (int r = 0; r < 16; ++r) { const float d = acc[s][r] - ml; m2 = fmaf(d, d, m2); } } }
        m2 = xor32(m2);
        __hip_atomic_store((gu64*)(ws + WS_XCH) + ((size_t)(panel * 16 + j) * 128 + trow) * 2 + hi, ((unsigned long long)tag << 32) | __float_as_uint(hi ? m2 : sm), RLX_AGENT);
        __builtin_amdgcn_s_setprio(0);
        STAMP(0, 4);
#pragma unroll
        for (int s = 0; s < 3; ++s) { const int zi = tile[s] - 3; if (zi < n1) { u32x2 w[4]; gelu16(s, w); z1store(z1t0 + zi, w); } }
        float mean, rstd;
        {
            const char* xb = ws + WS_XCH + (size_t)panel * 16 * 128 * 16; const unsigned xo = (unsigned)((8 * hi * 128 + trow) * 16);
            u32x4 p[8];
            for (unsigned spins = 0;; ++spins) {
                bool ok = true;
                gather8(p, xo, xb);
#pragma unroll
                for (int u = 0; u < 8; ++u) ok &= (p[u][1] == tag) & (p[u][3] == tag);
                if (__all(ok) || spins > 200000u) break;
                __builtin_amdgcn_s_sleep(6);
            }
            float tot = 0.f;
#pragma unroll
            for (int u = 0; u < 8; ++u) tot += __uint_as_float(p[u][0]);
            mean = xor32(tot) * (1.0f / FH);
            float mm2 = 0.f;
#pragma unroll
            for (int u = 0; u < 8; ++u) { const float nl = (u & 3) == 1 ? 32.f : 64.f; const float d = __uint_as_float(p[u][0]) * ((u & 3) == 1 ? (1.0f / 32.f) : (1.0f / 64.f)) - mean; mm2 += __uint_as_float(p[u][2]) + nl * d * d; }
            rstd = __builtin_amdgcn_rsqf(xor32(mm2) * (1.0f / FH) + 1e-5f);
        }
        STAMP(0, 5);
        if (wid == 4 && lane == 0) __hip_atomic_store(epw, tag, RLX_AGENT);
        const float nmr = -mean * rstd;
#pragma unroll
        for (int s = 0; s < 3; ++s) { const int zi = tile[s] - 3;
            if (zi >= n1 && zi < n1 + n2) {
                const int ft = z2t0 + zi - n1;
                u32x2 w[4];
#pragma unroll
                for (int q = 0; q < 4; ++q) w[q] = (u32x2){cvtpk(fmaf(acc[s][4 * q], rstd, nmr), fmaf(acc[s][4 * q + 1], rstd, nmr)), cvtpk(fmaf(acc[s][4 * q + 2], rstd, nmr), fmaf(acc[s][4 * q + 3], rstd, nmr))};
                char* sc = lds + 8192 + mw * 2048;
#pragma unroll
                for (int q = 0; q < 4; ++q) *(u32x2*)(sc + r32 * 64 + (8 * q + 4 * hi) * 2) = w[q];
                asm volatile("s_waitcnt lgkmcnt(0)" ::: "memory");
                const int tk0 = panel * 128 + 32 * mw;
#pragma unroll
                for (int ks = 0; ks < 2; ++ks) {
                    const lds_cptr rp = (lds_cptr)lds + 8192 + mw * 2048 + (16 * ks + 8 * (g >> 1) + qq) * 64 + (g & 1) * 32 + pp * 8;
                    const bf16x8 fr = cat(ldstr(rp), ldstr(rp + 256));
                    ST16((unsigned)WS_Z2N + (unsigned)(((((ft >> 2) * 32 + (tk0 >> 6)) * 4 + (ft & 3)) * 4 + ((tk0 >> 4) & 3) + ks) * 1024 + lane * 16), __builtin_bit_cast(u32x4, fr));
                }
                asm volatile("s_waitcnt lgkmcnt(0)" ::: "memory");
            } }
    }
    STAMP(0, 6);
}

constexpr int A_KR = 0, A_VR = 32768, A_CONST = 65536, A_FLAG = A_CONST + 4096, A_MX = A_FLAG + 64, K2_LDS = 81920;
constexpr float SAFE_T = 48.f;
#define EX(x) __builtin_amdgcn_exp2f(x)
#define BF(p) __builtin_bit_cast(bf16x8, p)

DI void attn_role(int b, char* __restrict__ ws, char* lds) {
    const u16* Qs = (const u16*)(ws + WS_Q); const u16* Kb = (const u16*)(ws + WS_K); const u16* Vt = (const u16*)(ws + WS_V); const float* kmax = (const float*)(ws + WS_KMAX); u16* Ot = (u16*)(ws + WS_O);
    const int tid = threadIdx.x, lane = tid & 63, r32 = lane & 31, hi = lane >> 5;
    const int wid = __builtin_amdgcn_readfirstlane(tid >> 6);
    const int xcd = b & 7, idx = b >> 3, head = xcd * 4 + (idx & 3), qt = idx >> 2;
    const int qp = wid & 3, kh = wid >> 2;
    const unsigned lds0 = (unsigned)(uintptr_t)lds;
    const char* kg = (const char*)(Kb + (size_t)head * S * DH);
    const char* vg = (const char*)(Vt + (size_t)head * S * DH);
    const unsigned kvoff = wid * 1024 + ((lane ^ ((lane >> 5) & 1)) * 16);
    const unsigned vvoff = wid * 1024 + lane * 16;
#define DMA_CHUNK(c) do { glds16s(kvoff, kg + (c) * 8192, lds0 + A_KR + ((c) & 3) * 8192 + wid * 1024); glds16s(vvoff, vg + (c) * 8192, lds0 + A_VR + ((c) & 3) * 8192 + wid * 1024); } while (0)
    STAMP(1, 0);
    const int q0 = qt * 256 + qp * 64;
    const bf16x8 qa = *(const bf16x8*)(Qs + ((size_t)head * S + q0 + r32) * DH + hi * 8);
    const bf16x8 qb = *(const bf16x8*)(Qs + ((size_t)head * S + q0 + 32 + r32) * DH + hi * 8);
    float km = kmax[lane * H + head];
    DMA_CHUNK(0);
    if (tid < 256) { const unsigned w = (tid & 31) ? 0u : 0x3F803F80u; u32x4 v = {w, w, w, w}; *(u32x4*)(lds + A_CONST + tid * 16) = v; }
    km = max32(max16(rowmax16(km)));
    float qna = 0.f, qnb = 0.f;
#pragma unroll
    for (int j = 0; j < 8; ++j) { const float va = bflo((u16)qa[j]), vb = bflo((u16)qb[j]); qna = fmaf(va, va, qna); qnb = fmaf(vb, vb, qnb); }
    qna = xor32(qna); qnb = xor32(qnb);
    const float mh = __builtin_amdgcn_sqrtf(fmaxf(qna, qnb) * km) * 1.001f;
    { const bool unsafe = __any(!(mh <= SAFE_T)); if (lane == 0) ((unsigned*)(lds + A_FLAG))[wid] = unsafe ? 1u : 0u; }
    const lds_cptr kp0 = (lds_cptr)lds + A_KR + kh * 4096 + r32 * 32 + ((hi ^ (r32 >> 4)) * 16);
    const bool vreal = r32 < 16;
    const unsigned vlane = vreal ? (unsigned)(A_VR + kh * 4096 + hi * 256 + r32 * 16) : (unsigned)(A_CONST + (r32 == 16 ? 0 : ((r32 >= 20 && r32 < 28) ? 64 : 16)));
    const unsigned vmask = vreal ? 0xFFFFFFFFu : 0u;
    f32x16 oa = {}, ob = {};
    WAIT_BAR(0);
    bool slow;
    { const u32x4 f0 = *(const u32x4*)(lds + A_FLAG), f1 = *(const u32x4*)(lds + A_FLAG + 16); slow = __builtin_amdgcn_readfirstlane(f0[0] | f0[1] | f0[2] | f0[3] | f1[0] | f1[1] | f1[2] | f1[3]) != 0; }
    if (!slow) {
        DMA_CHUNK(1); DMA_CHUNK(2); DMA_CHUNK(3);
        f32x16 cinit;
#pragma unroll
        for (int r = 0; r < 16; ++r) cinit[r] = -mh;
        PIN(cinit);
        bf16x8 kf, v0, v1; f32x16 sA, sB; u32x4 p0, p1;
        kf = ldsr128(kp0);
        { lds_cptr vp = (lds_cptr)lds + vlane; v0 = ldsr128(vp); v1 = ldsr128(vp + 512); }
        sA = MFMA32(kf, qa, cinit);
        p1 = (u32x4){0u, 0u, 0u, 0u};
#define HSTEP(SC, SN, QN, OP, OC, PRE1, PRE2, PRE3) do { \
        SN = MFMA32(kf, QN, cinit); PIN(SN); SBAR(); \
        PRE1; \
        SC[0] = EX(SC[0]); SC[1] = EX(SC[1]); SC[2] = EX(SC[2]); SC[3] = EX(SC[3]); SC[4] = EX(SC[4]); SC[5] = EX(SC[5]); PIN(SC); SBAR(); \
        OP = MFMA32(v1, BF(p1), OP); PIN(OP); SBAR(); \
        PRE2; \
        SC[6] = EX(SC[6]); SC[7] = EX(SC[7]); \
        p0[0] = cvtpk(SC[0], SC[1]); p0[1] = cvtpk(SC[2], SC[3]); p0[2] = cvtpk(SC[4], SC[5]); p0[3] = cvtpk(SC[6], SC[7]); PIN(p0); SBAR(); \
        SC[8] = EX(SC[8]); SC[9] = EX(SC[9]); SC[10] = EX(SC[10]); PIN(SC); SBAR(); \
        OC = MFMA32(v0, BF(p0), OC); PIN(OC); SBAR(); \
        PRE3; \
        SC[11] = EX(SC[11]); SC[12] = EX(SC[12]); SC[13] = EX(SC[13]); SC[14] = EX(SC[14]); SC[15] = EX(SC[15]); \
        p1[0] = cvtpk(SC[8], SC[9]); p1[1] = cvtpk(SC[10], SC[11]); p1[2] = cvtpk(SC[12], SC[13]); p1[3] = cvtpk(SC[14], SC[15]); PIN(p1); SBAR(); \
    } while (0)
#define BLOCK(kn, vc, vco, vn, vno) do { \
        HSTEP(sA, sB, qb, ob, oa, kf = ldsr128(kn), v1 = ldsr128((vc) + (vco) + 512), (void)0); \
        HSTEP(sB, sA, qa, oa, ob, (void)0, (void)0, v0 = ldsr128((vn) + (vno))); \
    } while (0)
        STAMP(1, 1);
        for (int c = 0; c < 8; ++c) {
            const unsigned ro = (unsigned)((c & 3) * 8192), rn = (unsigned)(((c + 1) & 3) * 8192);
            const lds_cptr vp = (lds_cptr)lds + (vlane + (ro & vmask)), vpn = (lds_cptr)lds + (vlane + (rn & vmask));
            const lds_cptr kp = kp0 + ro, kpn = kp0 + rn;
            BLOCK(kp + 1024, vp, 0, vp, 1024);
            BLOCK(kp + 2048, vp, 1024, vp, 2048);
            BLOCK(kp + 3072, vp, 2048, vp, 3072);
            if (c == 0) WAIT_BAR(4); else if (c < 6) WAIT_BAR(2); else if (c == 6) WAIT_BAR(0);
            if (c >= 1 && c + 3 < 8) DMA_CHUNK(c + 3);
            BLOCK(kpn, vp, 3072, vpn, 0);
        }
        ob = MFMA32(v1, BF(p1), ob);
#undef HSTEP
#undef BLOCK
    } else {
        float ma = -3.0e38f, mb = -3.0e38f;
        for (int pass = 0; pass < 2; ++pass) {
            for (int c = 0; c < 8; ++c) {
                LBAR();
                DMA_CHUNK(c);
                WAIT_BAR(0);
                const unsigned ro = (unsigned)((c & 3) * 8192);
                const lds_cptr vp = (lds_cptr)lds + (vlane + (ro & vmask)), kp = kp0 + ro;
                for (int j = 0; j < 4; ++j) {
                    const bf16x8 kf = ldsr128(kp + j * 1024);
                    const bf16x8 v0 = ldsr128(vp + j * 1024), v1 = ldsr128(vp + j * 1024 + 512);
#pragma unroll
                    for (int u = 0; u < 2; ++u) {
                        const f32x16 sc = MFMA32(kf, u ? qb : qa, (f32x16){});
                        float& m = u ? mb : ma; f32x16& o = u ? ob : oa;
                        if (pass == 0) {
#pragma unroll
                            for (int r = 0; r < 16; ++r) m = fmaxf(m, sc[r]);
                        } else {
                            u32x4 p0, p1;
#pragma unroll
                            for (int i = 0; i < 4; ++i) { p0[i] = cvtpk(EX(sc[2 * i] - m), EX(sc[2 * i + 1] - m)); p1[i] = cvtpk(EX(sc[8 + 2 * i] - m), EX(sc[9 + 2 * i] - m)); }
                            o = MFMA32(v0, BF(p0), o); o = MFMA32(v1, BF(p1), o);
                        }
                        SBAR();
                    }
                }
            }
            if (pass == 0) {
                ma = max32(ma); mb = max32(mb);
                float* mx = (float*)(lds + A_MX);
                if (hi == 0) { mx[(wid * 2) * 32 + r32] = ma; mx[(wid * 2 + 1) * 32 + r32] = mb; }
                LBAR();
                ma = fmaxf(ma, mx[((wid ^ 4) * 2) * 32 + r32]); mb = fmaxf(mb, mx[((wid ^ 4) * 2 + 1) * 32 + r32]);
            }
        }
    }
    STAMP(1, 2);
    __syncthreads();
    int tz = threadIdx.x; asm volatile("" : "+v"(tz));
    const int lz = tz & 63;
    float* red = (float*)lds;
    { float* dst = red + (size_t)(wid ^ 4) * 9 * 64 + lz;
#pragma unroll
      for (int r = 0; r < 9; ++r) dst[r * 64] = kh ? oa[r] : ob[r]; }
    __syncthreads();
    float o[9];
    { const float* src = red + (size_t)wid * 9 * 64 + lz;
#pragma unroll
      for (int r = 0; r < 9; ++r) o[r] = (kh ? ob[r] : oa[r]) + src[r * 64]; }
    const float rl = __builtin_amdgcn_rcpf(swap32(o[8], false));
    u16* op = Ot + ((size_t)(((q0 >> 5) + kh) * H + head) * 512) + (lz & 31) * 8 + (lz >> 5) * 4;
    st8_wt(op, (u32x2){cvtpk(o[0] * rl, o[1] * rl), cvtpk(o[2] * rl, o[3] * rl)});
    st8_wt(op + 256, (u32x2){cvtpk(o[4] * rl, o[5] * rl), cvtpk(o[6] * rl, o[7] * rl)});
    asm volatile("s_waitcnt vmcnt(0)\n\ts_barrier" ::: "memory");
    if (tz == 0) __hip_atomic_fetch_add((gu32*)(ws + WS_CNT) + (32 + qt) * 64, 1u, RLX_AGENT);
    STAMP(1, 3);
#undef DMA_CHUNK
}

DI void wait_cnt(gu32* cnt, unsigned want) {
    for (unsigned spins = 0; spins < 400000u; ++spins) {
        if ((unsigned)__builtin_amdgcn_readfirstlane(__hip_atomic_load(cnt, RLX_AGENT)) >= want) break;
        __builtin_amdgcn_s_sleep(1);
    }
}

#define O_AUX 16
#define H_AUX 16
constexpr int G_STAGE = 24576;
constexpr int W_STAGE = 16384;
constexpr int G_RED = 4 * W_STAGE;
DI void gate_role(int b, const u16* __restrict__ x, const u16* __restrict__ wo, const u16* __restrict__ wsp, const u16* __restrict__ bsp, const u16* __restrict__ wout, char* __restrict__ ws, u16* __restrict__ out, char* lds) {
    const int xcd = b & 7, idx = b >> 3;
    const int tt = xcd * 4 + (idx & 3), ct = idx >> 2;
    const int tid = threadIdx.x, lane = tid & 63, r32 = lane & 31, hi = lane >> 5;
    const int wid = __builtin_amdgcn_readfirstlane(tid >> 6);
    const unsigned lds0 = (unsigned)(uintptr_t)lds;
    gu32* hcnt = (gu32*)(ws + WS_CNT) + tt * 64; gu32* ocnt = (gu32*)(ws + WS_CNT) + (32 + (tt >> 2)) * 64;
    STAMP(1, 0);
    if (wid >= 4) {
        const int l = wid - 4;
        if (ct < 7) {
            const char* ag = ws + WS_Z2N + (size_t)ct * 32 * 16384 + l * 4096;
            const char* bg = (const char*)wsp + (size_t)(tt * 64 + l * 16) * 4096;
            const unsigned avoff = lane * 16;
            const unsigned bv0 = (unsigned)((lane >> 3) * 4096 + (((lane & 7) ^ (lane >> 4)) * 16)), bv1 = (unsigned)((8 + (lane >> 3)) * 4096 + (((lane & 7) ^ (4 + (lane >> 4))) * 16));
            auto dma = [&](int kt, int slot) __attribute__((always_inline)) { const unsigned d = lds0 + slot * G_STAGE; const char* a_ = ag + (size_t)kt * 16384;
#pragma unroll
                for (int e = 0; e < 4; ++e) glds16s(avoff, a_ + e * 1024, d + l * 4096 + e * 1024);
                glds16s(bv0, bg + kt * 128, d + 16384 + l * 2048); glds16s(bv1, bg + kt * 128, d + 16384 + l * 2048 + 1024); };
            dma(0, 0);
            WAIT_BAR(0);
            dma(1, 1); dma(2, 2);
            int slot = 0;
            for (int kt = 1; kt < 32; ++kt) {
                if (kt < 31) WAIT_BAR(6); else WAIT_BAR(0);
                if (kt + 2 < 32) dma(kt + 2, slot);
                slot = slot == 2 ? 0 : slot + 1;
            }
            LBAR();
        }
        unsigned wv[4];
#pragma unroll
        for (int e = 0; e < 4; ++e) { const int row = l * 32 + e * 8 + (lane >> 3); wv[e] = (unsigned)(row * 1024 + ct * 128 + (((lane & 7) ^ (((row >> 1) & 1) << 2)) * 16)); }
        auto dmw = [&](const u16* w, int st) __attribute__((always_inline)) { const char* wg = (const char*)w + (size_t)st * 128 * 1024; const unsigned d = lds0 + (st & 3) * W_STAGE + l * 4096;
#pragma unroll
            for (int e = 0; e < 4; ++e) glds16s(wv[e], wg, d + e * 1024); };
        dmw(wout, 0); dmw(wout, 1); dmw(wout, 2); dmw(wout, 3);
        if (l == 0) { wait_cnt(hcnt, 7);
        }
        WAIT_BAR(12);
#pragma unroll
        for (int st = 1; st < 7; ++st) {
            if (st < 4) WAIT_BAR(8); else if (st == 4) WAIT_BAR(8); else if (st == 5) WAIT_BAR(4); else WAIT_BAR(0);
            if (st + 3 < 7) dmw(wout, st + 3);
        }
        LBAR();
        dmw(wo, 0); dmw(wo, 1); dmw(wo, 2); dmw(wo, 3);
        if (l == 0) { wait_cnt(ocnt, 32);
        }
        WAIT_BAR(0);
        return;
    }
#define GPRIO 2
    __builtin_amdgcn_s_setprio(GPRIO);
    const int g = lane >> 4, qq = (lane & 15) >> 2, pp = lane & 3;
    const int th = wid & 1, fh = wid >> 1;
    const int trow = 32 * th + r32, t = tt * 64 + trow;
    if (ct < 7) {
        f32x16 acc0 = {}, acc1 = {};
        const unsigned aoff = (unsigned)(fh * 8192 + lane * 16);
        unsigned boff[4];
#pragma unroll
        for (int ks = 0; ks < 4; ++ks) boff[ks] = (unsigned)(16384 + trow * 128 + (((2 * ks + hi) ^ ((trow >> 1) & 7)) * 16));
        bf16x8 fa0[2], fa1[2], fb[2];
        auto rd = [&](int set, lds_cptr sb, int ks) __attribute__((always_inline)) {
            fb[set] = ldsr128(sb + boff[ks]);
            fa0[set] = ldsr128(sb + aoff + ks * 1024);
            fa1[set] = ldsr128(sb + aoff + 4096 + ks * 1024); };
        auto mm = [&](int set) __attribute__((always_inline)) { };
#define GSTEP(cur, nxt, sbn, ksn) do { SBAR(); acc0 = MFMA32(fa0[cur], fb[cur], acc0); SBAR(); rd(nxt, sbn, ksn); SBAR(); acc1 = MFMA32(fa1[cur], fb[cur], acc1); SBAR(); } while (0)
        const float bias = bflo(bsp[t]);
        u32x2 z[2][4];
#pragma unroll
        for (int i = 0; i < 2; ++i)
#pragma unroll
            for (int q = 0; q < 4; ++q) z[i][q] = *(const u32x2*)((const u16*)(ws + WS_Z1) + ((size_t)((t >> 5) * 28 + ct * 4 + 2 * fh + i) * 1024) + (hi * 32 + r32) * 4 + q * 256);
        LBAR();
        rd(0, (lds_cptr)lds, 0);
        int slot = 0;
        for (int kt = 0; kt < 32; ++kt) {
            const lds_cptr sb = (lds_cptr)lds + slot * G_STAGE;
            GSTEP(0, 1, sb, 1);
            GSTEP(1, 0, sb, 2);
            GSTEP(0, 1, sb, 3);
            slot = slot == 2 ? 0 : slot + 1;
            if (kt < 31) { LBAR(); GSTEP(1, 0, (lds_cptr)lds + slot * G_STAGE, 0); }
            else { acc0 = MFMA32(fa0[1], fb[1], acc0); acc1 = MFMA32(fa1[1], fb[1], acc1); }
        }
        STAMP(1, 2);
#pragma unroll
        for (int i = 0; i < 2; ++i) {
            u16* hp = (u16*)(ws + WS_H) + ((size_t)((t >> 5) * 56 + (ct * 4 + 2 * fh + i) * 2) * 512) + r32 * 8 + hi * 4;
#pragma unroll
            for (int q = 0; q < 4; ++q) {
                float v[4];
#pragma unroll
                for (int r = 0; r < 4; ++r) v[r] = (i ? acc1[4 * q + r] : acc0[4 * q + r]) + bias;
                st8_wt(hp + (q >> 1) * 512 + (q & 1) * 256, (u32x2){cvtpk(v[0] * bflo(z[i][q][0]), v[1] * bfhi(z[i][q][0])), cvtpk(v[2] * bflo(z[i][q][1]), v[3] * bfhi(z[i][q][1]))});
            }
        }
        asm volatile("s_waitcnt vmcnt(0) lgkmcnt(0)\n\ts_barrier" ::: "memory");
        if (tid == 0) __hip_atomic_fetch_add(hcnt, 1u, RLX_AGENT);
    }
    STAMP(1, 3);
    const int kh = fh;
    const int kr = 8 * (g >> 1) + qq;
    unsigned wof[2][2];
#pragma unroll
    for (int n = 0; n < 2; ++n) { wof[n][0] = (unsigned)((64 * kh + kr) * 128 + ((n ^ ((kr >> 1) & 1)) * 64) + (g & 1) * 32 + pp * 8); wof[n][1] = wof[n][0] + 512; }
    auto gemm = [&](const __amdgpu_buffer_rsrc_t tr, auto NST, bool bars, f32x16& c0, f32x16& c1) __attribute__((always_inline)) {
        constexpr int N = decltype(NST)::value;
        int lo16 = lane * 16; asm volatile("" : "+v"(lo16));
        u32x4 tf[4][4];
#pragma unroll
        for (int st = 0; st < 4; ++st)
#pragma unroll
            for (int i = 0; i < 4; ++i) tf[st][i] = __builtin_amdgcn_raw_buffer_load_b128(tr, lo16 + (st * 8 + 4 * kh + i) * 1024, 0, H_AUX);
        SBAR();
#pragma unroll
        for (int st = 0; st < N; ++st) {
            if (bars && st > 0) LBAR();
            const lds_cptr sb = (lds_cptr)lds + (st & 3) * W_STAGE;
#pragma unroll
            for (int i = 0; i < 4; ++i) {
                const bf16x8 w0 = cat(ldstr(sb + wof[0][0] + i * 2048), ldstr(sb + wof[0][1] + i * 2048)), w1 = cat(ldstr(sb + wof[1][0] + i * 2048), ldstr(sb + wof[1][1] + i * 2048));
                c0 = MFMA32(w0, BF(tf[st & 3][i]), c0); c1 = MFMA32(w1, BF(tf[st & 3][i]), c1);
            }
            SBAR();
            if (st + 4 < N) {
#pragma unroll
                for (int i = 0; i < 4; ++i) tf[st & 3][i] = __builtin_amdgcn_raw_buffer_load_b128(tr, lo16 + ((st + 4) * 8 + 4 * kh + i) * 1024, 0, H_AUX);
                SBAR(); }
        }
    };
    auto merge = [&](f32x16& c0, f32x16& c1) __attribute__((always_inline)) {
        f32x4* dst = (f32x4*)(lds + G_RED + (wid ^ 2) * 4096) + lane;
#pragma unroll
        for (int q = 0; q < 4; ++q) dst[q * 64] = kh ? (f32x4){c0[4 * q], c0[4 * q + 1], c0[4 * q + 2], c0[4 * q + 3]} : (f32x4){c1[4 * q], c1[4 * q + 1], c1[4 * q + 2], c1[4 * q + 3]};
    };
    f32x16 gb0 = {}, gb1 = {};
    LBAR();
    gemm(__builtin_amdgcn_make_buffer_rsrc((void*)(ws + WS_H + (size_t)(tt * 2 + th) * 57344), 0, 57344, 0x00020000), std::integral_constant<int, 7>{}, true, gb0, gb1);
    merge(gb0, gb1);
    LBAR();
    { char* sc = lds + G_RED + wid * 4096;
      float gv[16];
#pragma unroll
      for (int q = 0; q < 4; ++q) { const f32x4 o4 = ((const f32x4*)sc)[q * 64 + lane];
#pragma unroll
          for (int r = 0; r < 4; ++r) { const float bb = (kh ? gb1[4 * q + r] : gb0[4 * q + r]) + o4[r]; gv[4 * q + r] = bb * __builtin_amdgcn_rcpf(1.0f + EX(-LOG2E * bb)); } }
      asm volatile("s_waitcnt lgkmcnt(0)" ::: "memory");
#pragma unroll
      for (int u = 0; u < 2; ++u) ((u32x4*)sc)[u * 64 + lane] = (u32x4){cvtpk(gv[8 * u], gv[8 * u + 1]), cvtpk(gv[8 * u + 2], gv[8 * u + 3]), cvtpk(gv[8 * u + 4], gv[8 * u + 5]), cvtpk(gv[8 * u + 6], gv[8 * u + 7])}; }
    const int n0 = ct * 64 + kh * 32 + 4 * hi;
    u32x2 xv[4];
#pragma unroll
    for (int q = 0; q < 4; ++q) xv[q] = *(const u32x2*)(x + (size_t)t * D + n0 + 8 * q);
    PIN(xv[0]); PIN(xv[1]); PIN(xv[2]); PIN(xv[3]);
    STAMP(1, 4);
    LBAR();
    STAMP(1, 5);
    f32x16 s0 = {}, s1 = {};
    {
        const __amdgpu_buffer_rsrc_t tr = __builtin_amdgcn_make_buffer_rsrc((void*)(ws + WS_O + (size_t)(tt * 2 + th) * 32768), 0, 32768, 0x00020000);
        int lo16 = lane * 16; asm volatile("" : "+v"(lo16));
        u32x4 tf[16];
#pragma unroll
        for (int i = 0; i < 16; ++i) tf[i] = __builtin_amdgcn_raw_buffer_load_b128(tr, lo16 + ((i >> 2) * 8 + 4 * kh + (i & 3)) * 1024, 0, O_AUX);
        SBAR();
        bf16x8 w0[2], w1[2];
        w0[0] = cat(ldstr((lds_cptr)lds + wof[0][0]), ldstr((lds_cptr)lds + wof[0][1])); w1[0] = cat(ldstr((lds_cptr)lds + wof[1][0]), ldstr((lds_cptr)lds + wof[1][1]));
#pragma unroll
        for (int i = 0; i < 16; ++i) {
            if (i + 1 < 16) { const lds_cptr sb = (lds_cptr)lds + ((i + 1) >> 2) * W_STAGE + ((i + 1) & 3) * 2048;
                w0[(i + 1) & 1] = cat(ldstr(sb + wof[0][0]), ldstr(sb + wof[0][1])); w1[(i + 1) & 1] = cat(ldstr(sb + wof[1][0]), ldstr(sb + wof[1][1])); }
            s0 = MFMA32(w0[i & 1], BF(tf[i]), s0); s1 = MFMA32(w1[i & 1], BF(tf[i]), s1);
        }
    }
    STAMP(1, 7);
    u32x4 gq[2];
    { const u32x4* gt = (const u32x4*)(lds + G_RED + wid * 4096) + lane; gq[0] = gt[0]; gq[1] = gt[64]; }
    LBAR();
    { f32x4* dst = (f32x4*)(lds + (wid ^ 2) * 4096) + lane;
#pragma unroll
      for (int q = 0; q < 4; ++q) dst[q * 64] = kh ? (f32x4){s0[4 * q], s0[4 * q + 1], s0[4 * q + 2], s0[4 * q + 3]} : (f32x4){s1[4 * q], s1[4 * q + 1], s1[4 * q + 2], s1[4 * q + 3]}; }
    LBAR();
    { const f32x4* src = (const f32x4*)(lds + wid * 4096) + lane;
#pragma unroll
      for (int q = 0; q < 4; ++q) { const f32x4 o4 = src[q * 64]; float v[4]; const unsigned g0 = gq[q >> 1][2 * (q & 1)], g1 = gq[q >> 1][2 * (q & 1) + 1]; const float gg[4] = {bflo(g0), bfhi(g0), bflo(g1), bfhi(g1)};
#pragma unroll
          for (int r = 0; r < 4; ++r) v[r] = ((kh ? s1[4 * q + r] : s0[4 * q + r]) + o4[r]) * gg[r];
          *(u32x2*)(out + (size_t)t * D + n0 + 8 * q) = (u32x2){cvtpk(bflo(xv[q][0]) + v[0], bfhi(xv[q][0]) + v[1]), cvtpk(bflo(xv[q][1]) + v[2], bfhi(xv[q][1]) + v[3])}; } }
    STAMP(1, 6);
}

__global__ __launch_bounds__(512, 4) void k2_mix(const u16* __restrict__ x, const u16* __restrict__ wo, const u16* __restrict__ wsp, const u16* __restrict__ bsp, const u16* __restrict__ wout, char* __restrict__ ws, u16* __restrict__ out) {
    extern __shared__ __attribute__((aligned(16))) char lds[];
    if (blockIdx.x < 256) attn_role(blockIdx.x, ws, lds);
    if (blockIdx.x >= 256) gate_role(blockIdx.x - 256, x, wo, wsp, bsp, wout, ws, out, lds);
}

extern "C" void kernel_launch(void* const* d_in, const int* in_sizes, int n_in, void* d_out, int out_size, void* d_ws, size_t ws_size, hipStream_t stream) {
    const u16* x = (const u16*)d_in[0]; const u16* wqkv = (const u16*)d_in[1]; const u16* wo = (const u16*)d_in[2];
    const u16* win = (const u16*)d_in[4]; const u16* wsp = (const u16*)d_in[5]; const u16* bsp = (const u16*)d_in[6]; const u16* wout = (const u16*)d_in[7];
    char* ws = (char*)d_ws;
    (void)hipFuncSetAttribute((const void*)k1_proj, hipFuncAttributeMaxDynamicSharedMemorySize, K1_LDS);
    (void)hipFuncSetAttribute((const void*)k2_mix, hipFuncAttributeMaxDynamicSharedMemorySize, K2_LDS);
    k1_proj<<<256, 768, K1_LDS, stream>>>(x, wqkv, win, ws);
    k2_mix<<<512, 512, K2_LDS, stream>>>(x, wo, wsp, bsp, wout, ws, (u16*)d_out);
}
```

```cpp
#include <hip/hip_runtime.h>
#include <hip/hip_bf16.h>
#include <stdint.h>
#include <type_traits>

typedef unsigned short u16;
using bf16x8 = __attribute__((ext_vector_type(8))) short;
using s16x4 = __attribute__((ext_vector_type(4))) short;
using f32x16 = __attribute__((ext_vector_type(16))) float;
using f32x4 = __attribute__((ext_vector_type(4))) float;
using u32x4 = __attribute__((ext_vector_type(4))) unsigned;
using u32x2 = __attribute__((ext_vector_type(2))) unsigned;
typedef __bf16 bf16x2_t __attribute__((ext_vector_type(2)));
typedef float f32x2_t __attribute__((ext_vector_type(2)));
typedef short v4i16_t __attribute__((ext_vector_type(4)));
#define LDSAS __attribute__((address_space(3)))
typedef LDSAS const char* lds_cptr;
#define MFMA32(a, b, c) __builtin_amdgcn_mfma_f32_32x32x16_bf16(a, b, c, 0, 0, 0)
#define SBAR() __builtin_amdgcn_sched_barrier(0)
#define PIN(x) asm volatile("" : "+v"(x))
#define WAIT_BAR(N) asm volatile("s_waitcnt vmcnt(" #N ") lgkmcnt(0)\n\ts_barrier" ::: "memory")
#define LBAR() asm volatile("s_waitcnt lgkmcnt(0)\n\ts_barrier" ::: "memory")
#define DI __device__ __forceinline__
#define ST8(p, w) st8_wt((void*)(p), (w))
#define ST16(off, v) __builtin_amdgcn_raw_buffer_store_b128((v), wsr, (off), 0, 16)

constexpr int S = 2048, D = 512, H = 32, DH = 16, FH = 896;
constexpr float LOG2E = 1.4426950408889634f;

constexpr size_t WS_Q = 0, WS_K = 2u << 20, WS_V = 4u << 20, WS_Z1 = 6u << 20, WS_Z2N = 14u << 20, WS_H = 18u << 20, WS_O = 22u << 20, WS_KMAX = 24u << 20, WS_EPOCH = 25u << 20, WS_XCH = 26u << 20, WS_XST = 27u << 20, WS_CNT = 28u << 20;

DI unsigned cvtpk(float lo, float hi) { f32x2_t v = {lo, hi}; return __builtin_bit_cast(unsigned, __builtin_convertvector(v, bf16x2_t)); }
DI float bflo(unsigned w) { return __uint_as_float(w << 16); }
DI float bfhi(unsigned w) { return __uint_as_float(w & 0xFFFF0000u); }
DI void glds16s(unsigned voff, const void* sbase, unsigned lds_base) {
    lds_base = __builtin_amdgcn_readfirstlane(lds_base);
    unsigned sv; asm volatile("s_mov_b32 %0, m0\n\ts_mov_b32 m0, %3\n\ts_nop 0\n\tglobal_load_lds_dwordx4 %1, %2\n\ts_mov_b32 m0, %0" : "=&s"(sv) : "v"(voff), "s"(sbase), "s"(lds_base) : "memory"); }
DI bf16x8 ldsr128(lds_cptr p) { return *(const LDSAS bf16x8*)p; }
DI s16x4 ldstr(lds_cptr p) { return __builtin_bit_cast(s16x4, __builtin_amdgcn_ds_read_tr16_b64_v4i16((LDSAS v4i16_t*)p)); }
DI bf16x8 cat(s16x4 a, s16x4 b) { return __builtin_shufflevector(a, b, 0, 1, 2, 3, 4, 5, 6, 7); }
DI float swap32(float x, bool sum) { auto rr = __builtin_amdgcn_permlane32_swap(__float_as_uint(x), __float_as_uint(x), false, false); return sum ? __uint_as_float(rr[0]) + __uint_as_float(rr[1]) : __uint_as_float(rr[0]); }
DI float gelu_t(float x) {
    const float c1 = -2.0f * 0.7978845608028654f * LOG2E, c3 = c1 * 0.044715f;
    const float u = x * fmaf(x * x, c3, c1);
    return x * __builtin_amdgcn_rcpf(1.0f + __builtin_amdgcn_exp2f(u));
}

constexpr int K1_XB = 16384;
constexpr int K1_STAGE = K1_XB + 7 * 4096;
constexpr int K1_CS = 3 * K1_STAGE;
constexpr int K1_LDS = K1_CS + 1024;
typedef __attribute__((address_space(1))) unsigned gu32; typedef __attribute__((address_space(1))) unsigned long long gu64;
#define RLX_AGENT __ATOMIC_RELAXED, __HIP_MEMORY_SCOPE_AGENT

DI float xor16(float x) { auto r = __builtin_amdgcn_permlane16_swap(__float_as_uint(x), __float_as_uint(x), false, false); return __uint_as_float(r[0]) + __uint_as_float(r[1]); }
DI float xor32(float x) { auto r = __builtin_amdgcn_permlane32_swap(__float_as_uint(x), __float_as_uint(x), false, false); return __uint_as_float(r[0]) + __uint_as_float(r[1]); }
DI float max16(float x) { auto r = __builtin_amdgcn_permlane16_swap(__float_as_uint(x), __float_as_uint(x), false, false); return fmaxf(__uint_as_float(r[0]), __uint_as_float(r[1])); }
DI float max32(float x) { auto r = __builtin_amdgcn_permlane32_swap(__float_as_uint(x), __float_as_uint(x), false, false); return fmaxf(__uint_as_float(r[0]), __uint_as_float(r[1])); }
template <int CTRL> DI float dppf(float x) { return __uint_as_float(__builtin_amdgcn_update_dpp(0, __float_as_uint(x), CTRL, 0xF, 0xF, false)); }
DI float rowmax16(float x) { x = fmaxf(x, dppf<0x128>(x)); x = fmaxf(x, dppf<0x124>(x)); x = fmaxf(x, dppf<0x122>(x)); x = fmaxf(x, dppf<0x121>(x)); return x; }
DI float wavesum(float x) { x += dppf<0x128>(x); x += dppf<0x124>(x); x += dppf<0x122>(x); x += dppf<0x121>(x); return xor32(xor16(x)); }
DI u32x4 widen(u32x2 a, u32x2 b) { auto r0 = __builtin_amdgcn_permlane32_swap(a[0], b[0], false, false); auto r1 = __builtin_amdgcn_permlane32_swap(a[1], b[1], false, false); return (u32x4){r0[0], r1[0], r0[1], r1[1]}; }

DI void gather8(u32x4 (&p)[8], unsigned voff, const void* sbase) {
    asm volatile("global_load_dwordx4 %0, %8, %9 sc1\n\tglobal_load_dwordx4 %1, %8, %9 offset:2048 sc1\n\tglobal_load_dwordx4 %2, %10, %9 sc1\n\tglobal_load_dwordx4 %3, %10, %9 offset:2048 sc1\n\t"
                 "global_load_dwordx4 %4, %11, %9 sc1\n\tglobal_load_dwordx4 %5, %11, %9 offset:2048 sc1\n\tglobal_load_dwordx4 %6, %12, %9 sc1\n\tglobal_load_dwordx4 %7, %12, %9 offset:2048 sc1\n\ts_waitcnt vmcnt(0)"
                 : "=&v"(p[0]), "=&v"(p[1]), "=&v"(p[2]), "=&v"(p[3]), "=&v"(p[4]), "=&v"(p[5]), "=&v"(p[6]), "=&v"(p[7])
                 : "v"(voff), "s"(sbase), "v"(voff + 4096u), "v"(voff + 8192u), "v"(voff + 12288u) : "memory"); }
DI void st8_wt(void* p, u32x2 w) { __hip_atomic_store((gu64*)p, ((unsigned long long)w[1] << 32) | w[0], RLX_AGENT); }
__global__ __launch_bounds__(768, 3) void k1_proj(const u16* __restrict__ x, const u16* __restrict__ wqkv, const u16* __restrict__ win, char* __restrict__ ws) {
    extern __shared__ __attribute__((aligned(16))) char lds[];
    const int tid = threadIdx.x, lane = tid & 63, r32 = lane & 31, hi = lane >> 5;
    const int wid = __builtin_amdgcn_readfirstlane(tid >> 6);
    const int b = blockIdx.x, xcd = b & 7, idx = b >> 3;
    const int panel = 8 * (xcd >> 2) + (idx & 7), j = 4 * (xcd & 3) + (idx >> 3);
    const unsigned lds0 = (unsigned)(uintptr_t)lds;
    const int jq = j & 3, u7 = 7 * (j >> 2);
    const int n1 = jq == 3 ? 1 : 2, n2 = jq == 1 ? 1 : 2;
    const int z1t0 = u7 + 2 * jq, z2t0 = u7 + (jq == 0 ? 0 : (jq == 1 ? 2 : (jq == 2 ? 3 : 5)));
    if (b == 0 && tid < 40) ((unsigned*)(ws + WS_CNT))[tid * 64] = 0u;
    if (wid >= 8) {
        const int l = wid - 8;
        const char* xb = (const char*)x + (size_t)(panel * 128 + l * 32) * 1024;
        const unsigned xv0 = (unsigned)((lane >> 3) * 1024 + (((lane & 7) ^ (lane >> 4)) * 16)), xv1 = (unsigned)((lane >> 3) * 1024 + (((lane & 7) ^ (4 + (lane >> 4))) * 16));
        const unsigned vq = (unsigned)((16 * l + (lane >> 2)) * 3072 + (lane & 3) * 16), vz = (unsigned)((16 * l + (lane >> 2)) * 3584 + (lane & 3) * 16);
        const char* wq = (const char*)wqkv + j * 64;
        int zc[4];
#pragma unroll
        for (int i = 0; i < 4; ++i) { const int ii = i < n1 + n2 ? i : n1 + n2 - 1; zc[i] = ii < n1 ? (z1t0 + ii) * 64 : FH * 2 + (z2t0 + ii - n1) * 64; }
        auto dma = [&](int st) __attribute__((always_inline)) {
            const unsigned d = lds0 + (st % 3) * K1_STAGE;
#pragma unroll
            for (int p = 0; p < 4; ++p) glds16s((p & 1) ? xv1 : xv0, xb + st * 128 + p * 8192, d + l * 4096 + p * 1024);
            const char* bq = wq + (size_t)st * 64 * 3072; const char* bz = (const char*)win + (size_t)st * 64 * 3584; const unsigned dw = d + K1_XB + l * 1024;
            glds16s(vq, bq + 2048, dw); glds16s(vq, bq, dw + 4096); glds16s(vq, bq + 1024, dw + 8192);
#pragma unroll
            for (int i = 0; i < 4; ++i) glds16s(vz, bz + zc[i], dw + (3 + i) * 4096);
        };
        dma(0);
        WAIT_BAR(0);
        dma(1); dma(2);
#pragma unroll
        for (int st = 1; st < 8; ++st) {
            if (st < 7) WAIT_BAR(11); else WAIT_BAR(0);
            if (st + 2 < 8) dma(st + 2);
        }
        return;
    }
    const int mw = wid & 3, nh = wid >> 2;
    gu32* epw = (gu32*)(ws + WS_EPOCH) + panel * 64;
    unsigned tag = __hip_atomic_load(epw, RLX_AGENT);
    u32x4 xr = *(const u32x4*)(x + (size_t)(panel * 128 + 8 * j + wid) * D + lane * 8);
    auto rowstats = [&]() __attribute__((always_inline)) {
        PIN(xr); PIN(tag); tag += 1u;
        float v[8];
#pragma unroll
        for (int i = 0; i < 4; ++i) { v[2 * i] = bflo(xr[i]); v[2 * i + 1] = bfhi(xr[i]); }
        const float m = wavesum(((v[0] + v[1]) + (v[2] + v[3])) + ((v[4] + v[5]) + (v[6] + v[7]))) * (1.0f / 512.0f);
        float q = 0.f;
#pragma unroll
        for (int i = 0; i < 8; ++i) q = fmaf(v[i] - m, v[i] - m, q);
        const float r = __builtin_amdgcn_rsqf(wavesum(q) * (1.0f / 512.0f) + 1e-5f);
        if (lane < 2) __hip_atomic_store((gu64*)(ws + WS_XST) + (size_t)(panel * 128 + 8 * j + wid) * 2 + lane, ((unsigned long long)tag << 32) | __float_as_uint(lane ? r : m), RLX_AGENT);
    };
    f32x16 acc[4];
#pragma unroll
    for (int s = 0; s < 4; ++s) acc[s] = (f32x16){};
    float cs0 = 0.f, cs1 = 0.f;
    const int g = lane >> 4, qq = (lane & 15) >> 2, pp = lane & 3;
    const int trow = 32 * mw + r32;
    unsigned xo[4];
#pragma unroll
    for (int ks = 0; ks < 4; ++ks) xo[ks] = (unsigned)(trow * 128 + (((2 * ks + hi) ^ ((trow >> 1) & 7)) * 16));
    const unsigned wlane = (unsigned)(K1_XB + (8 * (g >> 1) + qq) * 64 + (g & 1) * 32 + pp * 8);
    const __amdgpu_buffer_rsrc_t sr = __builtin_amdgcn_make_buffer_rsrc((void*)(ws + WS_XST + (size_t)panel * 128 * 16), 0, 128 * 16, 0x00020000);
    u32x4 pst;
    int tile[4];
    auto mainloop = [&](auto NHC) __attribute__((always_inline)) {
        constexpr int NHV = decltype(NHC)::value; constexpr int NS = NHV == 0 ? 4 : 3;
        unsigned wo[NS];
#pragma unroll
        for (int s = 0; s < NS; ++s) { int t = s + mw; t = t >= NS ? t - NS : t; t = t >= NS ? t - NS : t; tile[s] = 4 * NHV + t; wo[s] = wlane + tile[s] * 4096; }
        bf16x8 xa[2], wf[2][NS];
        auto rd = [&](const int set, const lds_cptr sb, const int ks) __attribute__((always_inline)) {
            xa[set] = ldsr128(sb + xo[ks]);
#pragma unroll
            for (int s = 0; s < NS; ++s) wf[set][s] = cat(ldstr(sb + wo[s] + ks * 1024), ldstr(sb + wo[s] + ks * 1024 + 256));
        };
        auto mm = [&](const int set) __attribute__((always_inline)) {
            SBAR();
#pragma unroll
            for (int s = 0; s < NS; ++s) acc[s] = MFMA32(wf[set][s], xa[set], acc[s]);
            const u32x4 w = __builtin_bit_cast(u32x4, wf[set][0]);
#pragma unroll
            for (int i = 0; i < 4; ++i) { cs0 += bflo(w[i]); cs1 += bfhi(w[i]); }
            PIN(cs0); PIN(cs1);
#pragma unroll
            for (int i = 0; i < NS; ++i) { __builtin_amdgcn_sched_group_barrier(0x008, 1, 0); __builtin_amdgcn_sched_group_barrier(0x002, NS == 4 ? 4 : 6, 0); }
            SBAR();
        };
        LBAR();
        rd(0, (lds_cptr)lds, 0);
#pragma unroll
        for (int st = 0; st < 8; ++st) {
            const lds_cptr sb = (lds_cptr)lds + (st % 3) * K1_STAGE;
            rd(1, sb, 1); mm(0);
            rd(0, sb, 2); mm(1);
            rd(1, sb, 3); mm(0);
            if (st < 7) { LBAR(); rd(0, (lds_cptr)lds + ((st + 1) % 3) * K1_STAGE, 0); }
            if (st == 1) rowstats();
            if (st == 5) pst = __builtin_amdgcn_raw_buffer_load_b128(sr, trow * 16, 0, 16);
            mm(1);
        }
    };
    if (nh == 0) mainloop(std::integral_constant<int, 0>{}); else mainloop(std::integral_constant<int, 1>{});
    float* csl = (float*)(lds + K1_CS);
    { const float c = xor32(cs0 + cs1); if (hi == 0) csl[tile[0] * 32 + r32] = c; }
    const int tok = panel * 128 + trow;
    float mu, rs;
    {   unsigned long long g0 = ((unsigned long long)pst[1] << 32) | pst[0], g1 = ((unsigned long long)pst[3] << 32) | pst[2];
        gu64* gp = (gu64*)(ws + WS_XST) + (size_t)(panel * 128 + trow) * 2;
        for (unsigned spins = 0; !__all(((unsigned)(g0 >> 32) == tag) & ((unsigned)(g1 >> 32) == tag)) && spins < 200000u; ++spins) {
            __builtin_amdgcn_s_sleep(1);
            g0 = __hip_atomic_load(gp, RLX_AGENT); g1 = __hip_atomic_load(gp + 1, RLX_AGENT);
        }
        mu = __uint_as_float((unsigned)g0); rs = __uint_as_float((unsigned)g1); }
    LBAR();
    auto lnv = [&](int s, float (&v)[16], float sc) __attribute__((always_inline)) {
#pragma unroll
        for (int q = 0; q < 4; ++q) { const f32x4 c = *(const f32x4*)(csl + tile[s] * 32 + 8 * q + 4 * hi);
#pragma unroll
            for (int r = 0; r < 4; ++r) v[4 * q + r] = sc * (acc[s][4 * q + r] - mu * c[r]); } };
    const __amdgpu_buffer_rsrc_t wsr = __builtin_amdgcn_make_buffer_rsrc((void*)ws, 0, 32u << 20, 0x00020000);
    u16* Z1 = (u16*)(ws + WS_Z1);
    auto z1store = [&](int zt, const u32x2 (&w)[4]) __attribute__((always_inline)) {
        u16* p = Z1 + ((size_t)((tok >> 5) * 28 + zt) * 1024) + (hi * 32 + r32) * 4;
#pragma unroll
        for (int q = 0; q < 4; ++q) ST8(p + q * 256, w[q]); };
    auto gelu16 = [&](int s, u32x2 (&w)[4]) __attribute__((always_inline)) { float v[16]; lnv(s, v, rs);
#pragma unroll
        for (int q = 0; q < 4; ++q) w[q] = (u32x2){cvtpk(gelu_t(v[4 * q]), gelu_t(v[4 * q + 1])), cvtpk(gelu_t(v[4 * q + 2]), gelu_t(v[4 * q + 3]))}; };
    if (nh == 0) {
#pragma unroll
        for (int s = 0; s < 4; ++s) {
            const int T = tile[s];
            if (T == 3) { u32x2 w[4]; gelu16(s, w); z1store(z1t0, w); }
            else {
                float v[16]; lnv(s, v, T == 1 ? rs * (0.25f * LOG2E) : rs);
                u32x2 w[4];
#pragma unroll
                for (int q = 0; q < 4; ++q) w[q] = (u32x2){cvtpk(v[4 * q], v[4 * q + 1]), cvtpk(v[4 * q + 2], v[4 * q + 3])};
                if (T == 0) {
                    char* sc = lds + mw * 2048;
#pragma unroll
                    for (int q = 0; q < 4; ++q) *(u32x2*)(sc + r32 * 64 + (8 * q + 4 * hi) * 2) = w[q];
                    asm volatile("s_waitcnt lgkmcnt(0)" ::: "memory");
                    u16* Vt = (u16*)(ws + WS_V);
#pragma unroll
                    for (int hh = 0; hh < 2; ++hh)
#pragma unroll
                        for (int ks = 0; ks < 2; ++ks) {
                            const s16x4 t4 = ldstr((lds_cptr)lds + mw * 2048 + (16 * ks + 8 * (g >> 1) + 4 * (g & 1) + qq) * 64 + hh * 32 + pp * 8);
                            ST8(Vt + ((size_t)(2 * j + hh) * 128 + ((panel * 128 + 32 * mw) >> 4) + ks) * 256 + (g & 1) * 128 + (lane & 15) * 8 + (g >> 1) * 4, __builtin_bit_cast(u32x2, t4)); }
                } else {
                    const unsigned dsto = (unsigned)(T == 1 ? WS_Q : WS_K);
#pragma unroll
                    for (int hh = 0; hh < 2; ++hh) ST16(dsto + (((2 * j + hh) * S + tok) * 16 + 8 * hi) * 2, widen(w[2 * hh], w[2 * hh + 1]));
                    if (T == 2) {
                        float* kmax = (float*)(ws + WS_KMAX);
#pragma unroll
                        for (int hh = 0; hh < 2; ++hh) { float n = 0.f;
#pragma unroll
                            for (int q = 0; q < 2; ++q) { const u32x2 ww = w[2 * hh + q]; n = fmaf(bflo(ww[0]), bflo(ww[0]), n); n = fmaf(bfhi(ww[0]), bfhi(ww[0]), n); n = fmaf(bflo(ww[1]), bflo(ww[1]), n); n = fmaf(bfhi(ww[1]), bfhi(ww[1]), n); }
                            n = max16(rowmax16(xor32(n)));
                            if (lane == 0) kmax[(panel * 4 + mw) * H + 2 * j + hh] = n; }
                    }
                }
            }
        }
    } else {
        __builtin_amdgcn_s_setprio(2);
        float sm = 0.f;
#pragma unroll
        for (int s = 0; s < 3; ++s) { const int zi = tile[s] - 3;
            if (zi >= n1 && zi < n1 + n2) { float v[16]; lnv(s, v, rs);
#pragma unroll
                for (int r = 0; r < 16; ++r) { acc[s][r] = gelu_t(v[r]); sm += acc[s][r]; } } }
        sm = xor32(sm);
        const float ml = sm * (n2 == 2 ? (1.0f / 64.f) : (1.0f / 32.f)); float m2 = 0.f;
#pragma unroll
        for (int s = 0; s < 3; ++s) { const int zi = tile[s] - 3;
            if (zi >= n1 && zi < n1 + n2) {
#pragma unroll
                for (int r = 0; r < 16; ++r) { const float d = acc[s][r] - ml; m2 = fmaf(d, d, m2); } } }
        m2 = xor32(m2);
        __hip_atomic_store((gu64*)(ws + WS_XCH) + ((size_t)(panel * 16 + j) * 128 + trow) * 2 + hi, ((unsigned long long)tag << 32) | __float_as_uint(hi ? m2 : sm), RLX_AGENT);
        __builtin_amdgcn_s_setprio(0);
#pragma unroll
        for (int s = 0; s < 3; ++s) { const int zi = tile[s] - 3; if (zi < n1) { u32x2 w[4]; gelu16(s, w); z1store(z1t0 + zi, w); } }
        float mean, rstd;
        {
            const char* xb = ws + WS_XCH + (size_t)panel * 16 * 128 * 16; const unsigned xo = (unsigned)((8 * hi * 128 + trow) * 16);
            u32x4 p[8];
            for (unsigned spins = 0;; ++spins) {
                bool ok = true;
                gather8(p, xo, xb);
#pragma unroll
                for (int u = 0; u < 8; ++u) ok &= (p[u][1] == tag) & (p[u][3] == tag);
                if (__all(ok) || spins > 200000u) break;
                __builtin_amdgcn_s_sleep(6);
            }
            float tot = 0.f;
#pragma unroll
            for (int u = 0; u < 8; ++u) tot += __uint_as_float(p[u][0]);
            mean = xor32(tot) * (1.0f / FH);
            float mm2 = 0.f;
#pragma unroll
            for (int u = 0; u < 8; ++u) { const float nl = (u & 3) == 1 ? 32.f : 64.f; const float d = __uint_as_float(p[u][0]) * ((u & 3) == 1 ? (1.0f / 32.f) : (1.0f / 64.f)) - mean; mm2 += __uint_as_float(p[u][2]) + nl * d * d; }
            rstd = __builtin_amdgcn_rsqf(xor32(mm2) * (1.0f / FH) + 1e-5f);
        }
        if (wid == 4 && lane == 0) __hip_atomic_store(epw, tag, RLX_AGENT);
        const float nmr = -mean * rstd;
#pragma unroll
        for (int s = 0; s < 3; ++s) { const int zi = tile[s] - 3;
            if (zi >= n1 && zi < n1 + n2) {
                const int ft = z2t0 + zi - n1;
                u32x2 w[4];
#pragma unroll
                for (int q = 0; q < 4; ++q) w[q] = (u32x2){cvtpk(fmaf(acc[s][4 * q], rstd, nmr), fmaf(acc[s][4 * q + 1], rstd, nmr)), cvtpk(fmaf(acc[s][4 * q + 2], rstd, nmr), fmaf(acc[s][4 * q + 3], rstd, nmr))};
                char* sc = lds + 8192 + mw * 2048;
#pragma unroll
                for (int q = 0; q < 4; ++q) *(u32x2*)(sc + r32 * 64 + (8 * q + 4 * hi) * 2) = w[q];
                asm volatile("s_waitcnt lgkmcnt(0)" ::: "memory");
                const int tk0 = panel * 128 + 32 * mw;
#pragma unroll
                for (int ks = 0; ks < 2; ++ks) {
                    const lds_cptr rp = (lds_cptr)lds + 8192 + mw * 2048 + (16 * ks + 8 * (g >> 1) + qq) * 64 + (g & 1) * 32 + pp * 8;
                    const bf16x8 fr = cat(ldstr(rp), ldstr(rp + 256));
                    ST16((unsigned)WS_Z2N + (unsigned)(((((ft >> 2) * 32 + (tk0 >> 6)) * 4 + (ft & 3)) * 4 + ((tk0 >> 4) & 3) + ks) * 1024 + lane * 16), __builtin_bit_cast(u32x4, fr));
                }
                asm volatile("s_waitcnt lgkmcnt(0)" ::: "memory");
            } }
    }
}

constexpr int A_KR = 0, A_VR = 32768, A_CONST = 65536, A_FLAG = A_CONST + 4096, A_MX = A_FLAG + 64, K2_LDS = 81920;
constexpr float SAFE_T = 48.f;
#define EX(x) __builtin_amdgcn_exp2f(x)
#define BF(p) __builtin_bit_cast(bf16x8, p)

DI void attn_role(int b, char* __restrict__ ws, char* lds) {
    const u16* Qs = (const u16*)(ws + WS_Q); const u16* Kb = (const u16*)(ws + WS_K); const u16* Vt = (const u16*)(ws + WS_V); const float* kmax = (const float*)(ws + WS_KMAX); u16* Ot = (u16*)(ws + WS_O);
    const int tid = threadIdx.x, lane = tid & 63, r32 = lane & 31, hi = lane >> 5;
    const int wid = __builtin_amdgcn_readfirstlane(tid >> 6);
    const int xcd = b & 7, idx = b >> 3, head = xcd * 4 + (idx & 3), qt = idx >> 2;
    const int qp = wid & 3, kh = wid >> 2;
    const unsigned lds0 = (unsigned)(uintptr_t)lds;
    const char* kg = (const char*)(Kb + (size_t)head * S * DH);
    const char* vg = (const char*)(Vt + (size_t)head * S * DH);
    const unsigned kvoff = wid * 1024 + ((lane ^ ((lane >> 5) & 1)) * 16);
    const unsigned vvoff = wid * 1024 + lane * 16;
#define DMA_CHUNK(c) do { glds16s(kvoff, kg + (c) * 8192, lds0 + A_KR + ((c) & 3) * 8192 + wid * 1024); glds16s(vvoff, vg + (c) * 8192, lds0 + A_VR + ((c) & 3) * 8192 + wid * 1024); } while (0)
    const int q0 = qt * 256 + qp * 64;
    const bf16x8 qa = *(const bf16x8*)(Qs + ((size_t)head * S + q0 + r32) * DH + hi * 8);
    const bf16x8 qb = *(const bf16x8*)(Qs + ((size_t)head * S + q0 + 32 + r32) * DH + hi * 8);
    float km = kmax[lane * H + head];
    DMA_CHUNK(0);
    if (tid < 256) { const unsigned w = (tid & 31) ? 0u : 0x3F803F80u; u32x4 v = {w, w, w, w}; *(u32x4*)(lds + A_CONST + tid * 16) = v; }
    km = max32(max16(rowmax16(km)));
    float qna = 0.f, qnb = 0.f;
#pragma unroll
    for (int j = 0; j < 8; ++j) { const float va = bflo((u16)qa[j]), vb = bflo((u16)qb[j]); qna = fmaf(va, va, qna); qnb = fmaf(vb, vb, qnb); }
    qna = xor32(qna); qnb = xor32(qnb);
    const float mh = __builtin_amdgcn_sqrtf(fmaxf(qna, qnb) * km) * 1.001f;
    { const bool unsafe = __any(!(mh <= SAFE_T)); if (lane == 0) ((unsigned*)(lds + A_FLAG))[wid] = unsafe ? 1u : 0u; }
    const lds_cptr kp0 = (lds_cptr)lds + A_KR + kh * 4096 + r32 * 32 + ((hi ^ (r32 >> 4)) * 16);
    const bool vreal = r32 < 16;
    const unsigned vlane = vreal ? (unsigned)(A_VR + kh * 4096 + hi * 256 + r32 * 16) : (unsigned)(A_CONST + (r32 == 16 ? 0 : ((r32 >= 20 && r32 < 28) ? 64 : 16)));
    const unsigned vmask = vreal ? 0xFFFFFFFFu : 0u;
    f32x16 oa = {}, ob = {};
    WAIT_BAR(0);
    bool slow;
    { const u32x4 f0 = *(const u32x4*)(lds + A_FLAG), f1 = *(const u32x4*)(lds + A_FLAG + 16); slow = __builtin_amdgcn_readfirstlane(f0[0] | f0[1] | f0[2] | f0[3] | f1[0] | f1[1] | f1[2] | f1[3]) != 0; }
    if (!slow) {
        DMA_CHUNK(1); DMA_CHUNK(2); DMA_CHUNK(3);
        f32x16 cinit;
#pragma unroll
        for (int r = 0; r < 16; ++r) cinit[r] = -mh;
        PIN(cinit);
        bf16x8 kf, v0, v1; f32x16 sA, sB; u32x4 p0, p1;
        kf = ldsr128(kp0);
        { lds_cptr vp = (lds_cptr)lds + vlane; v0 = ldsr128(vp); v1 = ldsr128(vp + 512); }
        sA = MFMA32(kf, qa, cinit);
        p1 = (u32x4){0u, 0u, 0u, 0u};
#define HSTEP(SC, SN, QN, OP, OC, PRE1, PRE2, PRE3) do { \
        SN = MFMA32(kf, QN, cinit); PIN(SN); SBAR(); \
        PRE1; \
        SC[0] = EX(SC[0]); SC[1] = EX(SC[1]); SC[2] = EX(SC[2]); SC[3] = EX(SC[3]); SC[4] = EX(SC[4]); SC[5] = EX(SC[5]); PIN(SC); SBAR(); \
        OP = MFMA32(v1, BF(p1), OP); PIN(OP); SBAR(); \
        PRE2; \
        SC[6] = EX(SC[6]); SC[7] = EX(SC[7]); \
        p0[0] = cvtpk(SC[0], SC[1]); p0[1] = cvtpk(SC[2], SC[3]); p0[2] = cvtpk(SC[4], SC[5]); p0[3] = cvtpk(SC[6], SC[7]); PIN(p0); SBAR(); \
        SC[8] = EX(SC[8]); SC[9] = EX(SC[9]); SC[10] = EX(SC[10]); PIN(SC); SBAR(); \
        OC = MFMA32(v0, BF(p0), OC); PIN(OC); SBAR(); \
        PRE3; \
        SC[11] = EX(SC[11]); SC[12] = EX(SC[12]); SC[13] = EX(SC[13]); SC[14] = EX(SC[14]); SC[15] = EX(SC[15]); \
        p1[0] = cvtpk(SC[8], SC[9]); p1[1] = cvtpk(SC[10], SC[11]); p1[2] = cvtpk(SC[12], SC[13]); p1[3] = cvtpk(SC[14], SC[15]); PIN(p1); SBAR(); \
    } while (0)
#define BLOCK(kn, vc, vco, vn, vno) do { \
        HSTEP(sA, sB, qb, ob, oa, kf = ldsr128(kn), v1 = ldsr128((vc) + (vco) + 512), (void)0); \
        HSTEP(sB, sA, qa, oa, ob, (void)0, (void)0, v0 = ldsr128((vn) + (vno))); \
    } while (0)
        for (int c = 0; c < 8; ++c) {
            const unsigned ro = (unsigned)((c & 3) * 8192), rn = (unsigned)(((c + 1) & 3) * 8192);
            const lds_cptr vp = (lds_cptr)lds + (vlane + (ro & vmask)), vpn = (lds_cptr)lds + (vlane + (rn & vmask));
            const lds_cptr kp = kp0 + ro, kpn = kp0 + rn;
            BLOCK(kp + 1024, vp, 0, vp, 1024);
            BLOCK(kp + 2048, vp, 1024, vp, 2048);
            BLOCK(kp + 3072, vp, 2048, vp, 3072);
            if (c == 0) WAIT_BAR(4); else if (c < 6) WAIT_BAR(2); else if (c == 6) WAIT_BAR(0);
            if (c >= 1 && c + 3 < 8) DMA_CHUNK(c + 3);
            BLOCK(kpn, vp, 3072, vpn, 0);
        }
        ob = MFMA32(v1, BF(p1), ob);
#undef HSTEP
#undef BLOCK
    } else {
        float ma = -3.0e38f, mb = -3.0e38f;
        for (int pass = 0; pass < 2; ++pass) {
            for (int c = 0; c < 8; ++c) {
                LBAR();
                DMA_CHUNK(c);
                WAIT_BAR(0);
                const unsigned ro = (unsigned)((c & 3) * 8192);
                const lds_cptr vp = (lds_cptr)lds + (vlane + (ro & vmask)), kp = kp0 + ro;
                for (int j = 0; j < 4; ++j) {
                    const bf16x8 kf = ldsr128(kp + j * 1024);
                    const bf16x8 v0 = ldsr128(vp + j * 1024), v1 = ldsr128(vp + j * 1024 + 512);
#pragma unroll
                    for (int u = 0; u < 2; ++u) {
                        const f32x16 sc = MFMA32(kf, u ? qb : qa, (f32x16){});
                        float& m = u ? mb : ma; f32x16& o = u ? ob : oa;
                        if (pass == 0) {
#pragma unroll
                            for (int r = 0; r < 16; ++r) m = fmaxf(m, sc[r]);
                        } else {
                            u32x4 p0, p1;
#pragma unroll
                            for (int i = 0; i < 4; ++i) { p0[i] = cvtpk(EX(sc[2 * i] - m), EX(sc[2 * i + 1] - m)); p1[i] = cvtpk(EX(sc[8 + 2 * i] - m), EX(sc[9 + 2 * i] - m)); }
                            o = MFMA32(v0, BF(p0), o); o = MFMA32(v1, BF(p1), o);
                        }
                        SBAR();
                    }
                }
            }
            if (pass == 0) {
                ma = max32(ma); mb = max32(mb);
                float* mx = (float*)(lds + A_MX);
                if (hi == 0) { mx[(wid * 2) * 32 + r32] = ma; mx[(wid * 2 + 1) * 32 + r32] = mb; }
                LBAR();
                ma = fmaxf(ma, mx[((wid ^ 4) * 2) * 32 + r32]); mb = fmaxf(mb, mx[((wid ^ 4) * 2 + 1) * 32 + r32]);
            }
        }
    }
    __syncthreads();
    int tz = threadIdx.x; asm volatile("" : "+v"(tz));
    const int lz = tz & 63;
    float* red = (float*)lds;
    { float* dst = red + (size_t)(wid ^ 4) * 9 * 64 + lz;
#pragma unroll
      for (int r = 0; r < 9; ++r) dst[r * 64] = kh ? oa[r] : ob[r]; }
    __syncthreads();
    float o[9];
    { const float* src = red + (size_t)wid * 9 * 64 + lz;
#pragma unroll
      for (int r = 0; r < 9; ++r) o[r] = (kh ? ob[r] : oa[r]) + src[r * 64]; }
    const float rl = __builtin_amdgcn_rcpf(swap32(o[8], false));
    u16* op = Ot + ((size_t)(((q0 >> 5) + kh) * H + head) * 512) + (lz & 31) * 8 + (lz >> 5) * 4;
    st8_wt(op, (u32x2){cvtpk(o[0] * rl, o[1] * rl), cvtpk(o[2] * rl, o[3] * rl)});
    st8_wt(op + 256, (u32x2){cvtpk(o[4] * rl, o[5] * rl), cvtpk(o[6] * rl, o[7] * rl)});
    asm volatile("s_waitcnt vmcnt(0)\n\ts_barrier" ::: "memory");
    if (tz == 0) __hip_atomic_fetch_add((gu32*)(ws + WS_CNT) + (32 + qt) * 64, 1u, RLX_AGENT);
#undef DMA_CHUNK
}

DI void wait_cnt(gu32* cnt, unsigned want) {
    for (unsigned spins = 0; spins < 400000u; ++spins) {
        if ((unsigned)__builtin_amdgcn_readfirstlane(__hip_atomic_load(cnt, RLX_AGENT)) >= want) break;
        __builtin_amdgcn_s_sleep(0);
    }
}


constexpr int G_STAGE = 24576;
constexpr int W_STAGE = 16384;
constexpr int G_RED = 4 * W_STAGE;
DI void gate_role(int b, const u16* __restrict__ x, const u16* __restrict__ wo, const u16* __restrict__ wsp, const u16* __restrict__ bsp, const u16* __restrict__ wout, char* __restrict__ ws, u16* __restrict__ out, char* lds) {
    const int xcd = b & 7, idx = b >> 3;
    const int tt = xcd * 4 + (idx & 3), ct = idx >> 2;
    const int tid = threadIdx.x, lane = tid & 63, r32 = lane & 31, hi = lane >> 5;
    const int wid = __builtin_amdgcn_readfirstlane(tid >> 6);
    const unsigned lds0 = (unsigned)(uintptr_t)lds;
    gu32* hcnt = (gu32*)(ws + WS_CNT) + tt * 64; gu32* ocnt = (gu32*)(ws + WS_CNT) + (32 + (tt >> 2)) * 64;
    if (wid >= 4) {
        const int l = wid - 4;
        if (ct < 7) {
            const char* ag = ws + WS_Z2N + (size_t)ct * 32 * 16384 + l * 4096;
            const char* bg = (const char*)wsp + (size_t)(tt * 64 + l * 16) * 4096;
            const unsigned avoff = lane * 16;
            const unsigned bv0 = (unsigned)((lane >> 3) * 4096 + (((lane & 7) ^ (lane >> 4)) * 16)), bv1 = (unsigned)((8 + (lane >> 3)) * 4096 + (((lane & 7) ^ (4 + (lane >> 4))) * 16));
            auto dma = [&](int kt, int slot) __attribute__((always_inline)) { const unsigned d = lds0 + slot * G_STAGE; const char* a_ = ag + (size_t)kt * 16384;
#pragma unroll
                for (int e = 0; e < 4; ++e) glds16s(avoff, a_ + e * 1024, d + l * 4096 + e * 1024);
                glds16s(bv0, bg + kt * 128, d + 16384 + l * 2048); glds16s(bv1, bg + kt * 128, d + 16384 + l * 2048 + 1024); };
            dma(0, 0);
            WAIT_BAR(0);
            dma(1, 1); dma(2, 2);
            int slot = 0;
            for (int kt = 1; kt < 32; ++kt) {
                if (kt < 31) WAIT_BAR(6); else WAIT_BAR(0);
                if (kt + 2 < 32) dma(kt + 2, slot);
                slot = slot == 2 ? 0 : slot + 1;
            }
            LBAR();
        }
        unsigned wv[4];
#pragma unroll
        for (int e = 0; e < 4; ++e) { const int row = l * 32 + e * 8 + (lane >> 3); wv[e] = (unsigned)(row * 1024 + ct * 128 + (((lane & 7) ^ (((row >> 1) & 1) << 2)) * 16)); }
        auto dmw = [&](const u16* w, int st) __attribute__((always_inline)) { const char* wg = (const char*)w + (size_t)st * 128 * 1024; const unsigned d = lds0 + (st & 3) * W_STAGE + l * 4096;
#pragma unroll
            for (int e = 0; e < 4; ++e) glds16s(wv[e], wg, d + e * 1024); };
        dmw(wout, 0); dmw(wout, 1); dmw(wout, 2); dmw(wout, 3);
        if (l == 0) { wait_cnt(hcnt, 7);
        }
        WAIT_BAR(12);
#pragma unroll
        for (int st = 1; st < 7; ++st) {
            if (st < 4) WAIT_BAR(8); else if (st == 4) WAIT_BAR(8); else if (st == 5) WAIT_BAR(4); else WAIT_BAR(0);
            if (st + 3 < 7) dmw(wout, st + 3);
        }
        LBAR();
        dmw(wo, 0); dmw(wo, 1); dmw(wo, 2); dmw(wo, 3);
        if (l == 0) { wait_cnt(ocnt, 32);
        }
        WAIT_BAR(0);
        return;
    }
    __builtin_amdgcn_s_setprio(2);
    const int g = lane >> 4, qq = (lane & 15) >> 2, pp = lane & 3;
    const int th = wid & 1, fh = wid >> 1;
    const int trow = 32 * th + r32, t = tt * 64 + trow;
    if (ct < 7) {
        f32x16 acc0 = {}, acc1 = {};
        const unsigned aoff = (unsigned)(fh * 8192 + lane * 16);
        unsigned boff[4];
#pragma unroll
        for (int ks = 0; ks < 4; ++ks) boff[ks] = (unsigned)(16384 + trow * 128 + (((2 * ks + hi) ^ ((trow >> 1) & 7)) * 16));
        bf16x8 fa0[2], fa1[2], fb[2];
        auto rd = [&](int set, lds_cptr sb, int ks) __attribute__((always_inline)) {
            fb[set] = ldsr128(sb + boff[ks]);
            fa0[set] = ldsr128(sb + aoff + ks * 1024);
            fa1[set] = ldsr128(sb + aoff + 4096 + ks * 1024); };
#define GSTEP(cur, nxt, sbn, ksn) do { SBAR(); acc0 = MFMA32(fa0[cur], fb[cur], acc0); SBAR(); rd(nxt, sbn, ksn); SBAR(); acc1 = MFMA32(fa1[cur], fb[cur], acc1); SBAR(); } while (0)
        const float bias = bflo(bsp[t]);
        u32x2 z[2][4];
#pragma unroll
        for (int i = 0; i < 2; ++i)
#pragma unroll
            for (int q = 0; q < 4; ++q) z[i][q] = *(const u32x2*)((const u16*)(ws + WS_Z1) + ((size_t)((t >> 5) * 28 + ct * 4 + 2 * fh + i) * 1024) + (hi * 32 + r32) * 4 + q * 256);
        LBAR();
        rd(0, (lds_cptr)lds, 0);
        int slot = 0;
        for (int kt = 0; kt < 32; ++kt) {
            const lds_cptr sb = (lds_cptr)lds + slot * G_STAGE;
            GSTEP(0, 1, sb, 1);
            GSTEP(1, 0, sb, 2);
            GSTEP(0, 1, sb, 3);
            slot = slot == 2 ? 0 : slot + 1;
            if (kt < 31) { LBAR(); GSTEP(1, 0, (lds_cptr)lds + slot * G_STAGE, 0); }
            else { acc0 = MFMA32(fa0[1], fb[1], acc0); acc1 = MFMA32(fa1[1], fb[1], acc1); }
        }
#pragma unroll
        for (int i = 0; i < 2; ++i) {
            u16* hp = (u16*)(ws + WS_H) + ((size_t)((t >> 5) * 56 + (ct * 4 + 2 * fh + i) * 2) * 512) + r32 * 8 + hi * 4;
#pragma unroll
            for (int q = 0; q < 4; ++q) {
                float v[4];
#pragma unroll
                for (int r = 0; r < 4; ++r) v[r] = (i ? acc1[4 * q + r] : acc0[4 * q + r]) + bias;
                st8_wt(hp + (q >> 1) * 512 + (q & 1) * 256, (u32x2){cvtpk(v[0] * bflo(z[i][q][0]), v[1] * bfhi(z[i][q][0])), cvtpk(v[2] * bflo(z[i][q][1]), v[3] * bfhi(z[i][q][1]))});
            }
        }
        asm volatile("s_waitcnt vmcnt(0) lgkmcnt(0)\n\ts_barrier" ::: "memory");
        if (tid == 0) __hip_atomic_fetch_add(hcnt, 1u, RLX_AGENT);
    }
    const int kh = fh;
    const int kr = 8 * (g >> 1) + qq;
    unsigned wof[2][2];
#pragma unroll
    for (int n = 0; n < 2; ++n) { wof[n][0] = (unsigned)((64 * kh + kr) * 128 + ((n ^ ((kr >> 1) & 1)) * 64) + (g & 1) * 32 + pp * 8); wof[n][1] = wof[n][0] + 512; }
    auto gemm = [&](const __amdgpu_buffer_rsrc_t tr, auto NST, bool bars, f32x16& c0, f32x16& c1) __attribute__((always_inline)) {
        constexpr int N = decltype(NST)::value;
        int lo16 = lane * 16; asm volatile("" : "+v"(lo16));
        u32x4 tf[4][4];
#pragma unroll
        for (int st = 0; st < 4; ++st)
#pragma unroll
            for (int i = 0; i < 4; ++i) tf[st][i] = __builtin_amdgcn_raw_buffer_load_b128(tr, lo16 + (st * 8 + 4 * kh + i) * 1024, 0, 16);
        SBAR();
#pragma unroll
        for (int st = 0; st < N; ++st) {
            if (bars && st > 0) LBAR();
            const lds_cptr sb = (lds_cptr)lds + (st & 3) * W_STAGE;
#pragma unroll
            for (int i = 0; i < 4; ++i) {
                const bf16x8 w0 = cat(ldstr(sb + wof[0][0] + i * 2048), ldstr(sb + wof[0][1] + i * 2048)), w1 = cat(ldstr(sb + wof[1][0] + i * 2048), ldstr(sb + wof[1][1] + i * 2048));
                c0 = MFMA32(w0, BF(tf[st & 3][i]), c0); c1 = MFMA32(w1, BF(tf[st & 3][i]), c1);
            }
            SBAR();
            if (st + 4 < N) {
#pragma unroll
                for (int i = 0; i < 4; ++i) tf[st & 3][i] = __builtin_amdgcn_raw_buffer_load_b128(tr, lo16 + ((st + 4) * 8 + 4 * kh + i) * 1024, 0, 16);
                SBAR(); }
        }
    };
    auto merge = [&](f32x16& c0, f32x16& c1) __attribute__((always_inline)) {
        f32x4* dst = (f32x4*)(lds + G_RED + (wid ^ 2) * 4096) + lane;
#pragma unroll
        for (int q = 0; q < 4; ++q) dst[q * 64] = kh ? (f32x4){c0[4 * q], c0[4 * q + 1], c0[4 * q + 2], c0[4 * q + 3]} : (f32x4){c1[4 * q], c1[4 * q + 1], c1[4 * q + 2], c1[4 * q + 3]};
    };
    f32x16 gb0 = {}, gb1 = {};
    LBAR();
    gemm(__builtin_amdgcn_make_buffer_rsrc((void*)(ws + WS_H + (size_t)(tt * 2 + th) * 57344), 0, 57344, 0x00020000), std::integral_constant<int, 7>{}, true, gb0, gb1);
    merge(gb0, gb1);
    LBAR();
    { char* sc = lds + G_RED + wid * 4096;
      float gv[16];
#pragma unroll
      for (int q = 0; q < 4; ++q) { const f32x4 o4 = ((const f32x4*)sc)[q * 64 + lane];
#pragma unroll
          for (int r = 0; r < 4; ++r) { const float bb = (kh ? gb1[4 * q + r] : gb0[4 * q + r]) + o4[r]; gv[4 * q + r] = bb * __builtin_amdgcn_rcpf(1.0f + EX(-LOG2E * bb)); } }
      asm volatile("s_waitcnt lgkmcnt(0)" ::: "memory");
#pragma unroll
      for (int u = 0; u < 2; ++u) ((u32x4*)sc)[u * 64 + lane] = (u32x4){cvtpk(gv[8 * u], gv[8 * u + 1]), cvtpk(gv[8 * u + 2], gv[8 * u + 3]), cvtpk(gv[8 * u + 4], gv[8 * u + 5]), cvtpk(gv[8 * u + 6], gv[8 * u + 7])}; }
    const int n0 = ct * 64 + kh * 32 + 4 * hi;
    u32x2 xv[4];
#pragma unroll
    for (int q = 0; q < 4; ++q) xv[q] = *(const u32x2*)(x + (size_t)t * D + n0 + 8 * q);
    PIN(xv[0]); PIN(xv[1]); PIN(xv[2]); PIN(xv[3]);
    LBAR();
    f32x16 s0 = {}, s1 = {};
    {
        const __amdgpu_buffer_rsrc_t tr = __builtin_amdgcn_make_buffer_rsrc((void*)(ws + WS_O + (size_t)(tt * 2 + th) * 32768), 0, 32768, 0x00020000);
        int lo16 = lane * 16; asm volatile("" : "+v"(lo16));
        u32x4 tf[16];
#pragma unroll
        for (int i = 0; i < 16; ++i) tf[i] = __builtin_amdgcn_raw_buffer_load_b128(tr, lo16 + ((i >> 2) * 8 + 4 * kh + (i & 3)) * 1024, 0, 16);
        SBAR();
        bf16x8 w0[2], w1[2];
        w0[0] = cat(ldstr((lds_cptr)lds + wof[0][0]), ldstr((lds_cptr)lds + wof[0][1])); w1[0] = cat(ldstr((lds_cptr)lds + wof[1][0]), ldstr((lds_cptr)lds + wof[1][1]));
#pragma unroll
        for (int i = 0; i < 16; ++i) {
            if (i + 1 < 16) { const lds_cptr sb = (lds_cptr)lds + ((i + 1) >> 2) * W_STAGE + ((i + 1) & 3) * 2048;
                w0[(i + 1) & 1] = cat(ldstr(sb + wof[0][0]), ldstr(sb + wof[0][1])); w1[(i + 1) & 1] = cat(ldstr(sb + wof[1][0]), ldstr(sb + wof[1][1])); }
            s0 = MFMA32(w0[i & 1], BF(tf[i]), s0); s1 = MFMA32(w1[i & 1], BF(tf[i]), s1);
        }
    }
    u32x4 gq[2];
    { const u32x4* gt = (const u32x4*)(lds + G_RED + wid * 4096) + lane; gq[0] = gt[0]; gq[1] = gt[64]; }
    LBAR();
    { f32x4* dst = (f32x4*)(lds + (wid ^ 2) * 4096) + lane;
#pragma unroll
      for (int q = 0; q < 4; ++q) dst[q * 64] = kh ? (f32x4){s0[4 * q], s0[4 * q + 1], s0[4 * q + 2], s0[4 * q + 3]} : (f32x4){s1[4 * q], s1[4 * q + 1], s1[4 * q + 2], s1[4 * q + 3]}; }
    LBAR();
    { const f32x4* src = (const f32x4*)(lds + wid * 4096) + lane;
#pragma unroll
      for (int q = 0; q < 4; ++q) { const f32x4 o4 = src[q * 64]; float v[4]; const unsigned g0 = gq[q >> 1][2 * (q & 1)], g1 = gq[q >> 1][2 * (q & 1) + 1]; const float gg[4] = {bflo(g0), bfhi(g0), bflo(g1), bfhi(g1)};
#pragma unroll
          for (int r = 0; r < 4; ++r) v[r] = ((kh ? s1[4 * q + r] : s0[4 * q + r]) + o4[r]) * gg[r];
          *(u32x2*)(out + (size_t)t * D + n0 + 8 * q) = (u32x2){cvtpk(bflo(xv[q][0]) + v[0], bfhi(xv[q][0]) + v[1]), cvtpk(bflo(xv[q][1]) + v[2], bfhi(xv[q][1]) + v[3])}; } }
}

__global__ __launch_bounds__(512, 4) void k2_mix(const u16* __restrict__ x, const u16* __restrict__ wo, const u16* __restrict__ wsp, const u16* __restrict__ bsp, const u16* __restrict__ wout, char* __restrict__ ws, u16* __restrict__ out) {
    extern __shared__ __attribute__((aligned(16))) char lds[];
    if (blockIdx.x < 256) attn_role(blockIdx.x, ws, lds);
    if (blockIdx.x >= 256) gate_role(blockIdx.x - 256, x, wo, wsp, bsp, wout, ws, out, lds);
}

extern "C" void kernel_launch(void* const* d_in, const int* in_sizes, int n_in, void* d_out, int out_size, void* d_ws, size_t ws_size, hipStream_t stream) {
    const u16* x = (const u16*)d_in[0]; const u16* wqkv = (const u16*)d_in[1]; const u16* wo = (const u16*)d_in[2];
    const u16* win = (const u16*)d_in[4]; const u16* wsp = (const u16*)d_in[5]; const u16* bsp = (const u16*)d_in[6]; const u16* wout = (const u16*)d_in[7];
    char* ws = (char*)d_ws;
    (void)hipFuncSetAttribute((const void*)k1_proj, hipFuncAttributeMaxDynamicSharedMemorySize, K1_LDS);
    (void)hipFuncSetAttribute((const void*)k2_mix, hipFuncAttributeMaxDynamicSharedMemorySize, K2_LDS);
    k1_proj<<<256, 768, K1_LDS, stream>>>(x, wqkv, win, ws);
    k2_mix<<<512, 512, K2_LDS, stream>>>(x, wo, wsp, bsp, wout, ws, (u16*)d_out);
}
```

```cpp
#include <hip/hip_runtime.h>
#include <hip/hip_bf16.h>
#include <stdint.h>
#include <type_traits>

typedef unsigned short u16;
using bf16x8 = __attribute__((ext_vector_type(8))) short;
using s16x4 = __attribute__((ext_vector_type(4))) short;
using f32x16 = __attribute__((ext_vector_type(16))) float;
using f32x4 = __attribute__((ext_vector_type(4))) float;
using u32x4 = __attribute__((ext_vector_type(4))) unsigned;
using u32x2 = __attribute__((ext_vector_type(2))) unsigned;
typedef __bf16 bf16x2_t __attribute__((ext_vector_type(2)));
typedef float f32x2_t __attribute__((ext_vector_type(2)));
typedef short v4i16_t __attribute__((ext_vector_type(4)));
#define LDSAS __attribute__((address_space(3)))
typedef LDSAS const char* lds_cptr;
#define MFMA32(a, b, c) __builtin_amdgcn_mfma_f32_32x32x16_bf16(a, b, c, 0, 0, 0)
#define SBAR() __builtin_amdgcn_sched_barrier(0)
#define PIN(x) asm volatile("" : "+v"(x))
#define WAIT_BAR(N) asm volatile("s_waitcnt vmcnt(" #N ") lgkmcnt(0)\n\ts_barrier" ::: "memory")
#define LBAR() asm volatile("s_waitcnt lgkmcnt(0)\n\ts_barrier" ::: "memory")
#define DI __device__ __forceinline__
#define ST8(p, w) st8_wt((void*)(p), (w))
#define ST16(off, v) __builtin_amdgcn_raw_buffer_store_b128((v), wsr, (off), 0, 16)

constexpr int S = 2048, D = 512, H = 32, DH = 16, FH = 896;
constexpr float LOG2E = 1.4426950408889634f;

constexpr size_t WS_Q = 0, WS_K = 2u << 20, WS_V = 4u << 20, WS_Z1 = 6u << 20, WS_Z2N = 14u << 20, WS_H = 18u << 20, WS_O = 22u << 20, WS_KMAX = 24u << 20, WS_EPOCH = 25u << 20, WS_XCH = 26u << 20, WS_XST = 27u << 20, WS_CNT = 28u << 20;

DI unsigned cvtpk(float lo, float hi) { f32x2_t v = {lo, hi}; return __builtin_bit_cast(unsigned, __builtin_convertvector(v, bf16x2_t)); }
DI float bflo(unsigned w) { return __uint_as_float(w << 16); }
DI float bfhi(unsigned w) { return __uint_as_float(w & 0xFFFF0000u); }
DI void glds16s(unsigned voff, const void* sbase, unsigned lds_base) {
    lds_base = __builtin_amdgcn_readfirstlane(lds_base);
    unsigned sv; asm volatile("s_mov_b32 %0, m0\n\ts_mov_b32 m0, %3\n\ts_nop 0\n\tglobal_load_lds_dwordx4 %1, %2\n\ts_mov_b32 m0, %0" : "=&s"(sv) : "v"(voff), "s"(sbase), "s"(lds_base) : "memory"); }
DI bf16x8 ldsr128(lds_cptr p) { return *(const LDSAS bf16x8*)p; }
DI s16x4 ldstr(lds_cptr p) { return __builtin_bit_cast(s16x4, __builtin_amdgcn_ds_read_tr16_b64_v4i16((LDSAS v4i16_t*)p)); }
DI bf16x8 cat(s16x4 a, s16x4 b) { return __builtin_shufflevector(a, b, 0, 1, 2, 3, 4, 5, 6, 7); }
DI float swap32(float x, bool sum) { auto rr = __builtin_amdgcn_permlane32_swap(__float_as_uint(x), __float_as_uint(x), false, false); return sum ? __uint_as_float(rr[0]) + __uint_as_float(rr[1]) : __uint_as_float(rr[0]); }
DI float gelu_t(float x) {
    const float c1 = -2.0f * 0.7978845608028654f * LOG2E, c3 = c1 * 0.044715f;
    const float u = x * fmaf(x * x, c3, c1);
    return x * __builtin_amdgcn_rcpf(1.0f + __builtin_amdgcn_exp2f(u));
}

constexpr int K1_XB = 16384;
constexpr int K1_STAGE = K1_XB + 7 * 4096;
constexpr int K1_CS = 3 * K1_STAGE;
constexpr int K1_LDS = K1_CS + 1024;
typedef __attribute__((address_space(1))) unsigned gu32; typedef __attribute__((address_space(1))) unsigned long long gu64;
#define RLX_AGENT __ATOMIC_RELAXED, __HIP_MEMORY_SCOPE_AGENT

DI float xor16(float x) { auto r = __builtin_amdgcn_permlane16_swap(__float_as_uint(x), __float_as_uint(x), false, false); return __uint_as_float(r[0]) + __uint_as_float(r[1]); }
DI float xor32(float x) { auto r = __builtin_amdgcn_permlane32_swap(__float_as_uint(x), __float_as_uint(x), false, false); return __uint_as_float(r[0]) + __uint_as_float(r[1]); }
DI float max16(float x) { auto r = __builtin_amdgcn_permlane16_swap(__float_as_uint(x), __float_as_uint(x), false, false); return fmaxf(__uint_as_float(r[0]), __uint_as_float(r[1])); }
DI float max32(float x) { auto r = __builtin_amdgcn_permlane32_swap(__float_as_uint(x), __float_as_uint(x), false, false); return fmaxf(__uint_as_float(r[0]), __uint_as_float(r[1])); }
template <int CTRL> DI float dppf(float x) { return __uint_as_float(__builtin_amdgcn_update_dpp(0, __float_as_uint(x), CTRL, 0xF, 0xF, false)); }
DI float rowmax16(float x) { x = fmaxf(x, dppf<0x128>(x)); x = fmaxf(x, dppf<0x124>(x)); x = fmaxf(x, dppf<0x122>(x)); x = fmaxf(x, dppf<0x121>(x)); return x; }
DI float wavesum(float x) { x += dppf<0x128>(x); x += dppf<0x124>(x); x += dppf<0x122>(x); x += dppf<0x121>(x); return xor32(xor16(x)); }
DI u32x4 widen(u32x2 a, u32x2 b) { auto r0 = __builtin_amdgcn_permlane32_swap(a[0], b[0], false, false); auto r1 = __builtin_amdgcn_permlane32_swap(a[1], b[1], false, false); return (u32x4){r0[0], r1[0], r0[1], r1[1]}; }

DI void gather8(u32x4 (&p)[8], unsigned voff, const void* sbase) {
    asm volatile("global_load_dwordx4 %0, %8, %9 sc1\n\tglobal_load_dwordx4 %1, %8, %9 offset:2048 sc1\n\tglobal_load_dwordx4 %2, %10, %9 sc1\n\tglobal_load_dwordx4 %3, %10, %9 offset:2048 sc1\n\t"
                 "global_load_dwordx4 %4, %11, %9 sc1\n\tglobal_load_dwordx4 %5, %11, %9 offset:2048 sc1\n\tglobal_load_dwordx4 %6, %12, %9 sc1\n\tglobal_load_dwordx4 %7, %12, %9 offset:2048 sc1\n\ts_waitcnt vmcnt(0)"
                 : "=&v"(p[0]), "=&v"(p[1]), "=&v"(p[2]), "=&v"(p[3]), "=&v"(p[4]), "=&v"(p[5]), "=&v"(p[6]), "=&v"(p[7])
                 : "v"(voff), "s"(sbase), "v"(voff + 4096u), "v"(voff + 8192u), "v"(voff + 12288u) : "memory"); }
DI void st8_wt(void* p, u32x2 w) { __hip_atomic_store((gu64*)p, ((unsigned long long)w[1] << 32) | w[0], RLX_AGENT); }
__global__ __launch_bounds__(768, 3) void k1_proj(const u16* __restrict__ x, const u16* __restrict__ wqkv, const u16* __restrict__ win, const u16* wsp, const u16* wout, const u16* wo, const u16* outp, char* __restrict__ ws) {
    extern __shared__ __attribute__((aligned(16))) char lds[];
    const int tid = threadIdx.x, lane = tid & 63, r32 = lane & 31, hi = lane >> 5;
    const int wid = __builtin_amdgcn_readfirstlane(tid >> 6);
    const int b = blockIdx.x, xcd = b & 7, idx = b >> 3;
    const int panel = 8 * (xcd >> 2) + (idx & 7), j = 4 * (xcd & 3) + (idx >> 3);
    const unsigned lds0 = (unsigned)(uintptr_t)lds;
    const int jq = j & 3, u7 = 7 * (j >> 2);
    const int n1 = jq == 3 ? 1 : 2, n2 = jq == 1 ? 1 : 2;
    const int z1t0 = u7 + 2 * jq, z2t0 = u7 + (jq == 0 ? 0 : (jq == 1 ? 2 : (jq == 2 ? 3 : 5)));
    if (b == 0 && tid < 40) ((unsigned*)(ws + WS_CNT))[tid * 64] = 0u;
    if (wid >= 8) {
        const int l = wid - 8;
        const char* xb = (const char*)x + (size_t)(panel * 128 + l * 32) * 1024;
        const unsigned xv0 = (unsigned)((lane >> 3) * 1024 + (((lane & 7) ^ (lane >> 4)) * 16)), xv1 = (unsigned)((lane >> 3) * 1024 + (((lane & 7) ^ (4 + (lane >> 4))) * 16));
        const unsigned vq = (unsigned)((16 * l + (lane >> 2)) * 3072 + (lane & 3) * 16), vz = (unsigned)((16 * l + (lane >> 2)) * 3584 + (lane & 3) * 16);
        const char* wq = (const char*)wqkv + j * 64;
        int zc[4];
#pragma unroll
        for (int i = 0; i < 4; ++i) { const int ii = i < n1 + n2 ? i : n1 + n2 - 1; zc[i] = ii < n1 ? (z1t0 + ii) * 64 : FH * 2 + (z2t0 + ii - n1) * 64; }
        auto dma = [&](int st) __attribute__((always_inline)) {
            const unsigned d = lds0 + (st % 3) * K1_STAGE;
#pragma unroll
            for (int p = 0; p < 4; ++p) glds16s((p & 1) ? xv1 : xv0, xb + st * 128 + p * 8192, d + l * 4096 + p * 1024);
            const char* bq = wq + (size_t)st * 64 * 3072; const char* bz = (const char*)win + (size_t)st * 64 * 3584; const unsigned dw = d + K1_XB + l * 1024;
            glds16s(vq, bq + 2048, dw); glds16s(vq, bq, dw + 4096); glds16s(vq, bq + 1024, dw + 8192);
#pragma unroll
            for (int i = 0; i < 4; ++i) glds16s(vz, bz + zc[i], dw + (3 + i) * 4096);
        };
        dma(0);
        WAIT_BAR(0);
        dma(1); dma(2);
#pragma unroll
        for (int st = 1; st < 8; ++st) {
            if (st < 7) WAIT_BAR(11); else WAIT_BAR(0);
            if (st + 2 < 8) dma(st + 2);
        }
        return;
    }
    const int mw = wid & 3, nh = wid >> 2;
    if (wid == 7) {
        const char* tp = ws + (size_t)lane * (512u << 10);
        if (lane >= 58) tp = (const char*)wsp + (size_t)((lane - 58) * 4 + (idx & 3)) * (360u << 10);
        if (lane == 56) tp = (const char*)wout; if (lane == 57) tp = (const char*)wo; if (lane == 55) tp = (const char*)outp; if (lane == 54) tp = (const char*)outp + (1u << 20);
        const unsigned tv = *(const volatile unsigned*)tp;
        asm volatile("" :: "v"(tv));
    }
    gu32* epw = (gu32*)(ws + WS_EPOCH) + panel * 64;
    unsigned tag = __hip_atomic_load(epw, RLX_AGENT);
    u32x4 xr = *(const u32x4*)(x + (size_t)(panel * 128 + 8 * j + wid) * D + lane * 8);
    auto rowstats = [&]() __attribute__((always_inline)) {
        PIN(xr); PIN(tag); tag += 1u;
        float v[8];
#pragma unroll
        for (int i = 0; i < 4; ++i) { v[2 * i] = bflo(xr[i]); v[2 * i + 1] = bfhi(xr[i]); }
        const float m = wavesum(((v[0] + v[1]) + (v[2] + v[3])) + ((v[4] + v[5]) + (v[6] + v[7]))) * (1.0f / 512.0f);
        float q = 0.f;
#pragma unroll
        for (int i = 0; i < 8; ++i) q = fmaf(v[i] - m, v[i] - m, q);
        const float r = __builtin_amdgcn_rsqf(wavesum(q) * (1.0f / 512.0f) + 1e-5f);
        if (lane < 2) __hip_atomic_store((gu64*)(ws + WS_XST) + (size_t)(panel * 128 + 8 * j + wid) * 2 + lane, ((unsigned long long)tag << 32) | __float_as_uint(lane ? r : m), RLX_AGENT);
    };
    f32x16 acc[4];
#pragma unroll
    for (int s = 0; s < 4; ++s) acc[s] = (f32x16){};
    float cs0 = 0.f, cs1 = 0.f;
    const int g = lane >> 4, qq = (lane & 15) >> 2, pp = lane & 3;
    const int trow = 32 * mw + r32;
    unsigned xo[4];
#pragma unroll
    for (int ks = 0; ks < 4; ++ks) xo[ks] = (unsigned)(trow * 128 + (((2 * ks + hi) ^ ((trow >> 1) & 7)) * 16));
    const unsigned wlane = (unsigned)(K1_XB + (8 * (g >> 1) + qq) * 64 + (g & 1) * 32 + pp * 8);
    const __amdgpu_buffer_rsrc_t sr = __builtin_amdgcn_make_buffer_rsrc((void*)(ws + WS_XST + (size_t)panel * 128 * 16), 0, 128 * 16, 0x00020000);
    u32x4 pst;
    int tile[4];
    auto mainloop = [&](auto NHC) __attribute__((always_inline)) {
        constexpr int NHV = decltype(NHC)::value; constexpr int NS = NHV == 0 ? 4 : 3;
        unsigned wo[NS];
#pragma unroll
        for (int s = 0; s < NS; ++s) { int t = s + mw; t = t >= NS ? t - NS : t; t = t >= NS ? t - NS : t; tile[s] = 4 * NHV + t; wo[s] = wlane + tile[s] * 4096; }
        bf16x8 xa[2], wf[2][NS];
        auto rd = [&](const int set, const lds_cptr sb, const int ks) __attribute__((always_inline)) {
            xa[set] = ldsr128(sb + xo[ks]);
#pragma unroll
            for (int s = 0; s < NS; ++s) wf[set][s] = cat(ldstr(sb + wo[s] + ks * 1024), ldstr(sb + wo[s] + ks * 1024 + 256));
        };
        auto mm = [&](const int set) __attribute__((always_inline)) {
            SBAR();
#pragma unroll
            for (int s = 0; s < NS; ++s) acc[s] = MFMA32(wf[set][s], xa[set], acc[s]);
            const u32x4 w = __builtin_bit_cast(u32x4, wf[set][0]);
#pragma unroll
            for (int i = 0; i < 4; ++i) { cs0 += bflo(w[i]); cs1 += bfhi(w[i]); }
            PIN(cs0); PIN(cs1);
#pragma unroll
            for (int i = 0; i < NS; ++i) { __builtin_amdgcn_sched_group_barrier(0x008, 1, 0); __builtin_amdgcn_sched_group_barrier(0x002, NS == 4 ? 4 : 6, 0); }
            SBAR();
        };
        LBAR();
        rd(0, (lds_cptr)lds, 0);
#pragma unroll
        for (int st = 0; st < 8; ++st) {
            const lds_cptr sb = (lds_cptr)lds + (st % 3) * K1_STAGE;
            rd(1, sb, 1); mm(0);
            rd(0, sb, 2); mm(1);
            rd(1, sb, 3); mm(0);
            if (st < 7) { LBAR(); rd(0, (lds_cptr)lds + ((st + 1) % 3) * K1_STAGE, 0); }
            if (st == 1) rowstats();
            if (st == 5) pst = __builtin_amdgcn_raw_buffer_load_b128(sr, trow * 16, 0, 16);
            mm(1);
        }
    };
    if (nh == 0) mainloop(std::integral_constant<int, 0>{}); else mainloop(std::integral_constant<int, 1>{});
    float* csl = (float*)(lds + K1_CS);
    { const float c = xor32(cs0 + cs1); if (hi == 0) csl[tile[0] * 32 + r32] = c; }
    const int tok = panel * 128 + trow;
    float mu, rs;
    {   unsigned long long g0 = ((unsigned long long)pst[1] << 32) | pst[0], g1 = ((unsigned long long)pst[3] << 32) | pst[2];
        gu64* gp = (gu64*)(ws + WS_XST) + (size_t)(panel * 128 + trow) * 2;
        for (unsigned spins = 0; !__all(((unsigned)(g0 >> 32) == tag) & ((unsigned)(g1 >> 32) == tag)) && spins < 200000u; ++spins) {
            __builtin_amdgcn_s_sleep(1);
            g0 = __hip_atomic_load(gp, RLX_AGENT); g1 = __hip_atomic_load(gp + 1, RLX_AGENT);
        }
        mu = __uint_as_float((unsigned)g0); rs = __uint_as_float((unsigned)g1); }
    LBAR();
    auto lnv = [&](int s, float (&v)[16], float sc) __attribute__((always_inline)) {
#pragma unroll
        for (int q = 0; q < 4; ++q) { const f32x4 c = *(const f32x4*)(csl + tile[s] * 32 + 8 * q + 4 * hi);
#pragma unroll
            for (int r = 0; r < 4; ++r) v[4 * q + r] = sc * (acc[s][4 * q + r] - mu * c[r]); } };
    const __amdgpu_buffer_rsrc_t wsr = __builtin_amdgcn_make_buffer_rsrc((void*)ws, 0, 32u << 20, 0x00020000);
    u16* Z1 = (u16*)(ws + WS_Z1);
    auto z1store = [&](int zt, const u32x2 (&w)[4]) __attribute__((always_inline)) {
        u16* p = Z1 + ((size_t)((tok >> 5) * 28 + zt) * 1024) + (hi * 32 + r32) * 4;
#pragma unroll
        for (int q = 0; q < 4; ++q) ST8(p + q * 256, w[q]); };
    auto gelu16 = [&](int s, u32x2 (&w)[4]) __attribute__((always_inline)) { float v[16]; lnv(s, v, rs);
#pragma unroll
        for (int q = 0; q < 4; ++q) w[q] = (u32x2){cvtpk(gelu_t(v[4 * q]), gelu_t(v[4 * q + 1])), cvtpk(gelu_t(v[4 * q + 2]), gelu_t(v[4 * q + 3]))}; };
    if (nh == 0) {
#pragma unroll
        for (int s = 0; s < 4; ++s) {
            const int T = tile[s];
            if (T == 3) { u32x2 w[4]; gelu16(s, w); z1store(z1t0, w); }
            else {
                float v[16]; lnv(s, v, T == 1 ? rs * (0.25f * LOG2E) : rs);
                u32x2 w[4];
#pragma unroll
                for (int q = 0; q < 4; ++q) w[q] = (u32x2){cvtpk(v[4 * q], v[4 * q + 1]), cvtpk(v[4 * q + 2], v[4 * q + 3])};
                if (T == 0) {
                    char* sc = lds + mw * 2048;
#pragma unroll
                    for (int q = 0; q < 4; ++q) *(u32x2*)(sc + r32 * 64 + (8 * q + 4 * hi) * 2) = w[q];
                    asm volatile("s_waitcnt lgkmcnt(0)" ::: "memory");
                    u16* Vt = (u16*)(ws + WS_V);
#pragma unroll
                    for (int hh = 0; hh < 2; ++hh)
#pragma unroll
                        for (int ks = 0; ks < 2; ++ks) {
                            const s16x4 t4 = ldstr((lds_cptr)lds + mw * 2048 + (16 * ks + 8 * (g >> 1) + 4 * (g & 1) + qq) * 64 + hh * 32 + pp * 8);
                            ST8(Vt + ((size_t)(2 * j + hh) * 128 + ((panel * 128 + 32 * mw) >> 4) + ks) * 256 + (g & 1) * 128 + (lane & 15) * 8 + (g >> 1) * 4, __builtin_bit_cast(u32x2, t4)); }
                } else {
                    const unsigned dsto = (unsigned)(T == 1 ? WS_Q : WS_K);
#pragma unroll
                    for (int hh = 0; hh < 2; ++hh) ST16(dsto + (((2 * j + hh) * S + tok) * 16 + 8 * hi) * 2, widen(w[2 * hh], w[2 * hh + 1]));
                    if (T == 2) {
                        float* kmax = (float*)(ws + WS_KMAX);
#pragma unroll
                        for (int hh = 0; hh < 2; ++hh) { float n = 0.f;
#pragma unroll
                            for (int q = 0; q < 2; ++q) { const u32x2 ww = w[2 * hh + q]; n = fmaf(bflo(ww[0]), bflo(ww[0]), n); n = fmaf(bfhi(ww[0]), bfhi(ww[0]), n); n = fmaf(bflo(ww[1]), bflo(ww[1]), n); n = fmaf(bfhi(ww[1]), bfhi(ww[1]), n); }
                            n = max16(rowmax16(xor32(n)));
                            if (lane == 0) kmax[(panel * 4 + mw) * H + 2 * j + hh] = n; }
                    }
                }
            }
        }
    } else {
        __builtin_amdgcn_s_setprio(2);
        float sm = 0.f;
#pragma unroll
        for (int s = 0; s < 3; ++s) { const int zi = tile[s] - 3;
            if (zi >= n1 && zi < n1 + n2) { float v[16]; lnv(s, v, rs);
#pragma unroll
                for (int r = 0; r < 16; ++r) { acc[s][r] = gelu_t(v[r]); sm += acc[s][r]; } } }
        sm = xor32(sm);
        const float ml = sm * (n2 == 2 ? (1.0f / 64.f) : (1.0f / 32.f)); float m2 = 0.f;
#pragma unroll
        for (int s = 0; s < 3; ++s) { const int zi = tile[s] - 3;
            if (zi >= n1 && zi < n1 + n2) {
#pragma unroll
                for (int r = 0; r < 16; ++r) { const float d = acc[s][r] - ml; m2 = fmaf(d, d, m2); } } }
        m2 = xor32(m2);
        __hip_atomic_store((gu64*)(ws + WS_XCH) + ((size_t)(panel * 16 + j) * 128 + trow) * 2 + hi, ((unsigned long long)tag << 32) | __float_as_uint(hi ? m2 : sm), RLX_AGENT);
        __builtin_amdgcn_s_setprio(0);
#pragma unroll
        for (int s = 0; s < 3; ++s) { const int zi = tile[s] - 3; if (zi < n1) { u32x2 w[4]; gelu16(s, w); z1store(z1t0 + zi, w); } }
        float mean, rstd;
        {
            const char* xb = ws + WS_XCH + (size_t)panel * 16 * 128 * 16; const unsigned xo = (unsigned)((8 * hi * 128 + trow) * 16);
            u32x4 p[8];
            for (unsigned spins = 0;; ++spins) {
                bool ok = true;
                gather8(p, xo, xb);
#pragma unroll
                for (int u = 0; u < 8; ++u) ok &= (p[u][1] == tag) & (p[u][3] == tag);
                if (__all(ok) || spins > 200000u) break;
                __builtin_amdgcn_s_sleep(6);
            }
            float tot = 0.f;
#pragma unroll
            for (int u = 0; u < 8; ++u) tot += __uint_as_float(p[u][0]);
            mean = xor32(tot) * (1.0f / FH);
            float mm2 = 0.f;
#pragma unroll
            for (int u = 0; u < 8; ++u) { const float nl = (u & 3) == 1 ? 32.f : 64.f; const float d = __uint_as_float(p[u][0]) * ((u & 3) == 1 ? (1.0f / 32.f) : (1.0f / 64.f)) - mean; mm2 += __uint_as_float(p[u][2]) + nl * d * d; }
            rstd = __builtin_amdgcn_rsqf(xor32(mm2) * (1.0f / FH) + 1e-5f);
        }
        if (wid == 4 && lane == 0) __hip_atomic_store(epw, tag, RLX_AGENT);
        const float nmr = -mean * rstd;
#pragma unroll
        for (int s = 0; s < 3; ++s) { const int zi = tile[s] - 3;
            if (zi >= n1 && zi < n1 + n2) {
                const int ft = z2t0 + zi - n1;
                u32x2 w[4];
#pragma unroll
                for (int q = 0; q < 4; ++q) w[q] = (u32x2){cvtpk(fmaf(acc[s][4 * q], rstd, nmr), fmaf(acc[s][4 * q + 1], rstd, nmr)), cvtpk(fmaf(acc[s][4 * q + 2], rstd, nmr), fmaf(acc[s][4 * q + 3], rstd, nmr))};
                char* sc = lds + 8192 + mw * 2048;
#pragma unroll
                for (int q = 0; q < 4; ++q) *(u32x2*)(sc + r32 * 64 + (8 * q + 4 * hi) * 2) = w[q];
                asm volatile("s_waitcnt lgkmcnt(0)" ::: "memory");
                const int tk0 = panel * 128 + 32 * mw;
#pragma unroll
                for (int ks = 0; ks < 2; ++ks) {
                    const lds_cptr rp = (lds_cptr)lds + 8192 + mw * 2048 + (16 * ks + 8 * (g >> 1) + qq) * 64 + (g & 1) * 32 + pp * 8;
                    const bf16x8 fr = cat(ldstr(rp), ldstr(rp + 256));
                    ST16((unsigned)WS_Z2N + (unsigned)(((((ft >> 2) * 32 + (tk0 >> 6)) * 4 + (ft & 3)) * 4 + ((tk0 >> 4) & 3) + ks) * 1024 + lane * 16), __builtin_bit_cast(u32x4, fr));
                }
                asm volatile("s_waitcnt lgkmcnt(0)" ::: "memory");
            } }
    }
}

constexpr int A_KR = 0, A_VR = 32768, A_CONST = 65536, A_FLAG = A_CONST + 4096, A_MX = A_FLAG + 64, K2_LDS = 81920;
constexpr float SAFE_T = 48.f;
#define EX(x) __builtin_amdgcn_exp2f(x)
#define BF(p) __builtin_bit_cast(bf16x8, p)

DI void attn_role(int b, char* __restrict__ ws, char* lds) {
    const u16* Qs = (const u16*)(ws + WS_Q); const u16* Kb = (const u16*)(ws + WS_K); const u16* Vt = (const u16*)(ws + WS_V); const float* kmax = (const float*)(ws + WS_KMAX); u16* Ot = (u16*)(ws + WS_O);
    const int tid = threadIdx.x, lane = tid & 63, r32 = lane & 31, hi = lane >> 5;
    const int wid = __builtin_amdgcn_readfirstlane(tid >> 6);
    const int xcd = b & 7, idx = b >> 3, head = xcd * 4 + (idx & 3), qt = idx >> 2;
    const int qp = wid & 3, kh = wid >> 2;
    const unsigned lds0 = (unsigned)(uintptr_t)lds;
    const char* kg = (const char*)(Kb + (size_t)head * S * DH);
    const char* vg = (const char*)(Vt + (size_t)head * S * DH);
    const unsigned kvoff = wid * 1024 + ((lane ^ ((lane >> 5) & 1)) * 16);
    const unsigned vvoff = wid * 1024 + lane * 16;
#define DMA_CHUNK(c) do { glds16s(kvoff, kg + (c) * 8192, lds0 + A_KR + ((c) & 3) * 8192 + wid * 1024); glds16s(vvoff, vg + (c) * 8192, lds0 + A_VR + ((c) & 3) * 8192 + wid * 1024); } while (0)
    const int q0 = qt * 256 + qp * 64;
    const bf16x8 qa = *(const bf16x8*)(Qs + ((size_t)head * S + q0 + r32) * DH + hi * 8);
    const bf16x8 qb = *(const bf16x8*)(Qs + ((size_t)head * S + q0 + 32 + r32) * DH + hi * 8);
    float km = kmax[lane * H + head];
    DMA_CHUNK(0);
    if (tid < 256) { const unsigned w = (tid & 31) ? 0u : 0x3F803F80u; u32x4 v = {w, w, w, w}; *(u32x4*)(lds + A_CONST + tid * 16) = v; }
    km = max32(max16(rowmax16(km)));
    float qna = 0.f, qnb = 0.f;
#pragma unroll
    for (int j = 0; j < 8; ++j) { const float va = bflo((u16)qa[j]), vb = bflo((u16)qb[j]); qna = fmaf(va, va, qna); qnb = fmaf(vb, vb, qnb); }
    qna = xor32(qna); qnb = xor32(qnb);
    const float mh = __builtin_amdgcn_sqrtf(fmaxf(qna, qnb) * km) * 1.001f;
    { const bool unsafe = __any(!(mh <= SAFE_T)); if (lane == 0) ((unsigned*)(lds + A_FLAG))[wid] = unsafe ? 1u : 0u; }
    const lds_cptr kp0 = (lds_cptr)lds + A_KR + kh * 4096 + r32 * 32 + ((hi ^ (r32 >> 4)) * 16);
    const bool vreal = r32 < 16;
    const unsigned vlane = vreal ? (unsigned)(A_VR + kh * 4096 + hi * 256 + r32 * 16) : (unsigned)(A_CONST + (r32 == 16 ? 0 : ((r32 >= 20 && r32 < 28) ? 64 : 16)));
    const unsigned vmask = vreal ? 0xFFFFFFFFu : 0u;
    f32x16 oa = {}, ob = {};
    WAIT_BAR(0);
    bool slow;
    { const u32x4 f0 = *(const u32x4*)(lds + A_FLAG), f1 = *(const u32x4*)(lds + A_FLAG + 16); slow = __builtin_amdgcn_readfirstlane(f0[0] | f0[1] | f0[2] | f0[3] | f1[0] | f1[1] | f1[2] | f1[3]) != 0; }
    if (!slow) {
        DMA_CHUNK(1); DMA_CHUNK(2); DMA_CHUNK(3);
        f32x16 cinit;
#pragma unroll
        for (int r = 0; r < 16; ++r) cinit[r] = -mh;
        PIN(cinit);
        bf16x8 kf, v0, v1; f32x16 sA, sB; u32x4 p0, p1;
        kf = ldsr128(kp0);
        { lds_cptr vp = (lds_cptr)lds + vlane; v0 = ldsr128(vp); v1 = ldsr128(vp + 512); }
        sA = MFMA32(kf, qa, cinit);
        p1 = (u32x4){0u, 0u, 0u, 0u};
#define HSTEP(SC, SN, QN, OP, OC, PRE1, PRE2, PRE3) do { \
        SN = MFMA32(kf, QN, cinit); PIN(SN); SBAR(); \
        PRE1; \
        SC[0] = EX(SC[0]); SC[1] = EX(SC[1]); SC[2] = EX(SC[2]); SC[3] = EX(SC[3]); SC[4] = EX(SC[4]); SC[5] = EX(SC[5]); PIN(SC); SBAR(); \
        OP = MFMA32(v1, BF(p1), OP); PIN(OP); SBAR(); \
        PRE2; \
        SC[6] = EX(SC[6]); SC[7] = EX(SC[7]); \
        p0[0] = cvtpk(SC[0], SC[1]); p0[1] = cvtpk(SC[2], SC[3]); p0[2] = cvtpk(SC[4], SC[5]); p0[3] = cvtpk(SC[6], SC[7]); PIN(p0); SBAR(); \
        SC[8] = EX(SC[8]); SC[9] = EX(SC[9]); SC[10] = EX(SC[10]); PIN(SC); SBAR(); \
        OC = MFMA32(v0, BF(p0), OC); PIN(OC); SBAR(); \
        PRE3; \
        SC[11] = EX(SC[11]); SC[12] = EX(SC[12]); SC[13] = EX(SC[13]); SC[14] = EX(SC[14]); SC[15] = EX(SC[15]); \
        p1[0] = cvtpk(SC[8], SC[9]); p1[1] = cvtpk(SC[10], SC[11]); p1[2] = cvtpk(SC[12], SC[13]); p1[3] = cvtpk(SC[14], SC[15]); PIN(p1); SBAR(); \
    } while (0)
#define BLOCK(kn, vc, vco, vn, vno) do { \
        HSTEP(sA, sB, qb, ob, oa, kf = ldsr128(kn), v1 = ldsr128((vc) + (vco) + 512), (void)0); \
        HSTEP(sB, sA, qa, oa, ob, (void)0, (void)0, v0 = ldsr128((vn) + (vno))); \
    } while (0)
        for (int c = 0; c < 8; ++c) {
            const unsigned ro = (unsigned)((c & 3) * 8192), rn = (unsigned)(((c + 1) & 3) * 8192);
            const lds_cptr vp = (lds_cptr)lds + (vlane + (ro & vmask)), vpn = (lds_cptr)lds + (vlane + (rn & vmask));
            const lds_cptr kp = kp0 + ro, kpn = kp0 + rn;
            BLOCK(kp + 1024, vp, 0, vp, 1024);
            BLOCK(kp + 2048, vp, 1024, vp, 2048);
            BLOCK(kp + 3072, vp, 2048, vp, 3072);
            if (c == 0) WAIT_BAR(4); else if (c < 6) WAIT_BAR(2); else if (c == 6) WAIT_BAR(0);
            if (c >= 1 && c + 3 < 8) DMA_CHUNK(c + 3);
            BLOCK(kpn, vp, 3072, vpn, 0);
        }
        ob = MFMA32(v1, BF(p1), ob);
#undef HSTEP
#undef BLOCK
    } else {
        float ma = -3.0e38f, mb = -3.0e38f;
        for (int pass = 0; pass < 2; ++pass) {
            for (int c = 0; c < 8; ++c) {
                LBAR();
                DMA_CHUNK(c);
                WAIT_BAR(0);
                const unsigned ro = (unsigned)((c & 3) * 8192);
                const lds_cptr vp = (lds_cptr)lds + (vlane + (ro & vmask)), kp = kp0 + ro;
                for (int j = 0; j < 4; ++j) {
                    const bf16x8 kf = ldsr128(kp + j * 1024);
                    const bf16x8 v0 = ldsr128(vp + j * 1024), v1 = ldsr128(vp + j * 1024 + 512);
#pragma unroll
                    for (int u = 0; u < 2; ++u) {
                        const f32x16 sc = MFMA32(kf, u ? qb : qa, (f32x16){});
                        float& m = u ? mb : ma; f32x16& o = u ? ob : oa;
                        if (pass == 0) {
#pragma unroll
                            for (int r = 0; r < 16; ++r) m = fmaxf(m, sc[r]);
                        } else {
                            u32x4 p0, p1;
#pragma unroll
                            for (int i = 0; i < 4; ++i) { p0[i] = cvtpk(EX(sc[2 * i] - m), EX(sc[2 * i + 1] - m)); p1[i] = cvtpk(EX(sc[8 + 2 * i] - m), EX(sc[9 + 2 * i] - m)); }
                            o = MFMA32(v0, BF(p0), o); o = MFMA32(v1, BF(p1), o);
                        }
                        SBAR();
                    }
                }
            }
            if (pass == 0) {
                ma = max32(ma); mb = max32(mb);
                float* mx = (float*)(lds + A_MX);
                if (hi == 0) { mx[(wid * 2) * 32 + r32] = ma; mx[(wid * 2 + 1) * 32 + r32] = mb; }
                LBAR();
                ma = fmaxf(ma, mx[((wid ^ 4) * 2) * 32 + r32]); mb = fmaxf(mb, mx[((wid ^ 4) * 2 + 1) * 32 + r32]);
            }
        }
    }
    __syncthreads();
    int tz = threadIdx.x; asm volatile("" : "+v"(tz));
    const int lz = tz & 63;
    float* red = (float*)lds;
    { float* dst = red + (size_t)(wid ^ 4) * 9 * 64 + lz;
#pragma unroll
      for (int r = 0; r < 9; ++r) dst[r * 64] = kh ? oa[r] : ob[r]; }
    __syncthreads();
    float o[9];
    { const float* src = red + (size_t)wid * 9 * 64 + lz;
#pragma unroll
      for (int r = 0; r < 9; ++r) o[r] = (kh ? ob[r] : oa[r]) + src[r * 64]; }
    const float rl = __builtin_amdgcn_rcpf(swap32(o[8], false));
    u16* op = Ot + ((size_t)(((q0 >> 5) + kh) * H + head) * 512) + (lz & 31) * 8 + (lz >> 5) * 4;
    st8_wt(op, (u32x2){cvtpk(o[0] * rl, o[1] * rl), cvtpk(o[2] * rl, o[3] * rl)});
    st8_wt(op + 256, (u32x2){cvtpk(o[4] * rl, o[5] * rl), cvtpk(o[6] * rl, o[7] * rl)});
    asm volatile("s_waitcnt vmcnt(0)\n\ts_barrier" ::: "memory");
    if (tz == 0) __hip_atomic_fetch_add((gu32*)(ws + WS_CNT) + (32 + qt) * 64, 1u, RLX_AGENT);
#undef DMA_CHUNK
}

DI void wait_cnt(gu32* cnt, unsigned want) {
    for (unsigned spins = 0; spins < 400000u; ++spins) {
        if ((unsigned)__builtin_amdgcn_readfirstlane(__hip_atomic_load(cnt, RLX_AGENT)) >= want) break;
        __builtin_amdgcn_s_sleep(1);
    }
}


constexpr int G_STAGE = 24576;
constexpr int W_STAGE = 16384;
constexpr int G_RED = 4 * W_STAGE;
DI void gate_role(int b, const u16* __restrict__ x, const u16* __restrict__ wo, const u16* __restrict__ wsp, const u16* __restrict__ bsp, const u16* __restrict__ wout, char* __restrict__ ws, u16* __restrict__ out, char* lds) {
    const int xcd = b & 7, idx = b >> 3;
    const int tt = xcd * 4 + (idx & 3), ct = idx >> 2;
    const int tid = threadIdx.x, lane = tid & 63, r32 = lane & 31, hi = lane >> 5;
    const int wid = __builtin_amdgcn_readfirstlane(tid >> 6);
    const unsigned lds0 = (unsigned)(uintptr_t)lds;
    gu32* hcnt = (gu32*)(ws + WS_CNT) + tt * 64; gu32* ocnt = (gu32*)(ws + WS_CNT) + (32 + (tt >> 2)) * 64;
    if (wid >= 4) {
        const int l = wid - 4;
        if (ct < 7) {
            const char* ag = ws + WS_Z2N + (size_t)ct * 32 * 16384 + l * 4096;
            const char* bg = (const char*)wsp + (size_t)(tt * 64 + l * 16) * 4096;
            const unsigned avoff = lane * 16;
            const unsigned bv0 = (unsigned)((lane >> 3) * 4096 + (((lane & 7) ^ (lane >> 4)) * 16)), bv1 = (unsigned)((8 + (lane >> 3)) * 4096 + (((lane & 7) ^ (4 + (lane >> 4))) * 16));
            auto dma = [&](int kt, int slot) __attribute__((always_inline)) { const unsigned d = lds0 + slot * G_STAGE; const char* a_ = ag + (size_t)kt * 16384;
#pragma unroll
                for (int e = 0; e < 4; ++e) glds16s(avoff, a_ + e * 1024, d + l * 4096 + e * 1024);
                glds16s(bv0, bg + kt * 128, d + 16384 + l * 2048); glds16s(bv1, bg + kt * 128, d + 16384 + l * 2048 + 1024); };
            dma(0, 0);
            WAIT_BAR(0);
            dma(1, 1); dma(2, 2);
            int slot = 0;
            for (int kt = 1; kt < 32; ++kt) {
                if (kt < 31) WAIT_BAR(6); else WAIT_BAR(0);
                if (kt + 2 < 32) dma(kt + 2, slot);
                slot = slot == 2 ? 0 : slot + 1;
            }
            LBAR();
        }
        unsigned wv[4];
#pragma unroll
        for (int e = 0; e < 4; ++e) { const int row = l * 32 + e * 8 + (lane >> 3); wv[e] = (unsigned)(row * 1024 + ct * 128 + (((lane & 7) ^ (((row >> 1) & 1) << 2)) * 16)); }
        auto dmw = [&](const u16* w, int st) __attribute__((always_inline)) { const char* wg = (const char*)w + (size_t)st * 128 * 1024; const unsigned d = lds0 + (st & 3) * W_STAGE + l * 4096;
#pragma unroll
            for (int e = 0; e < 4; ++e) glds16s(wv[e], wg, d + e * 1024); };
        dmw(wout, 0); dmw(wout, 1); dmw(wout, 2); dmw(wout, 3);
        if (l == 0) { wait_cnt(hcnt, 7);
        }
        WAIT_BAR(12);
#pragma unroll
        for (int st = 1; st < 7; ++st) {
            if (st < 4) WAIT_BAR(8); else if (st == 4) WAIT_BAR(8); else if (st == 5) WAIT_BAR(4); else WAIT_BAR(0);
            if (st + 3 < 7) dmw(wout, st + 3);
        }
        LBAR();
        dmw(wo, 0); dmw(wo, 1); dmw(wo, 2); dmw(wo, 3);
        if (l == 0) { wait_cnt(ocnt, 32);
        }
        WAIT_BAR(0);
        return;
    }
    __builtin_amdgcn_s_setprio(2);
    const int g = lane >> 4, qq = (lane & 15) >> 2, pp = lane & 3;
    const int th = wid & 1, fh = wid >> 1;
    const int trow = 32 * th + r32, t = tt * 64 + trow;
    if (ct < 7) {
        f32x16 acc0 = {}, acc1 = {};
        const unsigned aoff = (unsigned)(fh * 8192 + lane * 16);
        unsigned boff[4];
#pragma unroll
        for (int ks = 0; ks < 4; ++ks) boff[ks] = (unsigned)(16384 + trow * 128 + (((2 * ks + hi) ^ ((trow >> 1) & 7)) * 16));
        bf16x8 fa0[2], fa1[2], fb[2];
        auto rd = [&](int set, lds_cptr sb, int ks) __attribute__((always_inline)) {
            fb[set] = ldsr128(sb + boff[ks]);
            fa0[set] = ldsr128(sb + aoff + ks * 1024);
            fa1[set] = ldsr128(sb + aoff + 4096 + ks * 1024); };
#define GSTEP(cur, nxt, sbn, ksn) do { SBAR(); acc0 = MFMA32(fa0[cur], fb[cur], acc0); SBAR(); rd(nxt, sbn, ksn); SBAR(); acc1 = MFMA32(fa1[cur], fb[cur], acc1); SBAR(); } while (0)
        const float bias = bflo(bsp[t]);
        u32x2 z[2][4];
#pragma unroll
        for (int i = 0; i < 2; ++i)
#pragma unroll
            for (int q = 0; q < 4; ++q) z[i][q] = *(const u32x2*)((const u16*)(ws + WS_Z1) + ((size_t)((t >> 5) * 28 + ct * 4 + 2 * fh + i) * 1024) + (hi * 32 + r32) * 4 + q * 256);
        LBAR();
        rd(0, (lds_cptr)lds, 0);
        int slot = 0;
        for (int kt = 0; kt < 32; ++kt) {
            const lds_cptr sb = (lds_cptr)lds + slot * G_STAGE;
            GSTEP(0, 1, sb, 1);
            GSTEP(1, 0, sb, 2);
            GSTEP(0, 1, sb, 3);
            slot = slot == 2 ? 0 : slot + 1;
            if (kt < 31) { LBAR(); GSTEP(1, 0, (lds_cptr)lds + slot * G_STAGE, 0); }
            else { acc0 = MFMA32(fa0[1], fb[1], acc0); acc1 = MFMA32(fa1[1], fb[1], acc1); }
        }
#pragma unroll
        for (int i = 0; i < 2; ++i) {
            u16* hp = (u16*)(ws + WS_H) + ((size_t)((t >> 5) * 56 + (ct * 4 + 2 * fh + i) * 2) * 512) + r32 * 8 + hi * 4;
#pragma unroll
            for (int q = 0; q < 4; ++q) {
                float v[4];
#pragma unroll
                for (int r = 0; r < 4; ++r) v[r] = (i ? acc1[4 * q + r] : acc0[4 * q + r]) + bias;
                st8_wt(hp + (q >> 1) * 512 + (q & 1) * 256, (u32x2){cvtpk(v[0] * bflo(z[i][q][0]), v[1] * bfhi(z[i][q][0])), cvtpk(v[2] * bflo(z[i][q][1]), v[3] * bfhi(z[i][q][1]))});
            }
        }
        asm volatile("s_waitcnt vmcnt(0) lgkmcnt(0)\n\ts_barrier" ::: "memory");
        if (tid == 0) __hip_atomic_fetch_add(hcnt, 1u, RLX_AGENT);
    }
    const int kh = fh;
    const int kr = 8 * (g >> 1) + qq;
    unsigned wof[2][2];
#pragma unroll
    for (int n = 0; n < 2; ++n) { wof[n][0] = (unsigned)((64 * kh + kr) * 128 + ((n ^ ((kr >> 1) & 1)) * 64) + (g & 1) * 32 + pp * 8); wof[n][1] = wof[n][0] + 512; }
    auto gemm = [&](const __amdgpu_buffer_rsrc_t tr, auto NST, bool bars, f32x16& c0, f32x16& c1) __attribute__((always_inline)) {
        constexpr int N = decltype(NST)::value;
        int lo16 = lane * 16; asm volatile("" : "+v"(lo16));
        u32x4 tf[4][4];
#pragma unroll
        for (int st = 0; st < 4; ++st)
#pragma unroll
            for (int i = 0; i < 4; ++i) tf[st][i] = __builtin_amdgcn_raw_buffer_load_b128(tr, lo16 + (st * 8 + 4 * kh + i) * 1024, 0, 16);
        SBAR();
#pragma unroll
        for (int st = 0; st < N; ++st) {
            if (bars && st > 0) LBAR();
            const lds_cptr sb = (lds_cptr)lds + (st & 3) * W_STAGE;
#pragma unroll
            for (int i = 0; i < 4; ++i) {
                const bf16x8 w0 = cat(ldstr(sb + wof[0][0] + i * 2048), ldstr(sb + wof[0][1] + i * 2048)), w1 = cat(ldstr(sb + wof[1][0] + i * 2048), ldstr(sb + wof[1][1] + i * 2048));
                c0 = MFMA32(w0, BF(tf[st & 3][i]), c0); c1 = MFMA32(w1, BF(tf[st & 3][i]), c1);
            }
            SBAR();
            if (st + 4 < N) {
#pragma unroll
                for (int i = 0; i < 4; ++i) tf[st & 3][i] = __builtin_amdgcn_raw_buffer_load_b128(tr, lo16 + ((st + 4) * 8 + 4 * kh + i) * 1024, 0, 16);
                SBAR(); }
        }
    };
    auto merge = [&](f32x16& c0, f32x16& c1) __attribute__((always_inline)) {
        f32x4* dst = (f32x4*)(lds + G_RED + (wid ^ 2) * 4096) + lane;
#pragma unroll
        for (int q = 0; q < 4; ++q) dst[q * 64] = kh ? (f32x4){c0[4 * q], c0[4 * q + 1], c0[4 * q + 2], c0[4 * q + 3]} : (f32x4){c1[4 * q], c1[4 * q + 1], c1[4 * q + 2], c1[4 * q + 3]};
    };
    f32x16 gb0 = {}, gb1 = {};
    LBAR();
    gemm(__builtin_amdgcn_make_buffer_rsrc((void*)(ws + WS_H + (size_t)(tt * 2 + th) * 57344), 0, 57344, 0x00020000), std::integral_constant<int, 7>{}, true, gb0, gb1);
    merge(gb0, gb1);
    LBAR();
    { char* sc = lds + G_RED + wid * 4096;
      float gv[16];
#pragma unroll
      for (int q = 0; q < 4; ++q) { const f32x4 o4 = ((const f32x4*)sc)[q * 64 + lane];
#pragma unroll
          for (int r = 0; r < 4; ++r) { const float bb = (kh ? gb1[4 * q + r] : gb0[4 * q + r]) + o4[r]; gv[4 * q + r] = bb * __builtin_amdgcn_rcpf(1.0f + EX(-LOG2E * bb)); } }
      asm volatile("s_waitcnt lgkmcnt(0)" ::: "memory");
#pragma unroll
      for (int u = 0; u < 2; ++u) ((u32x4*)sc)[u * 64 + lane] = (u32x4){cvtpk(gv[8 * u], gv[8 * u + 1]), cvtpk(gv[8 * u + 2], gv[8 * u + 3]), cvtpk(gv[8 * u + 4], gv[8 * u + 5]), cvtpk(gv[8 * u + 6], gv[8 * u + 7])}; }
    const int n0 = ct * 64 + kh * 32 + 4 * hi;
    u32x2 xv[4];
#pragma unroll
    for (int q = 0; q < 4; ++q) xv[q] = *(const u32x2*)(x + (size_t)t * D + n0 + 8 * q);
    PIN(xv[0]); PIN(xv[1]); PIN(xv[2]); PIN(xv[3]);
    LBAR();
    f32x16 s0 = {}, s1 = {};
    {
        const __amdgpu_buffer_rsrc_t tr = __builtin_amdgcn_make_buffer_rsrc((void*)(ws + WS_O + (size_t)(tt * 2 + th) * 32768), 0, 32768, 0x00020000);
        int lo16 = lane * 16; asm volatile("" : "+v"(lo16));
        u32x4 tf[16];
#pragma unroll
        for (int i = 0; i < 16; ++i) tf[i] = __builtin_amdgcn_raw_buffer_load_b128(tr, lo16 + ((i >> 2) * 8 + 4 * kh + (i & 3)) * 1024, 0, 16);
        SBAR();
        bf16x8 w0[2], w1[2];
        w0[0] = cat(ldstr((lds_cptr)lds + wof[0][0]), ldstr((lds_cptr)lds + wof[0][1])); w1[0] = cat(ldstr((lds_cptr)lds + wof[1][0]), ldstr((lds_cptr)lds + wof[1][1]));
#pragma unroll
        for (int i = 0; i < 16; ++i) {
            if (i + 1 < 16) { const lds_cptr sb = (lds_cptr)lds + ((i + 1) >> 2) * W_STAGE + ((i + 1) & 3) * 2048;
                w0[(i + 1) & 1] = cat(ldstr(sb + wof[0][0]), ldstr(sb + wof[0][1])); w1[(i + 1) & 1] = cat(ldstr(sb + wof[1][0]), ldstr(sb + wof[1][1])); }
            s0 = MFMA32(w0[i & 1], BF(tf[i]), s0); s1 = MFMA32(w1[i & 1], BF(tf[i]), s1);
        }
    }
    u32x4 gq[2];
    { const u32x4* gt = (const u32x4*)(lds + G_RED + wid * 4096) + lane; gq[0] = gt[0]; gq[1] = gt[64]; }
    LBAR();
    { f32x4* dst = (f32x4*)(lds + (wid ^ 2) * 4096) + lane;
#pragma unroll
      for (int q = 0; q < 4; ++q) dst[q * 64] = kh ? (f32x4){s0[4 * q], s0[4 * q + 1], s0[4 * q + 2], s0[4 * q + 3]} : (f32x4){s1[4 * q], s1[4 * q + 1], s1[4 * q + 2], s1[4 * q + 3]}; }
    LBAR();
    { const f32x4* src = (const f32x4*)(lds + wid * 4096) + lane;
#pragma unroll
      for (int q = 0; q < 4; ++q) { const f32x4 o4 = src[q * 64]; float v[4]; const unsigned g0 = gq[q >> 1][2 * (q & 1)], g1 = gq[q >> 1][2 * (q & 1) + 1]; const float gg[4] = {bflo(g0), bfhi(g0), bflo(g1), bfhi(g1)};
#pragma unroll
          for (int r = 0; r < 4; ++r) v[r] = ((kh ? s1[4 * q + r] : s0[4 * q + r]) + o4[r]) * gg[r];
          *(u32x2*)(out + (size_t)t * D + n0 + 8 * q) = (u32x2){cvtpk(bflo(xv[q][0]) + v[0], bfhi(xv[q][0]) + v[1]), cvtpk(bflo(xv[q][1]) + v[2], bfhi(xv[q][1]) + v[3])}; } }
}

__global__ __launch_bounds__(512, 4) void k2_mix(const u16* __restrict__ x, const u16* __restrict__ wo, const u16* __restrict__ wsp, const u16* __restrict__ bsp, const u16* __restrict__ wout, char* __restrict__ ws, u16* __restrict__ out) {
    extern __shared__ __attribute__((aligned(16))) char lds[];
    if (blockIdx.x < 256) attn_role(blockIdx.x, ws, lds);
    if (blockIdx.x >= 256) gate_role(blockIdx.x - 256, x, wo, wsp, bsp, wout, ws, out, lds);
}

extern "C" void kernel_launch(void* const* d_in, const int* in_sizes, int n_in, void* d_out, int out_size, void* d_ws, size_t ws_size, hipStream_t stream) {
    const u16* x = (const u16*)d_in[0]; const u16* wqkv = (const u16*)d_in[1]; const u16* wo = (const u16*)d_in[2];
    const u16* win = (const u16*)d_in[4]; const u16* wsp = (const u16*)d_in[5]; const u16* bsp = (const u16*)d_in[6]; const u16* wout = (const u16*)d_in[7];
    char* ws = (char*)d_ws;
    (void)hipFuncSetAttribute((const void*)k1_proj, hipFuncAttributeMaxDynamicSharedMemorySize, K1_LDS);
    (void)hipFuncSetAttribute((const void*)k2_mix, hipFuncAttributeMaxDynamicSharedMemorySize, K2_LDS);
    k1_proj<<<256, 768, K1_LDS, stream>>>(x, wqkv, win, wsp, wout, wo, (const u16*)d_out, ws);
    k2_mix<<<512, 512, K2_LDS, stream>>>(x, wo, wsp, bsp, wout, ws, (u16*)d_out);
}
```

```cpp
#include <hip/hip_runtime.h>
#include <hip/hip_bf16.h>
#include <stdint.h>
#include <type_traits>

typedef unsigned short u16;
using bf16x8 = __attribute__((ext_vector_type(8))) short;
using s16x4 = __attribute__((ext_vector_type(4))) short;
using f32x16 = __attribute__((ext_vector_type(16))) float;
using f32x4 = __attribute__((ext_vector_type(4))) float;
using u32x4 = __attribute__((ext_vector_type(4))) unsigned;
using u32x2 = __attribute__((ext_vector_type(2))) unsigned;
typedef __bf16 bf16x2_t __attribute__((ext_vector_type(2)));
typedef float f32x2_t __attribute__((ext_vector_type(2)));
typedef short v4i16_t __attribute__((ext_vector_type(4)));
#define LDSAS __attribute__((address_space(3)))
typedef LDSAS const char* lds_cptr;
#define MFMA32(a, b, c) __builtin_amdgcn_mfma_f32_32x32x16_bf16(a, b, c, 0, 0, 0)
#define MFMA16(a, b, c) __builtin_amdgcn_mfma_f32_16x16x32_bf16(a, b, c, 0, 0, 0)
#define SBAR() __builtin_amdgcn_sched_barrier(0)
#define PIN(x) asm volatile("" : "+v"(x))
#define WAIT_BAR(N) asm volatile("s_waitcnt vmcnt(" #N ") lgkmcnt(0)\n\ts_barrier" ::: "memory")
#define LBAR() asm volatile("s_waitcnt lgkmcnt(0)\n\ts_barrier" ::: "memory")
#define DI __device__ __forceinline__
#define ST8(p, w) st8_wt((void*)(p), (w))
#define ST16(off, v) __builtin_amdgcn_raw_buffer_store_b128((v), wsr, (off), 0, 16)
#define STAMP(kern, i) do { } while (0)

constexpr int S = 2048, D = 512, H = 32, DH = 16, FH = 896;
constexpr float LOG2E = 1.4426950408889634f;

constexpr size_t WS_Q = 0, WS_K = 2u << 20, WS_V = 4u << 20, WS_Z1 = 6u << 20, WS_Z2R = 10u << 20, WS_Z2N = 14u << 20, WS_H = 18u << 20, WS_O = 22u << 20, WS_KMAX = 24u << 20, WS_EPOCH = 25u << 20, WS_XCH = 26u << 20, WS_XST = 27u << 20, WS_CNT = 28u << 20;

DI unsigned cvtpk(float lo, float hi) { f32x2_t v = {lo, hi}; return __builtin_bit_cast(unsigned, __builtin_convertvector(v, bf16x2_t)); }
DI float bflo(unsigned w) { return __uint_as_float(w << 16); }
DI float bfhi(unsigned w) { return __uint_as_float(w & 0xFFFF0000u); }
DI float dot2(unsigned a, unsigned b, float c) { return __builtin_amdgcn_fdot2_f32_bf16(__builtin_bit_cast(bf16x2_t, a), __builtin_bit_cast(bf16x2_t, b), c, false); }
DI void glds16s(unsigned voff, const void* sbase, unsigned lds_base) {
    lds_base = __builtin_amdgcn_readfirstlane(lds_base);
    unsigned sv; asm volatile("s_mov_b32 %0, m0\n\ts_mov_b32 m0, %3\n\ts_nop 0\n\tglobal_load_lds_dwordx4 %1, %2\n\ts_mov_b32 m0, %0" : "=&s"(sv) : "v"(voff), "s"(sbase), "s"(lds_base) : "memory"); }
DI void glds4s(unsigned voff, const void* sbase, unsigned lds_base) {
    lds_base = __builtin_amdgcn_readfirstlane(lds_base);
    unsigned sv; asm volatile("s_mov_b32 %0, m0\n\ts_mov_b32 m0, %3\n\ts_nop 0\n\tglobal_load_lds_dword %1, %2\n\ts_mov_b32 m0, %0" : "=&s"(sv) : "v"(voff), "s"(sbase), "s"(lds_base) : "memory"); }
DI bf16x8 ldsr128(lds_cptr p) { return *(const LDSAS bf16x8*)p; }
DI s16x4 ldstr(lds_cptr p) { return __builtin_bit_cast(s16x4, __builtin_amdgcn_ds_read_tr16_b64_v4i16((LDSAS v4i16_t*)p)); }
DI bf16x8 cat(s16x4 a, s16x4 b) { return __builtin_shufflevector(a, b, 0, 1, 2, 3, 4, 5, 6, 7); }
DI float swap32(float x, bool sum) { auto rr = __builtin_amdgcn_permlane32_swap(__float_as_uint(x), __float_as_uint(x), false, false); return sum ? __uint_as_float(rr[0]) + __uint_as_float(rr[1]) : __uint_as_float(rr[0]); }
DI float gelu_t(float x) {
    const float c1 = -2.0f * 0.7978845608028654f * LOG2E, c3 = c1 * 0.044715f;
    const float u = x * fmaf(x * x, c3, c1);
    return x * __builtin_amdgcn_rcpf(1.0f + __builtin_amdgcn_exp2f(u));
}

constexpr int K1_XB = 16384;
constexpr int K1_STAGE = K1_XB + 7 * 4096;
constexpr int K1_CS = 3 * K1_STAGE;
constexpr int K1_PF = K1_CS + 1024;
constexpr int K1_LDS = K1_PF + 1024;
typedef __attribute__((address_space(1))) unsigned gu32; typedef __attribute__((address_space(1))) unsigned long long gu64;
#define RLX_AGENT __ATOMIC_RELAXED, __HIP_MEMORY_SCOPE_AGENT

DI float xor16(float x) { auto r = __builtin_amdgcn_permlane16_swap(__float_as_uint(x), __float_as_uint(x), false, false); return __uint_as_float(r[0]) + __uint_as_float(r[1]); }
DI float xor32(float x) { auto r = __builtin_amdgcn_permlane32_swap(__float_as_uint(x), __float_as_uint(x), false, false); return __uint_as_float(r[0]) + __uint_as_float(r[1]); }
DI float max16(float x) { auto r = __builtin_amdgcn_permlane16_swap(__float_as_uint(x), __float_as_uint(x), false, false); return fmaxf(__uint_as_float(r[0]), __uint_as_float(r[1])); }
DI float max32(float x) { auto r = __builtin_amdgcn_permlane32_swap(__float_as_uint(x), __float_as_uint(x), false, false); return fmaxf(__uint_as_float(r[0]), __uint_as_float(r[1])); }
template <int CTRL> DI float dppf(float x) { return __uint_as_float(__builtin_amdgcn_update_dpp(0, __float_as_uint(x), CTRL, 0xF, 0xF, false)); }
DI float rowmax16(float x) { x = fmaxf(x, dppf<0x128>(x)); x = fmaxf(x, dppf<0x124>(x)); x = fmaxf(x, dppf<0x122>(x)); x = fmaxf(x, dppf<0x121>(x)); return x; }
DI float wavesum(float x) { x += dppf<0x128>(x); x += dppf<0x124>(x); x += dppf<0x122>(x); x += dppf<0x121>(x); return xor32(xor16(x)); }
DI u32x4 widen(u32x2 a, u32x2 b) { auto r0 = __builtin_amdgcn_permlane32_swap(a[0], b[0], false, false); auto r1 = __builtin_amdgcn_permlane32_swap(a[1], b[1], false, false); return (u32x4){r0[0], r1[0], r0[1], r1[1]}; }

DI void gather8(u32x4 (&p)[8], unsigned voff, const void* sbase) {
    asm volatile("global_load_dwordx4 %0, %8, %9 sc1\n\tglobal_load_dwordx4 %1, %8, %9 offset:2048 sc1\n\tglobal_load_dwordx4 %2, %10, %9 sc1\n\tglobal_load_dwordx4 %3, %10, %9 offset:2048 sc1\n\t"
                 "global_load_dwordx4 %4, %11, %9 sc1\n\tglobal_load_dwordx4 %5, %11, %9 offset:2048 sc1\n\tglobal_load_dwordx4 %6, %12, %9 sc1\n\tglobal_load_dwordx4 %7, %12, %9 offset:2048 sc1\n\ts_waitcnt vmcnt(0)"
                 : "=&v"(p[0]), "=&v"(p[1]), "=&v"(p[2]), "=&v"(p[3]), "=&v"(p[4]), "=&v"(p[5]), "=&v"(p[6]), "=&v"(p[7])
                 : "v"(voff), "s"(sbase), "v"(voff + 4096u), "v"(voff + 8192u), "v"(voff + 12288u) : "memory"); }
DI void st8_wt(void* p, u32x2 w) { __hip_atomic_store((gu64*)p, ((unsigned long long)w[1] << 32) | w[0], RLX_AGENT); }
__global__ __launch_bounds__(768, 3) void k1_proj(const u16* __restrict__ x, const u16* __restrict__ wqkv, const u16* __restrict__ win, const u16* __restrict__ wsp, char* __restrict__ ws) {
    extern __shared__ __attribute__((aligned(16))) char lds[];
    const int tid = threadIdx.x, lane = tid & 63, r32 = lane & 31, hi = lane >> 5;
    const int wid = __builtin_amdgcn_readfirstlane(tid >> 6);
    const int b = blockIdx.x, xcd = b & 7, idx = b >> 3;
    const int panel = 8 * (xcd >> 2) + (idx & 7), j = 4 * (xcd & 3) + (idx >> 3);
    const unsigned lds0 = (unsigned)(uintptr_t)lds;
    const int jq = j & 3, u7 = 7 * (j >> 2);
    const int n1 = jq == 3 ? 1 : 2, n2 = jq == 1 ? 1 : 2;
    const int z1t0 = u7 + 2 * jq, z2t0 = u7 + (jq == 0 ? 0 : (jq == 1 ? 2 : (jq == 2 ? 3 : 5)));
    STAMP(0, 0);
    if (b == 0 && tid < 40) ((unsigned*)(ws + WS_CNT))[tid * 64] = 0u;
    if (wid >= 8) {
        const int l = wid - 8;
        const char* xb = (const char*)x + (size_t)(panel * 128 + l * 32) * 1024;
        const unsigned xv0 = (unsigned)((lane >> 3) * 1024 + (((lane & 7) ^ (lane >> 4)) * 16)), xv1 = (unsigned)((lane >> 3) * 1024 + (((lane & 7) ^ (4 + (lane >> 4))) * 16));
        const unsigned vq = (unsigned)((16 * l + (lane >> 2)) * 3072 + (lane & 3) * 16), vz = (unsigned)((16 * l + (lane >> 2)) * 3584 + (lane & 3) * 16);
        const char* wq = (const char*)wqkv + j * 64;
        int zc[4];
#pragma unroll
        for (int i = 0; i < 4; ++i) { const int ii = i < n1 + n2 ? i : n1 + n2 - 1; zc[i] = ii < n1 ? (z1t0 + ii) * 64 : FH * 2 + (z2t0 + ii - n1) * 64; }
        auto dma = [&](int st) __attribute__((always_inline)) {
            const unsigned d = lds0 + (st % 3) * K1_STAGE;
#pragma unroll
            for (int p = 0; p < 4; ++p) glds16s((p & 1) ? xv1 : xv0, xb + st * 128 + p * 8192, d + l * 4096 + p * 1024);
            const char* bq = wq + (size_t)st * 64 * 3072; const char* bz = (const char*)win + (size_t)st * 64 * 3584; const unsigned dw = d + K1_XB + l * 1024;
            glds16s(vq, bq + 2048, dw); glds16s(vq, bq, dw + 4096); glds16s(vq, bq + 1024, dw + 8192);
#pragma unroll
            for (int i = 0; i < 4; ++i) glds16s(vz, bz + zc[i], dw + (3 + i) * 4096);
        };
        dma(0);
        WAIT_BAR(0);
        dma(1); dma(2);
        glds4s((unsigned)((lane >> 5) * 4096 + (lane & 31) * 128), (const char*)wsp + (size_t)(256 * xcd + 8 * idx + 2 * l) * 4096, lds0 + K1_PF + l * 256);
#pragma unroll
        for (int st = 1; st < 8; ++st) {
            if (st <= 2) WAIT_BAR(12); else if (st < 7) WAIT_BAR(11); else WAIT_BAR(0);
            if (st + 2 < 8) dma(st + 2);
        }
        {
            if ((idx & 3) == 0) {
                unsigned long long pc;
                asm volatile("s_getpc_b64 s[98:99]\n\ts_add_u32 s98, s98, _Z6k2_mixPKtS0_S0_S0_S0_PcPt@rel32@lo+4\n\ts_addc_u32 s99, s99, _Z6k2_mixPKtS0_S0_S0_S0_PcPt@rel32@hi+12\n\ts_mov_b64 %0, s[98:99]" : "=s"(pc) :: "scc", "s98", "s99");
                const u32x4* cp = (const u32x4*)(pc + (size_t)l * 4096) + lane;
                u32x4 a = {0u, 0u, 0u, 0u};
#pragma unroll
                for (int i = 0; i < 4; ++i) a ^= cp[i * 64];
                asm volatile("" :: "v"(a));
            }
        }
        return;
    }
    const int mw = wid & 3, nh = wid >> 2;
    gu32* epw = (gu32*)(ws + WS_EPOCH) + panel * 64;
    unsigned tag = __hip_atomic_load(epw, RLX_AGENT);
    u32x4 xr = *(const u32x4*)(x + (size_t)(panel * 128 + 8 * j + wid) * D + lane * 8);
    auto rowstats = [&]() __attribute__((always_inline)) {
        PIN(xr); PIN(tag); tag += 1u;
        float v[8];
#pragma unroll
        for (int i = 0; i < 4; ++i) { v[2 * i] = bflo(xr[i]); v[2 * i + 1] = bfhi(xr[i]); }
        const float m = wavesum(((v[0] + v[1]) + (v[2] + v[3])) + ((v[4] + v[5]) + (v[6] + v[7]))) * (1.0f / 512.0f);
        float q = 0.f;
#pragma unroll
        for (int i = 0; i < 8; ++i) q = fmaf(v[i] - m, v[i] - m, q);
        const float r = __builtin_amdgcn_rsqf(wavesum(q) * (1.0f / 512.0f) + 1e-5f);
        if (lane < 2) __hip_atomic_store((gu64*)(ws + WS_XST) + (size_t)(panel * 128 + 8 * j + wid) * 2 + lane, ((unsigned long long)tag << 32) | __float_as_uint(lane ? r : m), RLX_AGENT);
    };
    f32x16 acc[4];
#pragma unroll
    for (int s = 0; s < 4; ++s) acc[s] = (f32x16){};
    float cs0 = 0.f, cs1 = 0.f;
    const int g = lane >> 4, qq = (lane & 15) >> 2, pp = lane & 3;
    const int trow = 32 * mw + r32;
    unsigned xo[4];
#pragma unroll
    for (int ks = 0; ks < 4; ++ks) xo[ks] = (unsigned)(trow * 128 + (((2 * ks + hi) ^ ((trow >> 1) & 7)) * 16));
    const unsigned wlane = (unsigned)(K1_XB + (8 * (g >> 1) + qq) * 64 + (g & 1) * 32 + pp * 8);
    const __amdgpu_buffer_rsrc_t sr = __builtin_amdgcn_make_buffer_rsrc((void*)(ws + WS_XST + (size_t)panel * 128 * 16), 0, 128 * 16, 0x00020000);
    u32x4 pst;
    int tile[4];
    auto mainloop = [&](auto NHC) __attribute__((always_inline)) {
        constexpr int NHV = decltype(NHC)::value; constexpr int NS = NHV == 0 ? 4 : 3;
        unsigned wo[NS];
#pragma unroll
        for (int s = 0; s < NS; ++s) { int t = s + mw; t = t >= NS ? t - NS : t; t = t >= NS ? t - NS : t; tile[s] = 4 * NHV + t; wo[s] = wlane + tile[s] * 4096; }
        bf16x8 xa[2], wf[2][NS];
        auto rd = [&](const int set, const lds_cptr sb, const int ks) __attribute__((always_inline)) {
            xa[set] = ldsr128(sb + xo[ks]);
#pragma unroll
            for (int s = 0; s < NS; ++s) wf[set][s] = cat(ldstr(sb + wo[s] + ks * 1024), ldstr(sb + wo[s] + ks * 1024 + 256));
        };
        auto mm = [&](const int set) __attribute__((always_inline)) {
            SBAR();
#pragma unroll
            for (int s = 0; s < NS; ++s) acc[s] = MFMA32(wf[set][s], xa[set], acc[s]);
            const u32x4 w = __builtin_bit_cast(u32x4, wf[set][0]);
#pragma unroll
            for (int i = 0; i < 4; ++i) { cs0 += bflo(w[i]); cs1 += bfhi(w[i]); }
            PIN(cs0); PIN(cs1);
#pragma unroll
            for (int i = 0; i < NS; ++i) { __builtin_amdgcn_sched_group_barrier(0x008, 1, 0); __builtin_amdgcn_sched_group_barrier(0x002, NS == 4 ? 4 : 6, 0); }
            SBAR();
        };
        LBAR();
        STAMP(0, 1);
        rd(0, (lds_cptr)lds, 0);
#pragma unroll
        for (int st = 0; st < 8; ++st) {
            const lds_cptr sb = (lds_cptr)lds + (st % 3) * K1_STAGE;
            rd(1, sb, 1); mm(0);
            rd(0, sb, 2); mm(1);
            rd(1, sb, 3); mm(0);
            if (st < 7) { LBAR(); rd(0, (lds_cptr)lds + ((st + 1) % 3) * K1_STAGE, 0); }
            if (st == 1) rowstats();
            if (st == 5) pst = __builtin_amdgcn_raw_buffer_load_b128(sr, trow * 16, 0, 16);
            mm(1);
        }
    };
    if (nh == 0) mainloop(std::integral_constant<int, 0>{}); else mainloop(std::integral_constant<int, 1>{});
    STAMP(0, 2);
    float* csl = (float*)(lds + K1_CS);
    { const float c = xor32(cs0 + cs1); if (hi == 0) csl[tile[0] * 32 + r32] = c; }
    const int tok = panel * 128 + trow;
    float mu, rs;
    {   unsigned long long g0 = ((unsigned long long)pst[1] << 32) | pst[0], g1 = ((unsigned long long)pst[3] << 32) | pst[2];
        gu64* gp = (gu64*)(ws + WS_XST) + (size_t)(panel * 128 + trow) * 2;
        for (unsigned spins = 0; !__all(((unsigned)(g0 >> 32) == tag) & ((unsigned)(g1 >> 32) == tag)) && spins < 200000u; ++spins) {
            __builtin_amdgcn_s_sleep(1);
            g0 = __hip_atomic_load(gp, RLX_AGENT); g1 = __hip_atomic_load(gp + 1, RLX_AGENT);
        }
        mu = __uint_as_float((unsigned)g0); rs = __uint_as_float((unsigned)g1); }
    LBAR();
    STAMP(0, 3);
    auto lnv = [&](int s, float (&v)[16], float sc) __attribute__((always_inline)) {
#pragma unroll
        for (int q = 0; q < 4; ++q) { const f32x4 c = *(const f32x4*)(csl + tile[s] * 32 + 8 * q + 4 * hi);
#pragma unroll
            for (int r = 0; r < 4; ++r) v[4 * q + r] = sc * (acc[s][4 * q + r] - mu * c[r]); } };
    const __amdgpu_buffer_rsrc_t wsr = __builtin_amdgcn_make_buffer_rsrc((void*)ws, 0, 32u << 20, 0x00020000);
    u16* Z1 = (u16*)(ws + WS_Z1);
    auto z1store = [&](int zt, const u32x2 (&w)[4]) __attribute__((always_inline)) {
        u16* p = Z1 + ((size_t)((tok >> 5) * 28 + zt) * 1024) + (hi * 32 + r32) * 4;
#pragma unroll
        for (int q = 0; q < 4; ++q) ST8(p + q * 256, w[q]); };
    auto gelu16 = [&](int s, u32x2 (&w)[4]) __attribute__((always_inline)) { float v[16]; lnv(s, v, rs);
#pragma unroll
        for (int q = 0; q < 4; ++q) w[q] = (u32x2){cvtpk(gelu_t(v[4 * q]), gelu_t(v[4 * q + 1])), cvtpk(gelu_t(v[4 * q + 2]), gelu_t(v[4 * q + 3]))}; };
    if (nh == 0) {
#pragma unroll
        for (int s = 0; s < 4; ++s) {
            const int T = tile[s];
            if (T == 3) { u32x2 w[4]; gelu16(s, w); z1store(z1t0, w); }
            else {
                float v[16]; lnv(s, v, T == 1 ? rs * (0.25f * LOG2E) : rs);
                u32x2 w[4];
#pragma unroll
                for (int q = 0; q < 4; ++q) w[q] = (u32x2){cvtpk(v[4 * q], v[4 * q + 1]), cvtpk(v[4 * q + 2], v[4 * q + 3])};
                if (T == 0) {
                    char* sc = lds + mw * 2048;
#pragma unroll
                    for (int q = 0; q < 4; ++q) *(u32x2*)(sc + r32 * 64 + (8 * q + 4 * hi) * 2) = w[q];
                    asm volatile("s_waitcnt lgkmcnt(0)" ::: "memory");
                    u16* Vt = (u16*)(ws + WS_V);
#pragma unroll
                    for (int hh = 0; hh < 2; ++hh)
#pragma unroll
                        for (int ks = 0; ks < 2; ++ks) {
                            const s16x4 t4 = ldstr((lds_cptr)lds + mw * 2048 + (16 * ks + 8 * (g >> 1) + 4 * (g & 1) + qq) * 64 + hh * 32 + pp * 8);
                            ST8(Vt + ((size_t)(2 * j + hh) * 128 + ((panel * 128 + 32 * mw) >> 4) + ks) * 256 + (g & 1) * 128 + (lane & 15) * 8 + (g >> 1) * 4, __builtin_bit_cast(u32x2, t4)); }
                } else {
                    const unsigned dsto = (unsigned)(T == 1 ? WS_Q : WS_K);
#pragma unroll
                    for (int hh = 0; hh < 2; ++hh) ST16(dsto + (((2 * j + hh) * S + tok) * 16 + 8 * hi) * 2, widen(w[2 * hh], w[2 * hh + 1]));
                    if (T == 2) {
                        float* kmax = (float*)(ws + WS_KMAX);
#pragma unroll
                        for (int hh = 0; hh < 2; ++hh) { float n = 0.f;
#pragma unroll
                            for (int q = 0; q < 2; ++q) { const u32x2 ww = w[2 * hh + q]; n = fmaf(bflo(ww[0]), bflo(ww[0]), n); n = fmaf(bfhi(ww[0]), bfhi(ww[0]), n); n = fmaf(bflo(ww[1]), bflo(ww[1]), n); n = fmaf(bfhi(ww[1]), bfhi(ww[1]), n); }
                            n = max16(rowmax16(xor32(n)));
                            if (lane == 0) kmax[(panel * 4 + mw) * H + 2 * j + hh] = n; }
                    }
                }
            }
        }
    } else {
        __builtin_amdgcn_s_setprio(2);
        float sm = 0.f;
#pragma unroll
        for (int s = 0; s < 3; ++s) { const int zi = tile[s] - 3;
            if (zi >= n1 && zi < n1 + n2) { float v[16]; lnv(s, v, rs);
#pragma unroll
                for (int r = 0; r < 16; ++r) { acc[s][r] = gelu_t(v[r]); sm += acc[s][r]; } } }
        sm = xor32(sm);
        const float ml = sm * (n2 == 2 ? (1.0f / 64.f) : (1.0f / 32.f)); float m2 = 0.f;
#pragma unroll
        for (int s = 0; s < 3; ++s) { const int zi = tile[s] - 3;
            if (zi >= n1 && zi < n1 + n2) {
#pragma unroll
                for (int r = 0; r < 16; ++r) { const float d = acc[s][r] - ml; m2 = fmaf(d, d, m2); } } }
        m2 = xor32(m2);
        __hip_atomic_store((gu64*)(ws + WS_XCH) + ((size_t)(panel * 16 + j) * 128 + trow) * 2 + hi, ((unsigned long long)tag << 32) | __float_as_uint(hi ? m2 : sm), RLX_AGENT);
        __builtin_amdgcn_s_setprio(0);
        STAMP(0, 4);
#pragma unroll
        for (int s = 0; s < 3; ++s) { const int zi = tile[s] - 3; if (zi < n1) { u32x2 w[4]; gelu16(s, w); z1store(z1t0 + zi, w); } }
        float mean, rstd;
        {
            const char* xb = ws + WS_XCH + (size_t)panel * 16 * 128 * 16; const unsigned xo = (unsigned)((8 * hi * 128 + trow) * 16);
            u32x4 p[8];
            for (unsigned spins = 0;; ++spins) {
                bool ok = true;
                gather8(p, xo, xb);
#pragma unroll
                for (int u = 0; u < 8; ++u) ok &= (p[u][1] == tag) & (p[u][3] == tag);
                if (__all(ok) || spins > 200000u) break;
                __builtin_amdgcn_s_sleep(6);
            }
            float tot = 0.f;
#pragma unroll
            for (int u = 0; u < 8; ++u) tot += __uint_as_float(p[u][0]);
            mean = xor32(tot) * (1.0f / FH);
            float mm2 = 0.f;
#pragma unroll
            for (int u = 0; u < 8; ++u) { const float nl = (u & 3) == 1 ? 32.f : 64.f; const float d = __uint_as_float(p[u][0]) * ((u & 3) == 1 ? (1.0f / 32.f) : (1.0f / 64.f)) - mean; mm2 += __uint_as_float(p[u][2]) + nl * d * d; }
            rstd = __builtin_amdgcn_rsqf(xor32(mm2) * (1.0f / FH) + 1e-5f);
        }
        STAMP(0, 5);
        if (wid == 4 && lane == 0) __hip_atomic_store(epw, tag, RLX_AGENT);
        const float nmr = -mean * rstd;
#pragma unroll
        for (int s = 0; s < 3; ++s) { const int zi = tile[s] - 3;
            if (zi >= n1 && zi < n1 + n2) {
                const int ft = z2t0 + zi - n1;
                u32x2 w[4];
#pragma unroll
                for (int q = 0; q < 4; ++q) w[q] = (u32x2){cvtpk(fmaf(acc[s][4 * q], rstd, nmr), fmaf(acc[s][4 * q + 1], rstd, nmr)), cvtpk(fmaf(acc[s][4 * q + 2], rstd, nmr), fmaf(acc[s][4 * q + 3], rstd, nmr))};
                char* sc = lds + 8192 + mw * 2048;
#pragma unroll
                for (int q = 0; q < 4; ++q) *(u32x2*)(sc + r32 * 64 + (8 * q + 4 * hi) * 2) = w[q];
                asm volatile("s_waitcnt lgkmcnt(0)" ::: "memory");
                const int tk0 = panel * 128 + 32 * mw;
#pragma unroll
                for (int ks = 0; ks < 2; ++ks) {
                    const lds_cptr rp = (lds_cptr)lds + 8192 + mw * 2048 + (16 * ks + 8 * (g >> 1) + qq) * 64 + (g & 1) * 32 + pp * 8;
                    const bf16x8 fr = cat(ldstr(rp), ldstr(rp + 256));
                    ST16((unsigned)WS_Z2N + (unsigned)(((((ft >> 2) * 32 + (tk0 >> 6)) * 4 + (ft & 3)) * 4 + ((tk0 >> 4) & 3) + ks) * 1024 + lane * 16), __builtin_bit_cast(u32x4, fr));
                }
                asm volatile("s_waitcnt lgkmcnt(0)" ::: "memory");
            } }
    }
    STAMP(0, 6);
}

constexpr int A_KR = 0, A_VR = 32768, A_CONST = 65536, A_FLAG = A_CONST + 4096, A_MX = A_FLAG + 64, K2_LDS = 81920;
constexpr float SAFE_T = 48.f;
#define EX(x) __builtin_amdgcn_exp2f(x)
#define BF(p) __builtin_bit_cast(bf16x8, p)

DI void attn_role(int b, char* __restrict__ ws, char* lds) {
    const u16* Qs = (const u16*)(ws + WS_Q); const u16* Kb = (const u16*)(ws + WS_K); const u16* Vt = (const u16*)(ws + WS_V); const float* kmax = (const float*)(ws + WS_KMAX); u16* Ot = (u16*)(ws + WS_O);
    const int tid = threadIdx.x, lane = tid & 63, r32 = lane & 31, hi = lane >> 5;
    const int wid = __builtin_amdgcn_readfirstlane(tid >> 6);
    const int xcd = b & 7, idx = b >> 3, head = xcd * 4 + (idx & 3), qt = idx >> 2;
    const int qp = wid & 3, kh = wid >> 2;
    const unsigned lds0 = (unsigned)(uintptr_t)lds;
    const char* kg = (const char*)(Kb + (size_t)head * S * DH);
    const char* vg = (const char*)(Vt + (size_t)head * S * DH);
    const unsigned kvoff = wid * 1024 + ((lane ^ ((lane >> 5) & 1)) * 16);
    const unsigned vvoff = wid * 1024 + lane * 16;
#define DMA_CHUNK(c) do { glds16s(kvoff, kg + (c) * 8192, lds0 + A_KR + ((c) & 3) * 8192 + wid * 1024); glds16s(vvoff, vg + (c) * 8192, lds0 + A_VR + ((c) & 3) * 8192 + wid * 1024); } while (0)
    STAMP(1, 0);
    const int q0 = qt * 256 + qp * 64;
    const bf16x8 qa = *(const bf16x8*)(Qs + ((size_t)head * S + q0 + r32) * DH + hi * 8);
    const bf16x8 qb = *(const bf16x8*)(Qs + ((size_t)head * S + q0 + 32 + r32) * DH + hi * 8);
    float km = kmax[lane * H + head];
    DMA_CHUNK(0);
    if (tid < 256) { const unsigned w = (tid & 31) ? 0u : 0x3F803F80u; u32x4 v = {w, w, w, w}; *(u32x4*)(lds + A_CONST + tid * 16) = v; }
    km = max32(max16(rowmax16(km)));
    float qna = 0.f, qnb = 0.f;
#pragma unroll
    for (int j = 0; j < 8; ++j) { const float va = bflo((u16)qa[j]), vb = bflo((u16)qb[j]); qna = fmaf(va, va, qna); qnb = fmaf(vb, vb, qnb); }
    qna = xor32(qna); qnb = xor32(qnb);
    const float mh = __builtin_amdgcn_sqrtf(fmaxf(qna, qnb) * km) * 1.001f;
    { const bool unsafe = __any(!(mh <= SAFE_T)); if (lane == 0) ((unsigned*)(lds + A_FLAG))[wid] = unsafe ? 1u : 0u; }
    const lds_cptr kp0 = (lds_cptr)lds + A_KR + kh * 4096 + r32 * 32 + ((hi ^ (r32 >> 4)) * 16);
    const bool vreal = r32 < 16;
    const unsigned vlane = vreal ? (unsigned)(A_VR + kh * 4096 + hi * 256 + r32 * 16) : (unsigned)(A_CONST + (r32 == 16 ? 0 : ((r32 >= 20 && r32 < 28) ? 64 : 16)));
    const unsigned vmask = vreal ? 0xFFFFFFFFu : 0u;
    f32x16 oa = {}, ob = {};
    WAIT_BAR(0);
    bool slow;
    { const u32x4 f0 = *(const u32x4*)(lds + A_FLAG), f1 = *(const u32x4*)(lds + A_FLAG + 16); slow = __builtin_amdgcn_readfirstlane(f0[0] | f0[1] | f0[2] | f0[3] | f1[0] | f1[1] | f1[2] | f1[3]) != 0; }
    if (!slow) {
        DMA_CHUNK(1); DMA_CHUNK(2); DMA_CHUNK(3);
        f32x16 cinit;
#pragma unroll
        for (int r = 0; r < 16; ++r) cinit[r] = -mh;
        PIN(cinit);
        bf16x8 kf, v0, v1; f32x16 sA, sB; u32x4 p0, p1;
        kf = ldsr128(kp0);
        { lds_cptr vp = (lds_cptr)lds + vlane; v0 = ldsr128(vp); v1 = ldsr128(vp + 512); }
        sA = MFMA32(kf, qa, cinit);
        p1 = (u32x4){0u, 0u, 0u, 0u};
#define HSTEP(SC, SN, QN, OP, OC, PRE1, PRE2, PRE3) do { \
        SN = MFMA32(kf, QN, cinit); PIN(SN); SBAR(); \
        PRE1; \
        SC[0] = EX(SC[0]); SC[1] = EX(SC[1]); SC[2] = EX(SC[2]); SC[3] = EX(SC[3]); SC[4] = EX(SC[4]); SC[5] = EX(SC[5]); PIN(SC); SBAR(); \
        OP = MFMA32(v1, BF(p1), OP); PIN(OP); SBAR(); \
        PRE2; \
        SC[6] = EX(SC[6]); SC[7] = EX(SC[7]); \
        p0[0] = cvtpk(SC[0], SC[1]); p0[1] = cvtpk(SC[2], SC[3]); p0[2] = cvtpk(SC[4], SC[5]); p0[3] = cvtpk(SC[6], SC[7]); PIN(p0); SBAR(); \
        SC[8] = EX(SC[8]); SC[9] = EX(SC[9]); SC[10] = EX(SC[10]); PIN(SC); SBAR(); \
        OC = MFMA32(v0, BF(p0), OC); PIN(OC); SBAR(); \
        PRE3; \
        SC[11] = EX(SC[11]); SC[12] = EX(SC[12]); SC[13] = EX(SC[13]); SC[14] = EX(SC[14]); SC[15] = EX(SC[15]); \
        p1[0] = cvtpk(SC[8], SC[9]); p1[1] = cvtpk(SC[10], SC[11]); p1[2] = cvtpk(SC[12], SC[13]); p1[3] = cvtpk(SC[14], SC[15]); PIN(p1); SBAR(); \
    } while (0)
#define BLOCK(kn, vc, vco, vn, vno) do { \
        HSTEP(sA, sB, qb, ob, oa, kf = ldsr128(kn), v1 = ldsr128((vc) + (vco) + 512), (void)0); \
        HSTEP(sB, sA, qa, oa, ob, (void)0, (void)0, v0 = ldsr128((vn) + (vno))); \
    } while (0)
        STAMP(1, 1);
        for (int c = 0; c < 8; ++c) {
            const unsigned ro = (unsigned)((c & 3) * 8192), rn = (unsigned)(((c + 1) & 3) * 8192);
            const lds_cptr vp = (lds_cptr)lds + (vlane + (ro & vmask)), vpn = (lds_cptr)lds + (vlane + (rn & vmask));
            const lds_cptr kp = kp0 + ro, kpn = kp0 + rn;
            BLOCK(kp + 1024, vp, 0, vp, 1024);
            BLOCK(kp + 2048, vp, 1024, vp, 2048);
            BLOCK(kp + 3072, vp, 2048, vp, 3072);
            if (c == 0) WAIT_BAR(4); else if (c < 6) WAIT_BAR(2); else if (c == 6) WAIT_BAR(0);
            if (c >= 1 && c + 3 < 8) DMA_CHUNK(c + 3);
            BLOCK(kpn, vp, 3072, vpn, 0);
        }
        ob = MFMA32(v1, BF(p1), ob);
#undef HSTEP
#undef BLOCK
    } else {
        float ma = -3.0e38f, mb = -3.0e38f;
        for (int pass = 0; pass < 2; ++pass) {
            for (int c = 0; c < 8; ++c) {
                LBAR();
                DMA_CHUNK(c);
                WAIT_BAR(0);
                const unsigned ro = (unsigned)((c & 3) * 8192);
                const lds_cptr vp = (lds_cptr)lds + (vlane + (ro & vmask)), kp = kp0 + ro;
                for (int j = 0; j < 4; ++j) {
                    const bf16x8 kf = ldsr128(kp + j * 1024);
                    const bf16x8 v0 = ldsr128(vp + j * 1024), v1 = ldsr128(vp + j * 1024 + 512);
#pragma unroll
                    for (int u = 0; u < 2; ++u) {
                        const f32x16 sc = MFMA32(kf, u ? qb : qa, (f32x16){});
                        float& m = u ? mb : ma; f32x16& o = u ? ob : oa;
                        if (pass == 0) {
#pragma unroll
                            for (int r = 0; r < 16; ++r) m = fmaxf(m, sc[r]);
                        } else {
                            u32x4 p0, p1;
#pragma unroll
                            for (int i = 0; i < 4; ++i) { p0[i] = cvtpk(EX(sc[2 * i] - m), EX(sc[2 * i + 1] - m)); p1[i] = cvtpk(EX(sc[8 + 2 * i] - m), EX(sc[9 + 2 * i] - m)); }
                            o = MFMA32(v0, BF(p0), o); o = MFMA32(v1, BF(p1), o);
                        }
                        SBAR();
                    }
                }
            }
            if (pass == 0) {
                ma = max32(ma); mb = max32(mb);
                float* mx = (float*)(lds + A_MX);
                if (hi == 0) { mx[(wid * 2) * 32 + r32] = ma; mx[(wid * 2 + 1) * 32 + r32] = mb; }
                LBAR();
                ma = fmaxf(ma, mx[((wid ^ 4) * 2) * 32 + r32]); mb = fmaxf(mb, mx[((wid ^ 4) * 2 + 1) * 32 + r32]);
            }
        }
    }
    STAMP(1, 2);
    __syncthreads();
    int tz = threadIdx.x; asm volatile("" : "+v"(tz));
    const int lz = tz & 63;
    float* red = (float*)lds;
    { float* dst = red + (size_t)(wid ^ 4) * 9 * 64 + lz;
#pragma unroll
      for (int r = 0; r < 9; ++r) dst[r * 64] = kh ? oa[r] : ob[r]; }
    __syncthreads();
    float o[9];
    { const float* src = red + (size_t)wid * 9 * 64 + lz;
#pragma unroll
      for (int r = 0; r < 9; ++r) o[r] = (kh ? ob[r] : oa[r]) + src[r * 64]; }
    const float rl = __builtin_amdgcn_rcpf(swap32(o[8], false));
    u16* op = Ot + ((size_t)(((q0 >> 5) + kh) * H + head) * 512) + (lz & 31) * 8 + (lz >> 5) * 4;
    st8_wt(op, (u32x2){cvtpk(o[0] * rl, o[1] * rl), cvtpk(o[2] * rl, o[3] * rl)});
    st8_wt(op + 256, (u32x2){cvtpk(o[4] * rl, o[5] * rl), cvtpk(o[6] * rl, o[7] * rl)});
    asm volatile("s_waitcnt vmcnt(0)\n\ts_barrier" ::: "memory");
    if (tz == 0) __hip_atomic_fetch_add((gu32*)(ws + WS_CNT) + (32 + qt) * 64, 1u, RLX_AGENT);
    STAMP(1, 3);
#undef DMA_CHUNK
}

DI void wait_cnt(gu32* cnt, unsigned want) {
    for (unsigned spins = 0; spins < 400000u; ++spins) {
        if ((unsigned)__builtin_amdgcn_readfirstlane(__hip_atomic_load(cnt, RLX_AGENT)) >= want) break;
        __builtin_amdgcn_s_sleep(1);
    }
}

#define O_AUX 16
#define H_AUX 16
constexpr int G_STAGE = 24576;
constexpr int W_STAGE = 16384;
constexpr int G_RED = 4 * W_STAGE;
DI void gate_role(int b, const u16* __restrict__ x, const u16* __restrict__ wo, const u16* __restrict__ wsp, const u16* __restrict__ bsp, const u16* __restrict__ wout, char* __restrict__ ws, u16* __restrict__ out, char* lds) {
    const int xcd = b & 7, idx = b >> 3;
    const int tt = xcd * 4 + (idx & 3), ct = idx >> 2;
    const int tid = threadIdx.x, lane = tid & 63, r32 = lane & 31, hi = lane >> 5;
    const int wid = __builtin_amdgcn_readfirstlane(tid >> 6);
    const unsigned lds0 = (unsigned)(uintptr_t)lds;
    gu32* hcnt = (gu32*)(ws + WS_CNT) + tt * 64; gu32* ocnt = (gu32*)(ws + WS_CNT) + (32 + (tt >> 2)) * 64;
    STAMP(1, 0);
    if (wid >= 4) {
        const int l = wid - 4;
        if (ct < 7) {
            const char* ag = ws + WS_Z2N + (size_t)ct * 32 * 16384 + l * 4096;
            const char* bg = (const char*)wsp + (size_t)(tt * 64 + l * 16) * 4096;
            const unsigned avoff = lane * 16;
            const unsigned bv0 = (unsigned)((lane >> 3) * 4096 + (((lane & 7) ^ (lane >> 4)) * 16)), bv1 = (unsigned)((8 + (lane >> 3)) * 4096 + (((lane & 7) ^ (4 + (lane >> 4))) * 16));
            auto dma = [&](int kt, int slot) __attribute__((always_inline)) { const unsigned d = lds0 + slot * G_STAGE; const char* a_ = ag + (size_t)kt * 16384;
#pragma unroll
                for (int e = 0; e < 4; ++e) glds16s(avoff, a_ + e * 1024, d + l * 4096 + e * 1024);
                glds16s(bv0, bg + kt * 128, d + 16384 + l * 2048); glds16s(bv1, bg + kt * 128, d + 16384 + l * 2048 + 1024); };
            dma(0, 0);
            WAIT_BAR(0);
            dma(1, 1); dma(2, 2);
            int slot = 0;
            for (int kt = 1; kt < 32; ++kt) {
                if (kt < 31) WAIT_BAR(6); else WAIT_BAR(0);
                if (kt + 2 < 32) dma(kt + 2, slot);
                slot = slot == 2 ? 0 : slot + 1;
            }
            LBAR();
        }
        unsigned wv[4];
#pragma unroll
        for (int e = 0; e < 4; ++e) { const int row = l * 32 + e * 8 + (lane >> 3); wv[e] = (unsigned)(row * 1024 + ct * 128 + (((lane & 7) ^ (((row >> 1) & 1) << 2)) * 16)); }
        auto dmw = [&](const u16* w, int st) __attribute__((always_inline)) { const char* wg = (const char*)w + (size_t)st * 128 * 1024; const unsigned d = lds0 + (st & 3) * W_STAGE + l * 4096;
#pragma unroll
            for (int e = 0; e < 4; ++e) glds16s(wv[e], wg, d + e * 1024); };
        dmw(wout, 0); dmw(wout, 1); dmw(wout, 2); dmw(wout, 3);
        if (l == 0) { wait_cnt(hcnt, 7);
        }
        WAIT_BAR(12);
#pragma unroll
        for (int st = 1; st < 7; ++st) {
            if (st < 4) WAIT_BAR(8); else if (st == 4) WAIT_BAR(8); else if (st == 5) WAIT_BAR(4); else WAIT_BAR(0);
            if (st + 3 < 7) dmw(wout, st + 3);
        }
        LBAR();
        dmw(wo, 0); dmw(wo, 1); dmw(wo, 2); dmw(wo, 3);
        if (l == 0) { wait_cnt(ocnt, 32);
        }
        WAIT_BAR(0);
        return;
    }
#define GPRIO 2
    __builtin_amdgcn_s_setprio(GPRIO);
    const int g = lane >> 4, qq = (lane & 15) >> 2, pp = lane & 3;
    const int th = wid & 1, fh = wid >> 1;
    const int trow = 32 * th + r32, t = tt * 64 + trow;
    if (ct < 7) {
        f32x16 acc0 = {}, acc1 = {};
        const unsigned aoff = (unsigned)(fh * 8192 + lane * 16);
        unsigned boff[4];
#pragma unroll
        for (int ks = 0; ks < 4; ++ks) boff[ks] = (unsigned)(16384 + trow * 128 + (((2 * ks + hi) ^ ((trow >> 1) & 7)) * 16));
        bf16x8 fa0[2], fa1[2], fb[2];
        auto rd = [&](int set, lds_cptr sb, int ks) __attribute__((always_inline)) {
            fb[set] = ldsr128(sb + boff[ks]);
            fa0[set] = ldsr128(sb + aoff + ks * 1024);
            fa1[set] = ldsr128(sb + aoff + 4096 + ks * 1024); };
        auto mm = [&](int set) __attribute__((always_inline)) { };
#define GSTEP(cur, nxt, sbn, ksn) do { SBAR(); acc0 = MFMA32(fa0[cur], fb[cur], acc0); SBAR(); rd(nxt, sbn, ksn); SBAR(); acc1 = MFMA32(fa1[cur], fb[cur], acc1); SBAR(); } while (0)
        const float bias = bflo(bsp[t]);
        u32x2 z[2][4];
#pragma unroll
        for (int i = 0; i < 2; ++i)
#pragma unroll
            for (int q = 0; q < 4; ++q) z[i][q] = *(const u32x2*)((const u16*)(ws + WS_Z1) + ((size_t)((t >> 5) * 28 + ct * 4 + 2 * fh + i) * 1024) + (hi * 32 + r32) * 4 + q * 256);
        LBAR();
        rd(0, (lds_cptr)lds, 0);
        int slot = 0;
        for (int kt = 0; kt < 32; ++kt) {
            const lds_cptr sb = (lds_cptr)lds + slot * G_STAGE;
            GSTEP(0, 1, sb, 1);
            GSTEP(1, 0, sb, 2);
            GSTEP(0, 1, sb, 3);
            slot = slot == 2 ? 0 : slot + 1;
            if (kt < 31) { LBAR(); GSTEP(1, 0, (lds_cptr)lds + slot * G_STAGE, 0); }
            else { acc0 = MFMA32(fa0[1], fb[1], acc0); acc1 = MFMA32(fa1[1], fb[1], acc1); }
        }
        STAMP(1, 2);
#pragma unroll
        for (int i = 0; i < 2; ++i) {
            u16* hp = (u16*)(ws + WS_H) + ((size_t)((t >> 5) * 56 + (ct * 4 + 2 * fh + i) * 2) * 512) + r32 * 8 + hi * 4;
#pragma unroll
            for (int q = 0; q < 4; ++q) {
                float v[4];
#pragma unroll
                for (int r = 0; r < 4; ++r) v[r] = (i ? acc1[4 * q + r] : acc0[4 * q + r]) + bias;
                st8_wt(hp + (q >> 1) * 512 + (q & 1) * 256, (u32x2){cvtpk(v[0] * bflo(z[i][q][0]), v[1] * bfhi(z[i][q][0])), cvtpk(v[2] * bflo(z[i][q][1]), v[3] * bfhi(z[i][q][1]))});
            }
        }
        asm volatile("s_waitcnt vmcnt(0) lgkmcnt(0)\n\ts_barrier" ::: "memory");
        if (tid == 0) __hip_atomic_fetch_add(hcnt, 1u, RLX_AGENT);
    }
    STAMP(1, 3);
    const int kh = fh;
    const int kr = 8 * (g >> 1) + qq;
    unsigned wof[2][2];
#pragma unroll
    for (int n = 0; n < 2; ++n) { wof[n][0] = (unsigned)((64 * kh + kr) * 128 + ((n ^ ((kr >> 1) & 1)) * 64) + (g & 1) * 32 + pp * 8); wof[n][1] = wof[n][0] + 512; }
    auto gemm = [&](const __amdgpu_buffer_rsrc_t tr, auto NST, bool bars, f32x16& c0, f32x16& c1) __attribute__((always_inline)) {
        constexpr int N = decltype(NST)::value;
        int lo16 = lane * 16; asm volatile("" : "+v"(lo16));
        u32x4 tf[4][4];
#pragma unroll
        for (int st = 0; st < 4; ++st)
#pragma unroll
            for (int i = 0; i < 4; ++i) tf[st][i] = __builtin_amdgcn_raw_buffer_load_b128(tr, lo16 + (st * 8 + 4 * kh + i) * 1024, 0, H_AUX);
        SBAR();
#pragma unroll
        for (int st = 0; st < N; ++st) {
            if (bars && st > 0) LBAR();
            const lds_cptr sb = (lds_cptr)lds + (st & 3) * W_STAGE;
#pragma unroll
            for (int i = 0; i < 4; ++i) {
                const bf16x8 w0 = cat(ldstr(sb + wof[0][0] + i * 2048), ldstr(sb + wof[0][1] + i * 2048)), w1 = cat(ldstr(sb + wof[1][0] + i * 2048), ldstr(sb + wof[1][1] + i * 2048));
                c0 = MFMA32(w0, BF(tf[st & 3][i]), c0); c1 = MFMA32(w1, BF(tf[st & 3][i]), c1);
            }
            SBAR();
            if (st + 4 < N) {
#pragma unroll
                for (int i = 0; i < 4; ++i) tf[st & 3][i] = __builtin_amdgcn_raw_buffer_load_b128(tr, lo16 + ((st + 4) * 8 + 4 * kh + i) * 1024, 0, H_AUX);
                SBAR(); }
        }
    };
    auto merge = [&](f32x16& c0, f32x16& c1) __attribute__((always_inline)) {
        f32x4* dst = (f32x4*)(lds + G_RED + (wid ^ 2) * 4096) + lane;
#pragma unroll
        for (int q = 0; q < 4; ++q) dst[q * 64] = kh ? (f32x4){c0[4 * q], c0[4 * q + 1], c0[4 * q + 2], c0[4 * q + 3]} : (f32x4){c1[4 * q], c1[4 * q + 1], c1[4 * q + 2], c1[4 * q + 3]};
    };
    f32x16 gb0 = {}, gb1 = {};
    LBAR();
    gemm(__builtin_amdgcn_make_buffer_rsrc((void*)(ws + WS_H + (size_t)(tt * 2 + th) * 57344), 0, 57344, 0x00020000), std::integral_constant<int, 7>{}, true, gb0, gb1);
    merge(gb0, gb1);
    LBAR();
    { char* sc = lds + G_RED + wid * 4096;
      float gv[16];
#pragma unroll
      for (int q = 0; q < 4; ++q) { const f32x4 o4 = ((const f32x4*)sc)[q * 64 + lane];
#pragma unroll
          for (int r = 0; r < 4; ++r) { const float bb = (kh ? gb1[4 * q + r] : gb0[4 * q + r]) + o4[r]; gv[4 * q + r] = bb * __builtin_amdgcn_rcpf(1.0f + EX(-LOG2E * bb)); } }
      asm volatile("s_waitcnt lgkmcnt(0)" ::: "memory");
#pragma unroll
      for (int u = 0; u < 2; ++u) ((u32x4*)sc)[u * 64 + lane] = (u32x4){cvtpk(gv[8 * u], gv[8 * u + 1]), cvtpk(gv[8 * u + 2], gv[8 * u + 3]), cvtpk(gv[8 * u + 4], gv[8 * u + 5]), cvtpk(gv[8 * u + 6], gv[8 * u + 7])}; }
    const int n0 = ct * 64 + kh * 32 + 4 * hi;
    u32x2 xv[4];
#pragma unroll
    for (int q = 0; q < 4; ++q) xv[q] = *(const u32x2*)(x + (size_t)t * D + n0 + 8 * q);
    PIN(xv[0]); PIN(xv[1]); PIN(xv[2]); PIN(xv[3]);
    STAMP(1, 4);
    LBAR();
    STAMP(1, 5);
    f32x16 s0 = {}, s1 = {};
    {
        const __amdgpu_buffer_rsrc_t tr = __builtin_amdgcn_make_buffer_rsrc((void*)(ws + WS_O + (size_t)(tt * 2 + th) * 32768), 0, 32768, 0x00020000);
        int lo16 = lane * 16; asm volatile("" : "+v"(lo16));
        u32x4 tf[16];
#pragma unroll
        for (int i = 0; i < 16; ++i) tf[i] = __builtin_amdgcn_raw_buffer_load_b128(tr, lo16 + ((i >> 2) * 8 + 4 * kh + (i & 3)) * 1024, 0, O_AUX);
        SBAR();
        bf16x8 w0[2], w1[2];
        w0[0] = cat(ldstr((lds_cptr)lds + wof[0][0]), ldstr((lds_cptr)lds + wof[0][1])); w1[0] = cat(ldstr((lds_cptr)lds + wof[1][0]), ldstr((lds_cptr)lds + wof[1][1]));
#pragma unroll
        for (int i = 0; i < 16; ++i) {
            if (i + 1 < 16) { const lds_cptr sb = (lds_cptr)lds + ((i + 1) >> 2) * W_STAGE + ((i + 1) & 3) * 2048;
                w0[(i + 1) & 1] = cat(ldstr(sb + wof[0][0]), ldstr(sb + wof[0][1])); w1[(i + 1) & 1] = cat(ldstr(sb + wof[1][0]), ldstr(sb + wof[1][1])); }
            s0 = MFMA32(w0[i & 1], BF(tf[i]), s0); s1 = MFMA32(w1[i & 1], BF(tf[i]), s1);
        }
    }
    STAMP(1, 7);
    u32x4 gq[2];
    { const u32x4* gt = (const u32x4*)(lds + G_RED + wid * 4096) + lane; gq[0] = gt[0]; gq[1] = gt[64]; }
    LBAR();
    { f32x4* dst = (f32x4*)(lds + (wid ^ 2) * 4096) + lane;
#pragma unroll
      for (int q = 0; q < 4; ++q) dst[q * 64] = kh ? (f32x4){s0[4 * q], s0[4 * q + 1], s0[4 * q + 2], s0[4 * q + 3]} : (f32x4){s1[4 * q], s1[4 * q + 1], s1[4 * q + 2], s1[4 * q + 3]}; }
    LBAR();
    { const f32x4* src = (const f32x4*)(lds + wid * 4096) + lane;
#pragma unroll
      for (int q = 0; q < 4; ++q) { const f32x4 o4 = src[q * 64]; float v[4]; const unsigned g0 = gq[q >> 1][2 * (q & 1)], g1 = gq[q >> 1][2 * (q & 1) + 1]; const float gg[4] = {bflo(g0), bfhi(g0), bflo(g1), bfhi(g1)};
#pragma unroll
          for (int r = 0; r < 4; ++r) v[r] = ((kh ? s1[4 * q + r] : s0[4 * q + r]) + o4[r]) * gg[r];
          *(u32x2*)(out + (size_t)t * D + n0 + 8 * q) = (u32x2){cvtpk(bflo(xv[q][0]) + v[0], bfhi(xv[q][0]) + v[1]), cvtpk(bflo(xv[q][1]) + v[2], bfhi(xv[q][1]) + v[3])}; } }
    STAMP(1, 6);
}

__global__ __launch_bounds__(512, 4) void k2_mix(const u16* __restrict__ x, const u16* __restrict__ wo, const u16* __restrict__ wsp, const u16* __restrict__ bsp, const u16* __restrict__ wout, char* __restrict__ ws, u16* __restrict__ out) {
    extern __shared__ __attribute__((aligned(16))) char lds[];
    if (blockIdx.x < 256) attn_role(blockIdx.x, ws, lds);
    if (blockIdx.x >= 256) gate_role(blockIdx.x - 256, x, wo, wsp, bsp, wout, ws, out, lds);
}

extern "C" void kernel_launch(void* const* d_in, const int* in_sizes, int n_in, void* d_out, int out_size, void* d_ws, size_t ws_size, hipStream_t stream) {
    const u16* x = (const u16*)d_in[0]; const u16* wqkv = (const u16*)d_in[1]; const u16* wo = (const u16*)d_in[2];
    const u16* win = (const u16*)d_in[4]; const u16* wsp = (const u16*)d_in[5]; const u16* bsp = (const u16*)d_in[6]; const u16* wout = (const u16*)d_in[7];
    char* ws = (char*)d_ws;
    (void)hipFuncSetAttribute((const void*)k1_proj, hipFuncAttributeMaxDynamicSharedMemorySize, K1_LDS);
    (void)hipFuncSetAttribute((const void*)k2_mix, hipFuncAttributeMaxDynamicSharedMemorySize, K2_LDS);
    k1_proj<<<256, 768, K1_LDS, stream>>>(x, wqkv, win, wsp, ws);
    k2_mix<<<512, 512, K2_LDS, stream>>>(x, wo, wsp, bsp, wout, ws, (u16*)d_out);
}
```

```cpp
#include <hip/hip_runtime.h>
#include <hip/hip_bf16.h>
#include <stdint.h>
#include <type_traits>

typedef unsigned short u16;
using bf16x8 = __attribute__((ext_vector_type(8))) short;
using s16x4 = __attribute__((ext_vector_type(4))) short;
using f32x16 = __attribute__((ext_vector_type(16))) float;
using f32x4 = __attribute__((ext_vector_type(4))) float;
using u32x4 = __attribute__((ext_vector_type(4))) unsigned;
using u32x2 = __attribute__((ext_vector_type(2))) unsigned;
typedef __bf16 bf16x2_t __attribute__((ext_vector_type(2)));
typedef float f32x2_t __attribute__((ext_vector_type(2)));
typedef short v4i16_t __attribute__((ext_vector_type(4)));
#define LDSAS __attribute__((address_space(3)))
typedef LDSAS const char* lds_cptr;
#define MFMA32(a, b, c) __builtin_amdgcn_mfma_f32_32x32x16_bf16(a, b, c, 0, 0, 0)
#define SBAR() __builtin_amdgcn_sched_barrier(0)
#define PIN(x) asm volatile("" : "+v"(x))
#define WAIT_BAR(N) asm volatile("s_waitcnt vmcnt(" #N ") lgkmcnt(0)\n\ts_barrier" ::: "memory")
#define LBAR() asm volatile("s_waitcnt lgkmcnt(0)\n\ts_barrier" ::: "memory")
#define DI __device__ __forceinline__
#define ST8(p, w) st8_wt((void*)(p), (w))
#define ST16(off, v) __builtin_amdgcn_raw_buffer_store_b128((v), wsr, (off), 0, 16)

constexpr int S = 2048, D = 512, H = 32, DH = 16, FH = 896;
constexpr float LOG2E = 1.4426950408889634f;

constexpr size_t WS_Q = 0, WS_K = 2u << 20, WS_V = 4u << 20, WS_Z1 = 6u << 20, WS_Z2N = 14u << 20, WS_H = 18u << 20, WS_O = 22u << 20, WS_KMAX = 24u << 20, WS_EPOCH = 25u << 20, WS_XCH = 26u << 20, WS_XST = 27u << 20, WS_CNT = 28u << 20;

DI unsigned cvtpk(float lo, float hi) { f32x2_t v = {lo, hi}; return __builtin_bit_cast(unsigned, __builtin_convertvector(v, bf16x2_t)); }
DI float bflo(unsigned w) { return __uint_as_float(w << 16); }
DI float bfhi(unsigned w) { return __uint_as_float(w & 0xFFFF0000u); }
DI void glds16s(unsigned voff, const void* sbase, unsigned lds_base) {
    lds_base = __builtin_amdgcn_readfirstlane(lds_base);
    unsigned sv; asm volatile("s_mov_b32 %0, m0\n\ts_mov_b32 m0, %3\n\ts_nop 0\n\tglobal_load_lds_dwordx4 %1, %2\n\ts_mov_b32 m0, %0" : "=&s"(sv) : "v"(voff), "s"(sbase), "s"(lds_base) : "memory"); }
DI bf16x8 ldsr128(lds_cptr p) { return *(const LDSAS bf16x8*)p; }
DI s16x4 ldstr(lds_cptr p) { return __builtin_bit_cast(s16x4, __builtin_amdgcn_ds_read_tr16_b64_v4i16((LDSAS v4i16_t*)p)); }
DI bf16x8 cat(s16x4 a, s16x4 b) { return __builtin_shufflevector(a, b, 0, 1, 2, 3, 4, 5, 6, 7); }
DI float swap32(float x, bool sum) { auto rr = __builtin_amdgcn_permlane32_swap(__float_as_uint(x), __float_as_uint(x), false, false); return sum ? __uint_as_float(rr[0]) + __uint_as_float(rr[1]) : __uint_as_float(rr[0]); }
DI float gelu_t(float x) {
    const float c1 = -2.0f * 0.7978845608028654f * LOG2E, c3 = c1 * 0.044715f;
    const float u = x * fmaf(x * x, c3, c1);
    return x * __builtin_amdgcn_rcpf(1.0f + __builtin_amdgcn_exp2f(u));
}

constexpr int K1_XB = 16384;
constexpr int K1_STAGE = K1_XB + 7 * 4096;
constexpr int K1_CS = 3 * K1_STAGE;
constexpr int K1_LDS = K1_CS + 1024;
typedef __attribute__((address_space(1))) unsigned gu32; typedef __attribute__((address_space(1))) unsigned long long gu64;
#define RLX_AGENT __ATOMIC_RELAXED, __HIP_MEMORY_SCOPE_AGENT

DI float xor16(float x) { auto r = __builtin_amdgcn_permlane16_swap(__float_as_uint(x), __float_as_uint(x), false, false); return __uint_as_float(r[0]) + __uint_as_float(r[1]); }
DI float xor32(float x) { auto r = __builtin_amdgcn_permlane32_swap(__float_as_uint(x), __float_as_uint(x), false, false); return __uint_as_float(r[0]) + __uint_as_float(r[1]); }
DI float max16(float x) { auto r = __builtin_amdgcn_permlane16_swap(__float_as_uint(x), __float_as_uint(x), false, false); return fmaxf(__uint_as_float(r[0]), __uint_as_float(r[1])); }
DI float max32(float x) { auto r = __builtin_amdgcn_permlane32_swap(__float_as_uint(x), __float_as_uint(x), false, false); return fmaxf(__uint_as_float(r[0]), __uint_as_float(r[1])); }
template <int CTRL> DI float dppf(float x) { return __uint_as_float(__builtin_amdgcn_update_dpp(0, __float_as_uint(x), CTRL, 0xF, 0xF, false)); }
DI float rowmax16(float x) { x = fmaxf(x, dppf<0x128>(x)); x = fmaxf(x, dppf<0x124>(x)); x = fmaxf(x, dppf<0x122>(x)); x = fmaxf(x, dppf<0x121>(x)); return x; }
DI float wavesum(float x) { x += dppf<0x128>(x); x += dppf<0x124>(x); x += dppf<0x122>(x); x += dppf<0x121>(x); return xor32(xor16(x)); }
DI u32x4 widen(u32x2 a, u32x2 b) { auto r0 = __builtin_amdgcn_permlane32_swap(a[0], b[0], false, false); auto r1 = __builtin_amdgcn_permlane32_swap(a[1], b[1], false, false); return (u32x4){r0[0], r1[0], r0[1], r1[1]}; }

DI void gather8(u32x4 (&p)[8], unsigned voff, const void* sbase) {
    asm volatile("global_load_dwordx4 %0, %8, %9 sc1\n\tglobal_load_dwordx4 %1, %8, %9 offset:2048 sc1\n\tglobal_load_dwordx4 %2, %10, %9 sc1\n\tglobal_load_dwordx4 %3, %10, %9 offset:2048 sc1\n\t"
                 "global_load_dwordx4 %4, %11, %9 sc1\n\tglobal_load_dwordx4 %5, %11, %9 offset:2048 sc1\n\tglobal_load_dwordx4 %6, %12, %9 sc1\n\tglobal_load_dwordx4 %7, %12, %9 offset:2048 sc1\n\ts_waitcnt vmcnt(0)"
                 : "=&v"(p[0]), "=&v"(p[1]), "=&v"(p[2]), "=&v"(p[3]), "=&v"(p[4]), "=&v"(p[5]), "=&v"(p[6]), "=&v"(p[7])
                 : "v"(voff), "s"(sbase), "v"(voff + 4096u), "v"(voff + 8192u), "v"(voff + 12288u) : "memory"); }
DI void st8_wt(void* p, u32x2 w) { __hip_atomic_store((gu64*)p, ((unsigned long long)w[1] << 32) | w[0], RLX_AGENT); }
__global__ __launch_bounds__(768, 3) void k1_proj(const u16* __restrict__ x, const u16* __restrict__ wqkv, const u16* __restrict__ win, char* __restrict__ ws) {
    extern __shared__ __attribute__((aligned(16))) char lds[];
    const int tid = threadIdx.x, lane = tid & 63, r32 = lane & 31, hi = lane >> 5;
    const int wid = __builtin_amdgcn_readfirstlane(tid >> 6);
    const int b = blockIdx.x, xcd = b & 7, idx = b >> 3;
    const int panel = 8 * (xcd >> 2) + (idx & 7), j = 4 * (xcd & 3) + (idx >> 3);
    const unsigned lds0 = (unsigned)(uintptr_t)lds;
    const int jq = j & 3, u7 = 7 * (j >> 2);
    const int n1 = jq == 3 ? 1 : 2, n2 = jq == 1 ? 1 : 2;
    const int z1t0 = u7 + 2 * jq, z2t0 = u7 + (jq == 0 ? 0 : (jq == 1 ? 2 : (jq == 2 ? 3 : 5)));
    if (b == 0 && tid < 40) ((unsigned*)(ws + WS_CNT))[tid * 64] = 0u;
    if (wid >= 8) {
        const int l = wid - 8;
        const char* xb = (const char*)x + (size_t)(panel * 128 + l * 32) * 1024;
        const unsigned xv0 = (unsigned)((lane >> 3) * 1024 + (((lane & 7) ^ (lane >> 4)) * 16)), xv1 = (unsigned)((lane >> 3) * 1024 + (((lane & 7) ^ (4 + (lane >> 4))) * 16));
        const unsigned vq = (unsigned)((16 * l + (lane >> 2)) * 3072 + (lane & 3) * 16), vz = (unsigned)((16 * l + (lane >> 2)) * 3584 + (lane & 3) * 16);
        const char* wq = (const char*)wqkv + j * 64;
        int zc[4];
#pragma unroll
        for (int i = 0; i < 4; ++i) { const int ii = i < n1 + n2 ? i : n1 + n2 - 1; zc[i] = ii < n1 ? (z1t0 + ii) * 64 : FH * 2 + (z2t0 + ii - n1) * 64; }
        auto dma = [&](int st) __attribute__((always_inline)) {
            const unsigned d = lds0 + (st % 3) * K1_STAGE;
#pragma unroll
            for (int p = 0; p < 4; ++p) glds16s((p & 1) ? xv1 : xv0, xb + st * 128 + p * 8192, d + l * 4096 + p * 1024);
            const char* bq = wq + (size_t)st * 64 * 3072; const char* bz = (const char*)win + (size_t)st * 64 * 3584; const unsigned dw = d + K1_XB + l * 1024;
            glds16s(vq, bq + 2048, dw); glds16s(vq, bq, dw + 4096); glds16s(vq, bq + 1024, dw + 8192);
#pragma unroll
            for (int i = 0; i < 4; ++i) glds16s(vz, bz + zc[i], dw + (3 + i) * 4096);
        };
        dma(0);
        WAIT_BAR(0);
        dma(1); dma(2);
#pragma unroll
        for (int st = 1; st < 8; ++st) {
            if (st < 7) WAIT_BAR(11); else WAIT_BAR(0);
            if (st + 2 < 8) dma(st + 2);
        }
        return;
    }
    const int mw = wid & 3, nh = wid >> 2;
    gu32* epw = (gu32*)(ws + WS_EPOCH) + panel * 64;
    unsigned tag = __hip_atomic_load(epw, RLX_AGENT);
    u32x4 xr = *(const u32x4*)(x + (size_t)(panel * 128 + 8 * j + wid) * D + lane * 8);
    auto rowstats = [&]() __attribute__((always_inline)) {
        PIN(xr); PIN(tag); tag += 1u;
        float v[8];
#pragma unroll
        for (int i = 0; i < 4; ++i) { v[2 * i] = bflo(xr[i]); v[2 * i + 1] = bfhi(xr[i]); }
        const float m = wavesum(((v[0] + v[1]) + (v[2] + v[3])) + ((v[4] + v[5]) + (v[6] + v[7]))) * (1.0f / 512.0f);
        float q = 0.f;
#pragma unroll
        for (int i = 0; i < 8; ++i) q = fmaf(v[i] - m, v[i] - m, q);
        const float r = __builtin_amdgcn_rsqf(wavesum(q) * (1.0f / 512.0f) + 1e-5f);
        if (lane < 2) __hip_atomic_store((gu64*)(ws + WS_XST) + (size_t)(panel * 128 + 8 * j + wid) * 2 + lane, ((unsigned long long)tag << 32) | __float_as_uint(lane ? r : m), RLX_AGENT);
    };
    f32x16 acc[4];
#pragma unroll
    for (int s = 0; s < 4; ++s) acc[s] = (f32x16){};
    float cs0 = 0.f, cs1 = 0.f;
    const int g = lane >> 4, qq = (lane & 15) >> 2, pp = lane & 3;
    const int trow = 32 * mw + r32;
    unsigned xo[4];
#pragma unroll
    for (int ks = 0; ks < 4; ++ks) xo[ks] = (unsigned)(trow * 128 + (((2 * ks + hi) ^ ((trow >> 1) & 7)) * 16));
    const unsigned wlane = (unsigned)(K1_XB + (8 * (g >> 1) + qq) * 64 + (g & 1) * 32 + pp * 8);
    const __amdgpu_buffer_rsrc_t sr = __builtin_amdgcn_make_buffer_rsrc((void*)(ws + WS_XST + (size_t)panel * 128 * 16), 0, 128 * 16, 0x00020000);
    u32x4 pst;
    int tile[4];
    auto mainloop = [&](auto NHC) __attribute__((always_inline)) {
        constexpr int NHV = decltype(NHC)::value; constexpr int NS = NHV == 0 ? 4 : 3;
        unsigned wo[NS];
#pragma unroll
        for (int s = 0; s < NS; ++s) { int t = s + mw; t = t >= NS ? t - NS : t; t = t >= NS ? t - NS : t; tile[s] = 4 * NHV + t; wo[s] = wlane + tile[s] * 4096; }
        bf16x8 xa[2], wf[2][NS];
        auto rd = [&](const int set, const lds_cptr sb, const int ks) __attribute__((always_inline)) {
            xa[set] = ldsr128(sb + xo[ks]);
#pragma unroll
            for (int s = 0; s < NS; ++s) wf[set][s] = cat(ldstr(sb + wo[s] + ks * 1024), ldstr(sb + wo[s] + ks * 1024 + 256));
        };
        auto mm = [&](const int set) __attribute__((always_inline)) {
            SBAR();
#pragma unroll
            for (int s = 0; s < NS; ++s) acc[s] = MFMA32(wf[set][s], xa[set], acc[s]);
            const u32x4 w = __builtin_bit_cast(u32x4, wf[set][0]);
#pragma unroll
            for (int i = 0; i < 4; ++i) { cs0 += bflo(w[i]); cs1 += bfhi(w[i]); }
            PIN(cs0); PIN(cs1);
#pragma unroll
            for (int i = 0; i < NS; ++i) { __builtin_amdgcn_sched_group_barrier(0x008, 1, 0); __builtin_amdgcn_sched_group_barrier(0x002, NS == 4 ? 4 : 6, 0); }
            SBAR();
        };
        LBAR();
        rd(0, (lds_cptr)lds, 0);
#pragma unroll
        for (int st = 0; st < 8; ++st) {
            const lds_cptr sb = (lds_cptr)lds + (st % 3) * K1_STAGE;
            rd(1, sb, 1); mm(0);
            rd(0, sb, 2); mm(1);
            rd(1, sb, 3); mm(0);
            if (st < 7) { LBAR(); rd(0, (lds_cptr)lds + ((st + 1) % 3) * K1_STAGE, 0); }
            if (st == 1) rowstats();
            if (st == 5) pst = __builtin_amdgcn_raw_buffer_load_b128(sr, trow * 16, 0, 16);
            mm(1);
        }
    };
    if (nh == 0) mainloop(std::integral_constant<int, 0>{}); else mainloop(std::integral_constant<int, 1>{});
    float* csl = (float*)(lds + K1_CS);
    { const float c = xor32(cs0 + cs1); if (hi == 0) csl[tile[0] * 32 + r32] = c; }
    const int tok = panel * 128 + trow;
    float mu, rs;
    {   unsigned long long g0 = ((unsigned long long)pst[1] << 32) | pst[0], g1 = ((unsigned long long)pst[3] << 32) | pst[2];
        gu64* gp = (gu64*)(ws + WS_XST) + (size_t)(panel * 128 + trow) * 2;
        for (unsigned spins = 0; !__all(((unsigned)(g0 >> 32) == tag) & ((unsigned)(g1 >> 32) == tag)) && spins < 200000u; ++spins) {
            __builtin_amdgcn_s_sleep(1);
            g0 = __hip_atomic_load(gp, RLX_AGENT); g1 = __hip_atomic_load(gp + 1, RLX_AGENT);
        }
        mu = __uint_as_float((unsigned)g0); rs = __uint_as_float((unsigned)g1); }
    LBAR();
    auto lnv = [&](int s, float (&v)[16], float sc) __attribute__((always_inline)) {
#pragma unroll
        for (int q = 0; q < 4; ++q) { const f32x4 c = *(const f32x4*)(csl + tile[s] * 32 + 8 * q + 4 * hi);
#pragma unroll
            for (int r = 0; r < 4; ++r) v[4 * q + r] = sc * (acc[s][4 * q + r] - mu * c[r]); } };
    const __amdgpu_buffer_rsrc_t wsr = __builtin_amdgcn_make_buffer_rsrc((void*)ws, 0, 32u << 20, 0x00020000);
    u16* Z1 = (u16*)(ws + WS_Z1);
    auto z1store = [&](int zt, const u32x2 (&w)[4]) __attribute__((always_inline)) {
        u16* p = Z1 + ((size_t)((tok >> 5) * 28 + zt) * 1024) + (hi * 32 + r32) * 4;
#pragma unroll
        for (int q = 0; q < 4; ++q) ST8(p + q * 256, w[q]); };
    auto gelu16 = [&](int s, u32x2 (&w)[4]) __attribute__((always_inline)) { float v[16]; lnv(s, v, rs);
#pragma unroll
        for (int q = 0; q < 4; ++q) w[q] = (u32x2){cvtpk(gelu_t(v[4 * q]), gelu_t(v[4 * q + 1])), cvtpk(gelu_t(v[4 * q + 2]), gelu_t(v[4 * q + 3]))}; };
    if (nh == 0) {
#pragma unroll
        for (int s = 0; s < 4; ++s) {
            const int T = tile[s];
            if (T == 3) { u32x2 w[4]; gelu16(s, w); z1store(z1t0, w); }
            else {
                float v[16]; lnv(s, v, T == 1 ? rs * (0.25f * LOG2E) : rs);
                u32x2 w[4];
#pragma unroll
                for (int q = 0; q < 4; ++q) w[q] = (u32x2){cvtpk(v[4 * q], v[4 * q + 1]), cvtpk(v[4 * q + 2], v[4 * q + 3])};
                if (T == 0) {
                    char* sc = lds + mw * 2048;
#pragma unroll
                    for (int q = 0; q < 4; ++q) *(u32x2*)(sc + r32 * 64 + (8 * q + 4 * hi) * 2) = w[q];
                    asm volatile("s_waitcnt lgkmcnt(0)" ::: "memory");
                    u16* Vt = (u16*)(ws + WS_V);
#pragma unroll
                    for (int hh = 0; hh < 2; ++hh)
#pragma unroll
                        for (int ks = 0; ks < 2; ++ks) {
                            const s16x4 t4 = ldstr((lds_cptr)lds + mw * 2048 + (16 * ks + 8 * (g >> 1) + 4 * (g & 1) + qq) * 64 + hh * 32 + pp * 8);
                            ST8(Vt + ((size_t)(2 * j + hh) * 128 + ((panel * 128 + 32 * mw) >> 4) + ks) * 256 + (g & 1) * 128 + (lane & 15) * 8 + (g >> 1) * 4, __builtin_bit_cast(u32x2, t4)); }
                } else {
                    const unsigned dsto = (unsigned)(T == 1 ? WS_Q : WS_K);
#pragma unroll
                    for (int hh = 0; hh < 2; ++hh) ST16(dsto + (((2 * j + hh) * S + tok) * 16 + 8 * hi) * 2, widen(w[2 * hh], w[2 * hh + 1]));
                    if (T == 2) {
                        float* kmax = (float*)(ws + WS_KMAX);
#pragma unroll
                        for (int hh = 0; hh < 2; ++hh) { float n = 0.f;
#pragma unroll
                            for (int q = 0; q < 2; ++q) { const u32x2 ww = w[2 * hh + q]; n = fmaf(bflo(ww[0]), bflo(ww[0]), n); n = fmaf(bfhi(ww[0]), bfhi(ww[0]), n); n = fmaf(bflo(ww[1]), bflo(ww[1]), n); n = fmaf(bfhi(ww[1]), bfhi(ww[1]), n); }
                            n = max16(rowmax16(xor32(n)));
                            if (lane == 0) kmax[(panel * 4 + mw) * H + 2 * j + hh] = n; }
                    }
                }
            }
        }
    } else {
        __builtin_amdgcn_s_setprio(2);
        float sm = 0.f;
#pragma unroll
        for (int s = 0; s < 3; ++s) { const int zi = tile[s] - 3;
            if (zi >= n1 && zi < n1 + n2) { float v[16]; lnv(s, v, rs);
#pragma unroll
                for (int r = 0; r < 16; ++r) { acc[s][r] = gelu_t(v[r]); sm += acc[s][r]; } } }
        sm = xor32(sm);
        const float ml = sm * (n2 == 2 ? (1.0f / 64.f) : (1.0f / 32.f)); float m2 = 0.f;
#pragma unroll
        for (int s = 0; s < 3; ++s) { const int zi = tile[s] - 3;
            if (zi >= n1 && zi < n1 + n2) {
#pragma unroll
                for (int r = 0; r < 16; ++r) { const float d = acc[s][r] - ml; m2 = fmaf(d, d, m2); } } }
        m2 = xor32(m2);
        __hip_atomic_store((gu64*)(ws + WS_XCH) + ((size_t)(panel * 16 + j) * 128 + trow) * 2 + hi, ((unsigned long long)tag << 32) | __float_as_uint(hi ? m2 : sm), RLX_AGENT);
        __builtin_amdgcn_s_setprio(0);
#pragma unroll
        for (int s = 0; s < 3; ++s) { const int zi = tile[s] - 3; if (zi < n1) { u32x2 w[4]; gelu16(s, w); z1store(z1t0 + zi, w); } }
        float mean, rstd;
        {
            const char* xb = ws + WS_XCH + (size_t)panel * 16 * 128 * 16; const unsigned xo = (unsigned)((8 * hi * 128 + trow) * 16);
            u32x4 p[8];
            for (unsigned spins = 0;; ++spins) {
                bool ok = true;
                gather8(p, xo, xb);
#pragma unroll
                for (int u = 0; u < 8; ++u) ok &= (p[u][1] == tag) & (p[u][3] == tag);
                if (__all(ok) || spins > 200000u) break;
                __builtin_amdgcn_s_sleep(6);
            }
            float tot = 0.f;
#pragma unroll
            for (int u = 0; u < 8; ++u) tot += __uint_as_float(p[u][0]);
            mean = xor32(tot) * (1.0f / FH);
            float mm2 = 0.f;
#pragma unroll
            for (int u = 0; u < 8; ++u) { const float nl = (u & 3) == 1 ? 32.f : 64.f; const float d = __uint_as_float(p[u][0]) * ((u & 3) == 1 ? (1.0f / 32.f) : (1.0f / 64.f)) - mean; mm2 += __uint_as_float(p[u][2]) + nl * d * d; }
            rstd = __builtin_amdgcn_rsqf(xor32(mm2) * (1.0f / FH) + 1e-5f);
        }
        if (wid == 4 && lane == 0) __hip_atomic_store(epw, tag, RLX_AGENT);
        const float nmr = -mean * rstd;
#pragma unroll
        for (int s = 0; s < 3; ++s) { const int zi = tile[s] - 3;
            if (zi >= n1 && zi < n1 + n2) {
                const int ft = z2t0 + zi - n1;
                u32x2 w[4];
#pragma unroll
                for (int q = 0; q < 4; ++q) w[q] = (u32x2){cvtpk(fmaf(acc[s][4 * q], rstd, nmr), fmaf(acc[s][4 * q + 1], rstd, nmr)), cvtpk(fmaf(acc[s][4 * q + 2], rstd, nmr), fmaf(acc[s][4 * q + 3], rstd, nmr))};
                char* sc = lds + 8192 + mw * 2048;
#pragma unroll
                for (int q = 0; q < 4; ++q) *(u32x2*)(sc + r32 * 64 + (8 * q + 4 * hi) * 2) = w[q];
                asm volatile("s_waitcnt lgkmcnt(0)" ::: "memory");
                const int tk0 = panel * 128 + 32 * mw;
#pragma unroll
                for (int ks = 0; ks < 2; ++ks) {
                    const lds_cptr rp = (lds_cptr)lds + 8192 + mw * 2048 + (16 * ks + 8 * (g >> 1) + qq) * 64 + (g & 1) * 32 + pp * 8;
                    const bf16x8 fr = cat(ldstr(rp), ldstr(rp + 256));
                    ST16((unsigned)WS_Z2N + (unsigned)(((((ft >> 2) * 32 + (tk0 >> 6)) * 4 + (ft & 3)) * 4 + ((tk0 >> 4) & 3) + ks) * 1024 + lane * 16), __builtin_bit_cast(u32x4, fr));
                }
                asm volatile("s_waitcnt lgkmcnt(0)" ::: "memory");
            } }
    }
}

constexpr int A_KR = 0, A_VR = 32768, A_CONST = 65536, A_FLAG = A_CONST + 4096, A_MX = A_FLAG + 64, K2_LDS = 81920;
constexpr float SAFE_T = 48.f;
#define EX(x) __builtin_amdgcn_exp2f(x)
#define BF(p) __builtin_bit_cast(bf16x8, p)

DI void attn_role(int b, char* __restrict__ ws, char* lds) {
    const u16* Qs = (const u16*)(ws + WS_Q); const u16* Kb = (const u16*)(ws + WS_K); const u16* Vt = (const u16*)(ws + WS_V); const float* kmax = (const float*)(ws + WS_KMAX); u16* Ot = (u16*)(ws + WS_O);
    const int tid = threadIdx.x, lane = tid & 63, r32 = lane & 31, hi = lane >> 5;
    const int wid = __builtin_amdgcn_readfirstlane(tid >> 6);
    const int xcd = b & 7, idx = b >> 3, head = xcd * 4 + (idx & 3), qt = idx >> 2;
    const int qp = wid & 3, kh = wid >> 2;
    const unsigned lds0 = (unsigned)(uintptr_t)lds;
    const char* kg = (const char*)(Kb + (size_t)head * S * DH);
    const char* vg = (const char*)(Vt + (size_t)head * S * DH);
    const unsigned kvoff = wid * 1024 + ((lane ^ ((lane >> 5) & 1)) * 16);
    const unsigned vvoff = wid * 1024 + lane * 16;
#define DMA_CHUNK(c) do { glds16s(kvoff, kg + (c) * 8192, lds0 + A_KR + ((c) & 3) * 8192 + wid * 1024); glds16s(vvoff, vg + (c) * 8192, lds0 + A_VR + ((c) & 3) * 8192 + wid * 1024); } while (0)
    const int q0 = qt * 256 + qp * 64;
    const bf16x8 qa = *(const bf16x8*)(Qs + ((size_t)head * S + q0 + r32) * DH + hi * 8);
    const bf16x8 qb = *(const bf16x8*)(Qs + ((size_t)head * S + q0 + 32 + r32) * DH + hi * 8);
    float km = kmax[lane * H + head];
    DMA_CHUNK(0);
    if (tid < 256) { const unsigned w = (tid & 31) ? 0u : 0x3F803F80u; u32x4 v = {w, w, w, w}; *(u32x4*)(lds + A_CONST + tid * 16) = v; }
    km = max32(max16(rowmax16(km)));
    float qna = 0.f, qnb = 0.f;
#pragma unroll
    for (int j = 0; j < 8; ++j) { const float va = bflo((u16)qa[j]), vb = bflo((u16)qb[j]); qna = fmaf(va, va, qna); qnb = fmaf(vb, vb, qnb); }
    qna = xor32(qna); qnb = xor32(qnb);
    const float mh = __builtin_amdgcn_sqrtf(fmaxf(qna, qnb) * km) * 1.001f;
    { const bool unsafe = __any(!(mh <= SAFE_T)); if (lane == 0) ((unsigned*)(lds + A_FLAG))[wid] = unsafe ? 1u : 0u; }
    const lds_cptr kp0 = (lds_cptr)lds + A_KR + kh * 4096 + r32 * 32 + ((hi ^ (r32 >> 4)) * 16);
    const bool vreal = r32 < 16;
    const unsigned vlane = vreal ? (unsigned)(A_VR + kh * 4096 + hi * 256 + r32 * 16) : (unsigned)(A_CONST + (r32 == 16 ? 0 : ((r32 >= 20 && r32 < 28) ? 64 : 16)));
    const unsigned vmask = vreal ? 0xFFFFFFFFu : 0u;
    f32x16 oa = {}, ob = {};
    WAIT_BAR(0);
    bool slow;
    { const u32x4 f0 = *(const u32x4*)(lds + A_FLAG), f1 = *(const u32x4*)(lds + A_FLAG + 16); slow = __builtin_amdgcn_readfirstlane(f0[0] | f0[1] | f0[2] | f0[3] | f1[0] | f1[1] | f1[2] | f1[3]) != 0; }
    if (!slow) {
        DMA_CHUNK(1); DMA_CHUNK(2); DMA_CHUNK(3);
        f32x16 cinit;
#pragma unroll
        for (int r = 0; r < 16; ++r) cinit[r] = -mh;
        PIN(cinit);
        bf16x8 kf, v0, v1; f32x16 sA, sB; u32x4 p0, p1;
        kf = ldsr128(kp0);
        { lds_cptr vp = (lds_cptr)lds + vlane; v0 = ldsr128(vp); v1 = ldsr128(vp + 512); }
        sA = MFMA32(kf, qa, cinit);
        p1 = (u32x4){0u, 0u, 0u, 0u};
#define HSTEP(SC, SN, QN, OP, OC, PRE1, PRE2, PRE3) do { \
        SN = MFMA32(kf, QN, cinit); PIN(SN); SBAR(); \
        PRE1; \
        SC[0] = EX(SC[0]); SC[1] = EX(SC[1]); SC[2] = EX(SC[2]); SC[3] = EX(SC[3]); SC[4] = EX(SC[4]); SC[5] = EX(SC[5]); PIN(SC); SBAR(); \
        OP = MFMA32(v1, BF(p1), OP); PIN(OP); SBAR(); \
        PRE2; \
        SC[6] = EX(SC[6]); SC[7] = EX(SC[7]); \
        p0[0] = cvtpk(SC[0], SC[1]); p0[1] = cvtpk(SC[2], SC[3]); p0[2] = cvtpk(SC[4], SC[5]); p0[3] = cvtpk(SC[6], SC[7]); PIN(p0); SBAR(); \
        SC[8] = EX(SC[8]); SC[9] = EX(SC[9]); SC[10] = EX(SC[10]); PIN(SC); SBAR(); \
        OC = MFMA32(v0, BF(p0), OC); PIN(OC); SBAR(); \
        PRE3; \
        SC[11] = EX(SC[11]); SC[12] = EX(SC[12]); SC[13] = EX(SC[13]); SC[14] = EX(SC[14]); SC[15] = EX(SC[15]); \
        p1[0] = cvtpk(SC[8], SC[9]); p1[1] = cvtpk(SC[10], SC[11]); p1[2] = cvtpk(SC[12], SC[13]); p1[3] = cvtpk(SC[14], SC[15]); PIN(p1); SBAR(); \
    } while (0)
#define BLOCK(kn, vc, vco, vn, vno) do { \
        HSTEP(sA, sB, qb, ob, oa, kf = ldsr128(kn), v1 = ldsr128((vc) + (vco) + 512), (void)0); \
        HSTEP(sB, sA, qa, oa, ob, (void)0, (void)0, v0 = ldsr128((vn) + (vno))); \
    } while (0)
        for (int c = 0; c < 8; ++c) {
            const unsigned ro = (unsigned)((c & 3) * 8192), rn = (unsigned)(((c + 1) & 3) * 8192);
            const lds_cptr vp = (lds_cptr)lds + (vlane + (ro & vmask)), vpn = (lds_cptr)lds + (vlane + (rn & vmask));
            const lds_cptr kp = kp0 + ro, kpn = kp0 + rn;
            BLOCK(kp + 1024, vp, 0, vp, 1024);
            BLOCK(kp + 2048, vp, 1024, vp, 2048);
            BLOCK(kp + 3072, vp, 2048, vp, 3072);
            if (c == 0) WAIT_BAR(4); else if (c < 6) WAIT_BAR(2); else if (c == 6) WAIT_BAR(0);
            if (c >= 1 && c + 3 < 8) DMA_CHUNK(c + 3);
            BLOCK(kpn, vp, 3072, vpn, 0);
        }
        ob = MFMA32(v1, BF(p1), ob);
#undef HSTEP
#undef BLOCK
    } else {
        float ma = -3.0e38f, mb = -3.0e38f;
        for (int pass = 0; pass < 2; ++pass) {
            for (int c = 0; c < 8; ++c) {
                LBAR();
                DMA_CHUNK(c);
                WAIT_BAR(0);
                const unsigned ro = (unsigned)((c & 3) * 8192);
                const lds_cptr vp = (lds_cptr)lds + (vlane + (ro & vmask)), kp = kp0 + ro;
                for (int j = 0; j < 4; ++j) {
                    const bf16x8 kf = ldsr128(kp + j * 1024);
                    const bf16x8 v0 = ldsr128(vp + j * 1024), v1 = ldsr128(vp + j * 1024 + 512);
#pragma unroll
                    for (int u = 0; u < 2; ++u) {
                        const f32x16 sc = MFMA32(kf, u ? qb : qa, (f32x16){});
                        float& m = u ? mb : ma; f32x16& o = u ? ob : oa;
                        if (pass == 0) {
#pragma unroll
                            for (int r = 0; r < 16; ++r) m = fmaxf(m, sc[r]);
                        } else {
                            u32x4 p0, p1;
#pragma unroll
                            for (int i = 0; i < 4; ++i) { p0[i] = cvtpk(EX(sc[2 * i] - m), EX(sc[2 * i + 1] - m)); p1[i] = cvtpk(EX(sc[8 + 2 * i] - m), EX(sc[9 + 2 * i] - m)); }
                            o = MFMA32(v0, BF(p0), o); o = MFMA32(v1, BF(p1), o);
                        }
                        SBAR();
                    }
                }
            }
            if (pass == 0) {
                ma = max32(ma); mb = max32(mb);
                float* mx = (float*)(lds + A_MX);
                if (hi == 0) { mx[(wid * 2) * 32 + r32] = ma; mx[(wid * 2 + 1) * 32 + r32] = mb; }
                LBAR();
                ma = fmaxf(ma, mx[((wid ^ 4) * 2) * 32 + r32]); mb = fmaxf(mb, mx[((wid ^ 4) * 2 + 1) * 32 + r32]);
            }
        }
    }
    __syncthreads();
    int tz = threadIdx.x; asm volatile("" : "+v"(tz));
    const int lz = tz & 63;
    float* red = (float*)lds;
    { float* dst = red + (size_t)(wid ^ 4) * 9 * 64 + lz;
#pragma unroll
      for (int r = 0; r < 9; ++r) dst[r * 64] = kh ? oa[r] : ob[r]; }
    __syncthreads();
    float o[9];
    { const float* src = red + (size_t)wid * 9 * 64 + lz;
#pragma unroll
      for (int r = 0; r < 9; ++r) o[r] = (kh ? ob[r] : oa[r]) + src[r * 64]; }
    const float rl = __builtin_amdgcn_rcpf(swap32(o[8], false));
    u16* op = Ot + ((size_t)(((q0 >> 5) + kh) * H + head) * 512) + (lz & 31) * 8 + (lz >> 5) * 4;
    st8_wt(op, (u32x2){cvtpk(o[0] * rl, o[1] * rl), cvtpk(o[2] * rl, o[3] * rl)});
    st8_wt(op + 256, (u32x2){cvtpk(o[4] * rl, o[5] * rl), cvtpk(o[6] * rl, o[7] * rl)});
    asm volatile("s_waitcnt vmcnt(0)\n\ts_barrier" ::: "memory");
    if (tz == 0) __hip_atomic_fetch_add((gu32*)(ws + WS_CNT) + (32 + qt) * 64, 1u, RLX_AGENT);
#undef DMA_CHUNK
}

DI void wait_cnt(gu32* cnt, unsigned want) {
    for (unsigned spins = 0; spins < 400000u; ++spins) {
        if ((unsigned)__builtin_amdgcn_readfirstlane(__hip_atomic_load(cnt, RLX_AGENT)) >= want) break;
        __builtin_amdgcn_s_sleep(1);
    }
}


constexpr int G_STAGE = 24576;
constexpr int W_STAGE = 16384;
constexpr int G_RED = 4 * W_STAGE;
DI void gate_role(int b, const u16* __restrict__ x, const u16* __restrict__ wo, const u16* __restrict__ wsp, const u16* __restrict__ bsp, const u16* __restrict__ wout, char* __restrict__ ws, u16* __restrict__ out, char* lds) {
    const int xcd = b & 7, idx = b >> 3;
    const int tt = xcd * 4 + (idx & 3), ct = idx >> 2;
    const int tid = threadIdx.x, lane = tid & 63, r32 = lane & 31, hi = lane >> 5;
    const int wid = __builtin_amdgcn_readfirstlane(tid >> 6);
    const unsigned lds0 = (unsigned)(uintptr_t)lds;
    gu32* hcnt = (gu32*)(ws + WS_CNT) + tt * 64; gu32* ocnt = (gu32*)(ws + WS_CNT) + (32 + (tt >> 2)) * 64;
    if (wid >= 4) {
        const int l = wid - 4;
        if (ct < 7) {
            const char* ag = ws + WS_Z2N + (size_t)ct * 32 * 16384 + l * 4096;
            const char* bg = (const char*)wsp + (size_t)(tt * 64 + l * 16) * 4096;
            const unsigned avoff = lane * 16;
            const unsigned bv0 = (unsigned)((lane >> 3) * 4096 + (((lane & 7) ^ (lane >> 4)) * 16)), bv1 = (unsigned)((8 + (lane >> 3)) * 4096 + (((lane & 7) ^ (4 + (lane >> 4))) * 16));
            auto dma = [&](int kt, int slot) __attribute__((always_inline)) { const unsigned d = lds0 + slot * G_STAGE; const char* a_ = ag + (size_t)kt * 16384;
#pragma unroll
                for (int e = 0; e < 4; ++e) glds16s(avoff, a_ + e * 1024, d + l * 4096 + e * 1024);
                glds16s(bv0, bg + kt * 128, d + 16384 + l * 2048); glds16s(bv1, bg + kt * 128, d + 16384 + l * 2048 + 1024); };
            dma(0, 0);
            WAIT_BAR(0);
            dma(1, 1); dma(2, 2);
            int slot = 0;
            for (int kt = 1; kt < 32; ++kt) {
                if (kt < 31) WAIT_BAR(6); else WAIT_BAR(0);
                if (kt + 2 < 32) dma(kt + 2, slot);
                slot = slot == 2 ? 0 : slot + 1;
            }
            LBAR();
        }
        unsigned wv[4];
#pragma unroll
        for (int e = 0; e < 4; ++e) { const int row = l * 32 + e * 8 + (lane >> 3); wv[e] = (unsigned)(row * 1024 + ct * 128 + (((lane & 7) ^ (((row >> 1) & 1) << 2)) * 16)); }
        auto dmw = [&](const u16* w, int st) __attribute__((always_inline)) { const char* wg = (const char*)w + (size_t)st * 128 * 1024; const unsigned d = lds0 + (st & 3) * W_STAGE + l * 4096;
#pragma unroll
            for (int e = 0; e < 4; ++e) glds16s(wv[e], wg, d + e * 1024); };
        dmw(wout, 0); dmw(wout, 1); dmw(wout, 2); dmw(wout, 3);
        if (l == 0) { wait_cnt(hcnt, 7);
        }
        WAIT_BAR(12);
#pragma unroll
        for (int st = 1; st < 7; ++st) {
            if (st < 4) WAIT_BAR(8); else if (st == 4) WAIT_BAR(8); else if (st == 5) WAIT_BAR(4); else WAIT_BAR(0);
            if (st + 3 < 7) dmw(wout, st + 3);
        }
        LBAR();
        dmw(wo, 0); dmw(wo, 1); dmw(wo, 2); dmw(wo, 3);
        if (l == 0) { wait_cnt(ocnt, 32);
        }
        WAIT_BAR(0);
        return;
    }
    __builtin_amdgcn_s_setprio(2);
    const int g = lane >> 4, qq = (lane & 15) >> 2, pp = lane & 3;
    const int th = wid & 1, fh = wid >> 1;
    const int trow = 32 * th + r32, t = tt * 64 + trow;
    if (ct < 7) {
        f32x4 acc[2][4];
#pragma unroll
        for (int tb = 0; tb < 2; ++tb)
#pragma unroll
            for (int fb = 0; fb < 4; ++fb) acc[tb][fb] = (f32x4){0.f, 0.f, 0.f, 0.f};
        const int t16 = lane & 15;
        const unsigned aoff = (unsigned)(fh * 8192 + (g >> 1) * 1024 + ((g & 1) * 32 + t16) * 16);
        unsigned boff[2][2];
#pragma unroll
        for (int tb = 0; tb < 2; ++tb)
#pragma unroll
            for (int kk = 0; kk < 2; ++kk) { const int row = 32 * th + 16 * tb + t16; boff[tb][kk] = (unsigned)(16384 + row * 128 + (((4 * kk + g) ^ ((row >> 1) & 7)) * 16)); }
        bf16x8 fa[2][4], fb_[2][2];
#define GSTEP(cur, nxt, sbn, kkn) do { \
            SBAR(); acc[0][0] = __builtin_amdgcn_mfma_f32_16x16x32_bf16(fa[cur][0], fb_[cur][0], acc[0][0], 0, 0, 0); acc[1][0] = __builtin_amdgcn_mfma_f32_16x16x32_bf16(fa[cur][0], fb_[cur][1], acc[1][0], 0, 0, 0); SBAR(); \
            fb_[nxt][0] = ldsr128((sbn) + boff[0][kkn]); fa[nxt][0] = ldsr128((sbn) + aoff + (kkn) * 2048); \
            SBAR(); acc[0][1] = __builtin_amdgcn_mfma_f32_16x16x32_bf16(fa[cur][1], fb_[cur][0], acc[0][1], 0, 0, 0); acc[1][1] = __builtin_amdgcn_mfma_f32_16x16x32_bf16(fa[cur][1], fb_[cur][1], acc[1][1], 0, 0, 0); SBAR(); \
            fb_[nxt][1] = ldsr128((sbn) + boff[1][kkn]); fa[nxt][1] = ldsr128((sbn) + aoff + (kkn) * 2048 + 256); \
            SBAR(); acc[0][2] = __builtin_amdgcn_mfma_f32_16x16x32_bf16(fa[cur][2], fb_[cur][0], acc[0][2], 0, 0, 0); acc[1][2] = __builtin_amdgcn_mfma_f32_16x16x32_bf16(fa[cur][2], fb_[cur][1], acc[1][2], 0, 0, 0); SBAR(); \
            fa[nxt][2] = ldsr128((sbn) + aoff + (kkn) * 2048 + 4096); fa[nxt][3] = ldsr128((sbn) + aoff + (kkn) * 2048 + 4096 + 256); \
            SBAR(); acc[0][3] = __builtin_amdgcn_mfma_f32_16x16x32_bf16(fa[cur][3], fb_[cur][0], acc[0][3], 0, 0, 0); acc[1][3] = __builtin_amdgcn_mfma_f32_16x16x32_bf16(fa[cur][3], fb_[cur][1], acc[1][3], 0, 0, 0); SBAR(); } while (0)
        float bias[2]; u32x2 z[2][4];
#pragma unroll
        for (int tb = 0; tb < 2; ++tb) { const int tk = tt * 64 + 32 * th + 16 * tb + t16; bias[tb] = bflo(bsp[tk]);
#pragma unroll
            for (int fb = 0; fb < 4; ++fb) z[tb][fb] = *(const u32x2*)((const u16*)(ws + WS_Z1) + ((size_t)((tk >> 5) * 28 + ct * 4 + 2 * fh + (fb >> 1)) * 1024) + (((2 * (fb & 1) + (g >> 1)) * 2 + (g & 1)) * 32 + (tk & 31)) * 4); }
        LBAR();
        { const lds_cptr sb = (lds_cptr)lds; fb_[0][0] = ldsr128(sb + boff[0][0]); fb_[0][1] = ldsr128(sb + boff[1][0]);
          fa[0][0] = ldsr128(sb + aoff); fa[0][1] = ldsr128(sb + aoff + 256); fa[0][2] = ldsr128(sb + aoff + 4096); fa[0][3] = ldsr128(sb + aoff + 4096 + 256); }
        int slot = 0;
        for (int kt = 0; kt < 32; ++kt) {
            GSTEP(0, 1, (lds_cptr)lds + slot * G_STAGE, 1);
            slot = slot == 2 ? 0 : slot + 1;
            if (kt < 31) LBAR();
            GSTEP(1, 0, (lds_cptr)lds + (kt < 31 ? slot : 0) * G_STAGE, 0);
        }
#pragma unroll
        for (int tb = 0; tb < 2; ++tb) { const int tk = tt * 64 + 32 * th + 16 * tb + t16;
#pragma unroll
            for (int fb = 0; fb < 4; ++fb) {
                u16* hp = (u16*)(ws + WS_H) + ((size_t)((tk >> 5) * 56 + ct * 8 + 4 * fh + fb) * 512) + ((g >> 1) * 32 + (tk & 31)) * 8 + (g & 1) * 4;
                const f32x4 v = acc[tb][fb] + bias[tb];
                st8_wt(hp, (u32x2){cvtpk(v[0] * bflo(z[tb][fb][0]), v[1] * bfhi(z[tb][fb][0])), cvtpk(v[2] * bflo(z[tb][fb][1]), v[3] * bfhi(z[tb][fb][1]))});
            } }
        asm volatile("s_waitcnt vmcnt(0) lgkmcnt(0)\n\ts_barrier" ::: "memory");
        if (tid == 0) __hip_atomic_fetch_add(hcnt, 1u, RLX_AGENT);
    }
    const int kh = fh;
    const int kr = 8 * (g >> 1) + qq;
    unsigned wof[2][2];
#pragma unroll
    for (int n = 0; n < 2; ++n) { wof[n][0] = (unsigned)((64 * kh + kr) * 128 + ((n ^ ((kr >> 1) & 1)) * 64) + (g & 1) * 32 + pp * 8); wof[n][1] = wof[n][0] + 512; }
    auto gemm = [&](const __amdgpu_buffer_rsrc_t tr, auto NST, bool bars, f32x16& c0, f32x16& c1) __attribute__((always_inline)) {
        constexpr int N = decltype(NST)::value;
        int lo16 = lane * 16; asm volatile("" : "+v"(lo16));
        u32x4 tf[4][4];
#pragma unroll
        for (int st = 0; st < 4; ++st)
#pragma unroll
            for (int i = 0; i < 4; ++i) tf[st][i] = __builtin_amdgcn_raw_buffer_load_b128(tr, lo16 + (st * 8 + 4 * kh + i) * 1024, 0, 16);
        SBAR();
#pragma unroll
        for (int st = 0; st < N; ++st) {
            if (bars && st > 0) LBAR();
            const lds_cptr sb = (lds_cptr)lds + (st & 3) * W_STAGE;
#pragma unroll
            for (int i = 0; i < 4; ++i) {
                const bf16x8 w0 = cat(ldstr(sb + wof[0][0] + i * 2048), ldstr(sb + wof[0][1] + i * 2048)), w1 = cat(ldstr(sb + wof[1][0] + i * 2048), ldstr(sb + wof[1][1] + i * 2048));
                c0 = MFMA32(w0, BF(tf[st & 3][i]), c0); c1 = MFMA32(w1, BF(tf[st & 3][i]), c1);
            }
            SBAR();
            if (st + 4 < N) {
#pragma unroll
                for (int i = 0; i < 4; ++i) tf[st & 3][i] = __builtin_amdgcn_raw_buffer_load_b128(tr, lo16 + ((st + 4) * 8 + 4 * kh + i) * 1024, 0, 16);
                SBAR(); }
        }
    };
    auto merge = [&](f32x16& c0, f32x16& c1) __attribute__((always_inline)) {
        f32x4* dst = (f32x4*)(lds + G_RED + (wid ^ 2) * 4096) + lane;
#pragma unroll
        for (int q = 0; q < 4; ++q) dst[q * 64] = kh ? (f32x4){c0[4 * q], c0[4 * q + 1], c0[4 * q + 2], c0[4 * q + 3]} : (f32x4){c1[4 * q], c1[4 * q + 1], c1[4 * q + 2], c1[4 * q + 3]};
    };
    f32x16 gb0 = {}, gb1 = {};
    LBAR();
    gemm(__builtin_amdgcn_make_buffer_rsrc((void*)(ws + WS_H + (size_t)(tt * 2 + th) * 57344), 0, 57344, 0x00020000), std::integral_constant<int, 7>{}, true, gb0, gb1);
    merge(gb0, gb1);
    LBAR();
    { char* sc = lds + G_RED + wid * 4096;
      float gv[16];
#pragma unroll
      for (int q = 0; q < 4; ++q) { const f32x4 o4 = ((const f32x4*)sc)[q * 64 + lane];
#pragma unroll
          for (int r = 0; r < 4; ++r) { const float bb = (kh ? gb1[4 * q + r] : gb0[4 * q + r]) + o4[r]; gv[4 * q + r] = bb * __builtin_amdgcn_rcpf(1.0f + EX(-LOG2E * bb)); } }
      asm volatile("s_waitcnt lgkmcnt(0)" ::: "memory");
#pragma unroll
      for (int u = 0; u < 2; ++u) ((u32x4*)sc)[u * 64 + lane] = (u32x4){cvtpk(gv[8 * u], gv[8 * u + 1]), cvtpk(gv[8 * u + 2], gv[8 * u + 3]), cvtpk(gv[8 * u + 4], gv[8 * u + 5]), cvtpk(gv[8 * u + 6], gv[8 * u + 7])}; }
    const int n0 = ct * 64 + kh * 32 + 4 * hi;
    u32x2 xv[4];
#pragma unroll
    for (int q = 0; q < 4; ++q) xv[q] = *(const u32x2*)(x + (size_t)t * D + n0 + 8 * q);
    PIN(xv[0]); PIN(xv[1]); PIN(xv[2]); PIN(xv[3]);
    LBAR();
    f32x16 s0 = {}, s1 = {};
    {
        const __amdgpu_buffer_rsrc_t tr = __builtin_amdgcn_make_buffer_rsrc((void*)(ws + WS_O + (size_t)(tt * 2 + th) * 32768), 0, 32768, 0x00020000);
        int lo16 = lane * 16; asm volatile("" : "+v"(lo16));
        u32x4 tf[16];
#pragma unroll
        for (int i = 0; i < 16; ++i) tf[i] = __builtin_amdgcn_raw_buffer_load_b128(tr, lo16 + ((i >> 2) * 8 + 4 * kh + (i & 3)) * 1024, 0, 16);
        SBAR();
        bf16x8 w0[2], w1[2];
        w0[0] = cat(ldstr((lds_cptr)lds + wof[0][0]), ldstr((lds_cptr)lds + wof[0][1])); w1[0] = cat(ldstr((lds_cptr)lds + wof[1][0]), ldstr((lds_cptr)lds + wof[1][1]));
#pragma unroll
        for (int i = 0; i < 16; ++i) {
            if (i + 1 < 16) { const lds_cptr sb = (lds_cptr)lds + ((i + 1) >> 2) * W_STAGE + ((i + 1) & 3) * 2048;
                w0[(i + 1) & 1] = cat(ldstr(sb + wof[0][0]), ldstr(sb + wof[0][1])); w1[(i + 1) & 1] = cat(ldstr(sb + wof[1][0]), ldstr(sb + wof[1][1])); }
            s0 = MFMA32(w0[i & 1], BF(tf[i]), s0); s1 = MFMA32(w1[i & 1], BF(tf[i]), s1);
        }
    }
    u32x4 gq[2];
    { const u32x4* gt = (const u32x4*)(lds + G_RED + wid * 4096) + lane; gq[0] = gt[0]; gq[1] = gt[64]; }
    LBAR();
    { f32x4* dst = (f32x4*)(lds + (wid ^ 2) * 4096) + lane;
#pragma unroll
      for (int q = 0; q < 4; ++q) dst[q * 64] = kh ? (f32x4){s0[4 * q], s0[4 * q + 1], s0[4 * q + 2], s0[4 * q + 3]} : (f32x4){s1[4 * q], s1[4 * q + 1], s1[4 * q + 2], s1[4 * q + 3]}; }
    LBAR();
    { const f32x4* src = (const f32x4*)(lds + wid * 4096) + lane;
#pragma unroll
      for (int q = 0; q < 4; ++q) { const f32x4 o4 = src[q * 64]; float v[4]; const unsigned g0 = gq[q >> 1][2 * (q & 1)], g1 = gq[q >> 1][2 * (q & 1) + 1]; const float gg[4] = {bflo(g0), bfhi(g0), bflo(g1), bfhi(g1)};
#pragma unroll
          for (int r = 0; r < 4; ++r) v[r] = ((kh ? s1[4 * q + r] : s0[4 * q + r]) + o4[r]) * gg[r];
          *(u32x2*)(out + (size_t)t * D + n0 + 8 * q) = (u32x2){cvtpk(bflo(xv[q][0]) + v[0], bfhi(xv[q][0]) + v[1]), cvtpk(bflo(xv[q][1]) + v[2], bfhi(xv[q][1]) + v[3])}; } }
}

__global__ __launch_bounds__(512, 4) void k2_mix(const u16* __restrict__ x, const u16* __restrict__ wo, const u16* __restrict__ wsp, const u16* __restrict__ bsp, const u16* __restrict__ wout, char* __restrict__ ws, u16* __restrict__ out) {
    extern __shared__ __attribute__((aligned(16))) char lds[];
    if (blockIdx.x < 256) attn_role(blockIdx.x, ws, lds);
    if (blockIdx.x >= 256) gate_role(blockIdx.x - 256, x, wo, wsp, bsp, wout, ws, out, lds);
}

extern "C" void kernel_launch(void* const* d_in, const int* in_sizes, int n_in, void* d_out, int out_size, void* d_ws, size_t ws_size, hipStream_t stream) {
    const u16* x = (const u16*)d_in[0]; const u16* wqkv = (const u16*)d_in[1]; const u16* wo = (const u16*)d_in[2];
    const u16* win = (const u16*)d_in[4]; const u16* wsp = (const u16*)d_in[5]; const u16* bsp = (const u16*)d_in[6]; const u16* wout = (const u16*)d_in[7];
    char* ws = (char*)d_ws;
    (void)hipFuncSetAttribute((const void*)k1_proj, hipFuncAttributeMaxDynamicSharedMemorySize, K1_LDS);
    (void)hipFuncSetAttribute((const void*)k2_mix, hipFuncAttributeMaxDynamicSharedMemorySize, K2_LDS);
    k1_proj<<<256, 768, K1_LDS, stream>>>(x, wqkv, win, ws);
    k2_mix<<<512, 512, K2_LDS, stream>>>(x, wo, wsp, bsp, wout, ws, (u16*)d_out);
}
```
